```python
import torch

def _softmax(x, axis=-1):
    return torch.softmax(x, dim=axis)
def _gelu(x):
    return torch.nn.functional.gelu(x, approximate="tanh")
def _silu(x):
    return torch.nn.functional.silu(x)
def _sigmoid(x):
    return torch.sigmoid(x)
def _layernorm(x, axis=-1, eps=1e-5):
    m = x.mean(dim=axis, keepdim=True)
    v = x.var(dim=axis, keepdim=True, unbiased=False)
    return (x - m) / torch.sqrt(v + eps)
def _scatter_add(tgt, idx, src, axis=0):
    return tgt.clone().index_add_(axis, idx, src)
def _segment_sum(src, seg, n):
    out = torch.zeros((n,) + src.shape[1:], dtype=src.dtype)
    return out.index_add_(0, seg, src)
def _pad(x, pw):
    flat = tuple(v for pair in reversed(pw) for v in pair)
    return torch.nn.functional.pad(x, flat)

kernel_name = 'recurrent_scan__sliding_block_cached__dense_activation_gelu__decode'

def setup_inputs():

    _w = (lambda a: (lambda t: t.to(torch.bfloat16) if t.is_floating_point() else t)(torch.as_tensor(a)))
    def _A_setup():
        B, Sq, D = 1, 1, 512
        Hq, Hkv, Sk, Dh = 32, 8, 32768, 128
        assert Hq % Hkv == 0, (Hq, Hkv)
        return {
            "x": _w((lambda *s: torch.randn(*s, dtype=torch.bfloat16) * 0.4)(B, Sq, D)),
            "k_cache": _w((lambda *s: torch.randn(*s, dtype=torch.bfloat16) * 1.6)(B, Hkv, Sk, Dh)),
            "v_cache": _w((lambda *s: torch.randn(*s, dtype=torch.bfloat16) * 1.6)(B, Hkv, Sk, Dh)),
            "wq": _w((lambda *s: torch.randn(*s, dtype=torch.bfloat16) * 1.6)(D, Hq * Dh) * (D ** -0.5)),
            "wo": _w((lambda *s: torch.randn(*s, dtype=torch.bfloat16) * 1.6)(Hq * Dh, D) * ((Hq * Dh) ** -0.5)),
        }
    _A_in = _A_setup()

    def _B_setup():
        B, S, D, F = 1, 1, 512, 1792
        return {
            "x": _w((lambda *s: torch.randn(*s, dtype=torch.bfloat16) * 0.4)(B, S, D)),
            "w1": _w((lambda *s: torch.randn(*s, dtype=torch.bfloat16) * 1.6)(D, F) * (D ** -0.5)),
            "b1": _w((lambda *s: torch.randn(*s, dtype=torch.bfloat16) * 1.6)(F) * 0.01),
            "w2": _w((lambda *s: torch.randn(*s, dtype=torch.bfloat16) * 1.6)(F, D) * (F ** -0.5)),
            "b2": _w((lambda *s: torch.randn(*s, dtype=torch.bfloat16) * 1.6)(D) * 0.01),
        }
    _B_in = _B_setup()

    return {
        "A_x": _A_in["x"],
        "A_k_cache": _A_in["k_cache"],
        "A_v_cache": _A_in["v_cache"],
        "A_wq": _A_in["wq"],
        "A_wo": _A_in["wo"],
        "B_x": _B_in["x"],
        "B_w1": _B_in["w1"],
        "B_b1": _B_in["b1"],
        "B_w2": _B_in["w2"],
        "B_b2": _B_in["b2"],
    }

def reference(A_x, A_k_cache, A_v_cache, A_wq, A_wo, B_x, B_w1, B_b1, B_w2, B_b2):
    def _A_ref(x, k_cache, v_cache, wq, wo):
        B, Sq, D = x.shape
        Hq, Dh = 32, 128
        Hkv, Sk = k_cache.shape[1], k_cache.shape[2]
        W = min(4096, max(256, Sk // 4))
        rep = Hq // Hkv
        q = (lambda x, *d: x.permute(*d))((x @ wq).reshape(B, Sq, Hq, Dh), 0, 2, 1, 3)
        k = (lambda x, n, axis: torch.repeat_interleave(x, n, dim=axis))(k_cache[:, :, Sk - W:, :], rep, axis=1)
        v = (lambda x, n, axis: torch.repeat_interleave(x, n, dim=axis))(v_cache[:, :, Sk - W:, :], rep, axis=1)
        scores = torch.einsum("bhqd,bhkd->bhqk", q, k) * (Dh ** -0.5)
        p = _softmax(scores, axis=-1)
        o = torch.einsum("bhqk,bhkd->bhqd", p, v)
        return (lambda x, *d: x.permute(*d))(o, 0, 2, 1, 3).reshape(B, Sq, Hq * Dh) @ wo

    def _B_ref(x, w1, b1, w2, b2):
        h = torch.einsum("bsd,df->bsf", x, w1) + b1
        h = _gelu(h)
        return torch.einsum("bsf,fd->bsd", h, w2) + b2

    x = A_x
    xn = _layernorm(x, axis=-1)
    s = _A_ref(xn, A_k_cache, A_v_cache, A_wq, A_wo)
    g = _silu(_B_ref(xn, B_w1, B_b1, B_w2, B_b2))
    return x + s * g
```

```cpp
#include <hip/hip_runtime.h>
#include <hip/hip_bf16.h>
typedef unsigned short u16;
typedef unsigned v4u __attribute__((ext_vector_type(4)));
constexpr int D = 512, HQ = 32, HKV = 8, SK = 32768, DH = 128, W = 4096, F = 1792;
constexpr int NSPLIT = 32, KPS = W / NSPLIT;
#define MIMIC 1
__device__ __forceinline__ float bf2f(u16 b) { return __uint_as_float((unsigned)b << 16); }
__device__ __forceinline__ float rbf(float f) { return __bfloat162float(__float2bfloat16(f)); }
__device__ __forceinline__ float rb(float f) { return MIMIC ? rbf(f) : f; }
__device__ float block_sum(float v, float* red) {
  for (int m = 32; m >= 1; m >>= 1) v += __shfl_xor(v, m);
  __syncthreads();
  if ((threadIdx.x & 63) == 0) red[threadIdx.x >> 6] = v;
  __syncthreads();
  float s = 0; for (int i = 0; i < (int)(blockDim.x >> 6); ++i) s += red[i];
  return s;
}
__device__ void layernorm(const u16* x, float* xn, float* red) {
  const int t = threadIdx.x;
  float a = bf2f(x[t]), b = bf2f(x[t + 256]);
  float mean = block_sum(a + b, red) * (1.f / D);
  float var = block_sum((a - mean) * (a - mean) + (b - mean) * (b - mean), red) * (1.f / D);
  float mb = rb(mean), vb = rb(var);
  float sd = rb(sqrtf(rb(vb + 1e-5f)));
  xn[t] = rb(rb(a - mb) / sd); xn[t + 256] = rb(rb(b - mb) / sd);
  __syncthreads();
}
__global__ void k1(const u16* x, const u16* wq, const u16* w1, const u16* b1, float* q, float* h) {
  __shared__ float xn[D]; __shared__ float red[8];
  layernorm(x, xn, red);
  int c = blockIdx.x * 256 + threadIdx.x;
  if (c < 4096) { float s = 0; for (int k = 0; k < D; ++k) s += xn[k] * bf2f(wq[k * 4096 + c]); q[c] = rb(s); }
  else { c -= 4096; if (c < F) { float s = 0; for (int k = 0; k < D; ++k) s += xn[k] * bf2f(w1[k * F + c]);
      s = rb(rb(s) + bf2f(b1[c]));
      float u = 0.7978845608028654f * (s + 0.044715f * s * s * s);
      h[c] = rb(0.5f * s * (1.f + tanhf(u))); } }
}
__global__ void k2(const u16* kc, const u16* vc, const float* q, float* pml, float* po) {
  __shared__ float qs[4 * DH]; __shared__ float sc[4][KPS]; __shared__ float ml[4][2];
  const int g = blockIdx.x & 7, sp = blockIdx.x >> 3, t = threadIdx.x;
  qs[t] = q[g * 512 + t]; qs[t + 256] = q[g * 512 + 256 + t];
  __syncthreads();
  const u16* kb = kc + ((size_t)g * SK + (SK - W) + sp * KPS) * DH; const u16* vb = vc + ((size_t)g * SK + (SK - W) + sp * KPS) * DH;
  for (int i = t; i < 4 * KPS; i += 256) { int hh = i / KPS, k = i % KPS; float s = 0; for (int d = 0; d < DH; ++d) s += qs[hh * DH + d] * bf2f(kb[k * DH + d]);
    sc[hh][k] = rb(rb(s) * 0.08838834764831845f); }
  __syncthreads();
  if (t < 4) { float m = -INFINITY; for (int k = 0; k < KPS; ++k) m = fmaxf(m, sc[t][k]); float l = 0; for (int k = 0; k < KPS; ++k) { float p = __expf(sc[t][k] - m); sc[t][k] = p; l += p; } ml[t][0] = m; ml[t][1] = l; }
  __syncthreads();
  for (int i = t; i < 4 * DH; i += 256) { int hh = i / DH, d = i % DH; float s = 0; for (int k = 0; k < KPS; ++k) s += sc[hh][k] * bf2f(vb[k * DH + d]);
    po[((g * 4 + hh) * NSPLIT + sp) * DH + d] = s; }
  if (t < 4) { pml[((g * 4 + t) * NSPLIT + sp) * 2] = ml[t][0]; pml[((g * 4 + t) * NSPLIT + sp) * 2 + 1] = ml[t][1]; }
}
__global__ void k3(const float* pml, const float* po, float* o) {
  int hh = blockIdx.x, d = threadIdx.x; float M = -INFINITY;
  for (int s = 0; s < NSPLIT; ++s) M = fmaxf(M, pml[(hh * NSPLIT + s) * 2]);
  float L = 0, a = 0;
  for (int s = 0; s < NSPLIT; ++s) { float w = __expf(pml[(hh * NSPLIT + s) * 2] - M); L += w * pml[(hh * NSPLIT + s) * 2 + 1]; a += w * po[(hh * NSPLIT + s) * DH + d]; }
  o[hh * DH + d] = rb(a / L);
}
__global__ void k4(const u16* x, const float* o, const float* h, const u16* wo, const u16* w2, const u16* b2, __hip_bfloat16* out) {
  __shared__ float red[8];
  int c = blockIdx.x, t = threadIdx.x; float s = 0, f = 0;
  for (int r = t; r < 4096; r += 256) s += o[r] * bf2f(wo[r * D + c]);
  for (int r = t; r < F; r += 256) f += h[r] * bf2f(w2[r * D + c]);
  s = block_sum(s, red); f = block_sum(f, red);
  if (t == 0) { s = rb(s); f = rb(rb(f) + bf2f(b2[c])); float g = rb(f / (1.f + __expf(-f))); out[c] = __float2bfloat16(bf2f(x[c]) + rb(s * g)); }
}
extern "C" void kernel_launch(void* const* d_in, const int* in_sizes, int n_in, void* d_out, int out_size, void* d_ws, size_t ws_size, hipStream_t stream) {
  const u16* x = (const u16*)d_in[0]; const u16* kc = (const u16*)d_in[1]; const u16* vc = (const u16*)d_in[2];
  const u16* wq = (const u16*)d_in[3]; const u16* wo = (const u16*)d_in[4]; const u16* w1 = (const u16*)d_in[6]; const u16* b1 = (const u16*)d_in[7];
  const u16* w2 = (const u16*)d_in[8]; const u16* b2 = (const u16*)d_in[9];
  float* ws = (float*)d_ws; float* q = ws; float* h = ws + 4096; float* o = ws + 8192; float* pml = ws + 16384; float* po = ws + 32768;
  k1<<<23, 256, 0, stream>>>(x, wq, w1, b1, q, h);
  k2<<<256, 256, 0, stream>>>(kc, vc, q, pml, po);
  k3<<<32, 128, 0, stream>>>(pml, po, o);
  k4<<<512, 256, 0, stream>>>(x, o, h, wo, w2, b2, (__hip_bfloat16*)d_out);
}
```

```cpp
#include <hip/hip_runtime.h>
#include <hip/hip_bf16.h>

typedef unsigned short u16;
typedef unsigned long long u64;
typedef unsigned v4u __attribute__((ext_vector_type(4)));
typedef unsigned v2u __attribute__((ext_vector_type(2)));
typedef float v4f __attribute__((ext_vector_type(4)));
typedef short s16x4 __attribute__((ext_vector_type(4)));
typedef __bf16 bf16x2 __attribute__((ext_vector_type(2)));
typedef __bf16 bf16x8 __attribute__((ext_vector_type(8)));
typedef __amdgpu_buffer_rsrc_t rsrc_t;
#define GAS __attribute__((address_space(1)))
#define CAS __attribute__((address_space(4)))
#define LAS __attribute__((address_space(3)))
#define SB() __builtin_amdgcn_sched_barrier(0)
#define LD_AUX 2
#define DELAY2 0
#define KPRE 0
#define XOFF 1
#define CODEPF 1
#define QAUX 0

constexpr int SK = 32768, WIN = 4096, FF = 1792;
constexpr int NTHR = 576;
constexpr unsigned WS_QG = 4096;
constexpr unsigned WS_HG = WS_QG + 131072;
constexpr unsigned WS_ML = WS_HG + 57344;
constexpr unsigned WS_OB = WS_ML + 8192;
constexpr unsigned WS_SB = WS_OB + 262144;
constexpr unsigned WS_END = WS_SB + 524288;
constexpr unsigned WS_PROF = 4u << 20;
constexpr int VSTR = 288, L_V = 0, L_Q = 128 * VSTR, QSTR = 272, L_S = L_Q + 4 * QSTR, L_XN = L_S + 2048, L_H = L_XN + 1024, L_WG = L_H + 64, L_O = L_WG + 128, L_END = L_O + 4096;
constexpr unsigned SENT = 0xFFFFFFFFu;
constexpr unsigned SPIN_CAP = 1u << 18;
constexpr float LOG2E = 1.4426950408889634f;
constexpr int OOB = 0x40000000;

__device__ __forceinline__ rsrc_t mk(const void* p, unsigned bytes) { return __builtin_amdgcn_make_buffer_rsrc((void*)p, 0, bytes, 0x00020000); }
__device__ __forceinline__ v4u ldw(rsrc_t r, int voff, int soff) { return __builtin_amdgcn_raw_buffer_load_b128(r, voff, soff, LD_AUX); }
__device__ __forceinline__ float bflo(unsigned u) { return __uint_as_float(u << 16); }
__device__ __forceinline__ float bfhi(unsigned u) { return __uint_as_float(u & 0xFFFF0000u); }
__device__ __forceinline__ unsigned pk(float lo, float hi) { bf16x2 r = {(__bf16)lo, (__bf16)hi}; return __builtin_bit_cast(unsigned, r); }
__device__ __forceinline__ float rb(float f) { return bflo(pk(f, f)); }
__device__ __forceinline__ float dot2(unsigned a, unsigned b, float c) { return __builtin_amdgcn_fdot2_f32_bf16(__builtin_bit_cast(bf16x2, a), __builtin_bit_cast(bf16x2, b), c, false); }
__device__ __forceinline__ v4f mfma(v4u a, v4u b, v4f c) { return __builtin_amdgcn_mfma_f32_16x16x32_bf16(__builtin_bit_cast(bf16x8, a), __builtin_bit_cast(bf16x8, b), c, 0, 0, 0); }
template <int CTRL> __device__ __forceinline__ float dpp(float x) { return __builtin_bit_cast(float, __builtin_amdgcn_mov_dpp(__builtin_bit_cast(int, x), CTRL, 0xf, 0xf, true)); }
constexpr int X1 = 0xB1, X2 = 0x4E, HM = 0x141, R8 = 0x128, R4 = 0x124;
__device__ __forceinline__ float sw16(float a, float b, float& o) { auto r = __builtin_amdgcn_permlane16_swap(__float_as_uint(a), __float_as_uint(b), false, false); o = __uint_as_float(r[1]); return __uint_as_float(r[0]); }
__device__ __forceinline__ float sw32(float a, float b, float& o) { auto r = __builtin_amdgcn_permlane32_swap(__float_as_uint(a), __float_as_uint(b), false, false); o = __uint_as_float(r[1]); return __uint_as_float(r[0]); }
__device__ __forceinline__ float fold16(float a, float b) { float o; float p = sw16(a, b, o); return p + o; }
__device__ __forceinline__ float fold32(float a, float b) { float o; float p = sw32(a, b, o); return p + o; }
__device__ __forceinline__ float fmx(float a, float b) { return __builtin_elementwise_maximum(a, b); }
__device__ __forceinline__ float max16(float a) { float o; float p = sw16(a, a, o); return fmx(p, o); }
__device__ __forceinline__ float max32(float a) { float o; float p = sw32(a, a, o); return fmx(p, o); }
__device__ __forceinline__ float sum64(float v) { v += dpp<X1>(v); v += dpp<X2>(v); v += dpp<HM>(v); v += dpp<R8>(v); v = fold16(v, v); return fold32(v, v); }
__device__ __forceinline__ unsigned nosent(unsigned u) { return min(u, 0xFFFFFFFEu); }
__device__ __forceinline__ unsigned max4(v4u v) { return max(max(v[0], v[1]), max(v[2], v[3])); }
__device__ __forceinline__ v2u trd(LAS unsigned char* p) { return __builtin_bit_cast(v2u, __builtin_amdgcn_ds_read_tr16_b64_v4i16((LAS s16x4*)p)); }
__device__ __forceinline__ v4u cat(v2u a, v2u b) { return v4u{a[0], a[1], b[0], b[1]}; }
__device__ __forceinline__ float ex2(float x) { return __builtin_amdgcn_exp2f(x); }
__device__ __forceinline__ float rcp(float x) { return __builtin_amdgcn_rcpf(x); }
__device__ __forceinline__ float gval(unsigned lo) { return __uint_as_float(lo & 0xFFFFFF00u); }
#define BAR() do { asm volatile("s_waitcnt lgkmcnt(0)" ::: "memory"); __builtin_amdgcn_s_barrier(); asm volatile("" ::: "memory"); } while (0)
#define FENCE() asm volatile("" ::: "memory")
#define DRAIN() asm volatile("s_waitcnt vmcnt(0)" ::: "memory")
#define LD16(off, soff) __builtin_amdgcn_raw_buffer_load_b128(rs, (off), (soff), 16)
#define LD8(off, soff) __builtin_amdgcn_raw_buffer_load_b64(rs, (off), (soff), 16)
#define ST16(v, off, soff) __builtin_amdgcn_raw_buffer_store_b128((v), rs, (off), (soff), 16)
#define ST8(v, off, soff) __builtin_amdgcn_raw_buffer_store_b64((v), rs, (off), (soff), 16)
#define GRAN(f) (v2u{((__float_as_uint(f) + 0x80u) & 0xFFFFFF00u) | tlo, thi})
#define GBAD(v) (((v)[1] ^ thi) | ((v)[3] ^ thi) | ((((v)[0] ^ tlo) | ((v)[2] ^ tlo)) & 0xFFu))
#define EPOCH() const u64 ep = *(const CAS u64*)ws + 1; const unsigned tlo = (unsigned)ep & 0xFFu, thi = (unsigned)(ep >> 8)
#define STAMP(i)
#define NOTE(i, v)
#define FLUSH()

__device__ __forceinline__ void mac2(float (&acc)[8], v4u a, v4u b, unsigned xx) {
#pragma unroll
  for (int d = 0; d < 4; ++d) {
    acc[2 * d] = dot2(__builtin_amdgcn_perm(b[d], a[d], 0x05040100u), xx, acc[2 * d]);
    acc[2 * d + 1] = dot2(__builtin_amdgcn_perm(b[d], a[d], 0x07060302u), xx, acc[2 * d + 1]);
  }
}
__device__ __forceinline__ float red8(float (&acc)[8], int l) {
  float a4[4], a2[2];
#pragma unroll
  for (int i = 0; i < 4; ++i) a4[i] = fold32(acc[i], acc[i + 4]);
#pragma unroll
  for (int i = 0; i < 2; ++i) a2[i] = fold16(a4[i], a4[i + 2]);
  const float t0 = a2[0] + dpp<R8>(a2[0]), t1 = a2[1] + dpp<R8>(a2[1]);
  return (l & 8) ? t1 : t0;
}

__global__ void __launch_bounds__(NTHR, 3) fused_decode(const u16* __restrict__ x, const u16* __restrict__ kc, const u16* __restrict__ vc, const u16* __restrict__ wq,
                                                        const u16* __restrict__ wo, const u16* __restrict__ w1, const u16* __restrict__ b1, const u16* __restrict__ w2,
                                                        const u16* __restrict__ b2, u16* __restrict__ out, unsigned char* ws) {
  __shared__ __attribute__((aligned(16))) unsigned char lds_[L_END];
  LAS unsigned char* L = (LAS unsigned char*)lds_;
  const int b = blockIdx.x, g = b & 7, j = b >> 3, l = threadIdx.x & 63;
  int w = __builtin_amdgcn_readfirstlane(threadIdx.x >> 6);
  STAMP(0);
  asm volatile("" :: "s"(x), "s"(kc), "s"(vc), "s"(wq), "s"(wo), "s"(w1), "s"(b1), "s"(w2), "s"(b2), "s"(out), "s"(ws));
  const rsrc_t rs = mk(ws, WS_END);
  const int kq = j & 3, rb_ = j >> 2, gn = (g + XOFF) & 7, gp = (g - XOFF) & 7;
  const int gq = l >> 4, n = l & 15;
  const int kvo = (g * SK + (SK - WIN) + j * 128 + (w - 1) * 16) * 256;
  const rsrc_t rk = mk(kc, 8u * SK * 256);
  v4u xv, r16[16]; unsigned cpf = 0;
  if (w == 0) {
    __builtin_amdgcn_s_setprio(3);
    xv = *(const GAS v4u*)(x + 8 * l);
    const rsrc_t r = mk(wq, 512 * 4096 * 2);
    const int vo = (l >> 3) * (16 * 8192) + (l & 7) * 16, so = kq * (128 * 8192) + gn * 1024 + rb_ * 128;
    { u64 pc; asm volatile("s_getpc_b64 %0" : "=s"(pc));
      cpf = *(const GAS unsigned*)((pc & ~127ull) + min(j * 2 + (l & 1), 59) * 128); }
#pragma unroll
    for (int i = 0; i < 16; ++i) r16[i] = ldw(r, vo, so + i * 8192);
    SB();
    ST16((v4u{SENT, SENT, SENT, SENT}), (l >> 3) * 4096 + (l & 7) * 16, WS_OB + g * 32768 + j * 128);
    ST8((v2u{SENT, SENT}), (l & 3) * 256, WS_ML + g * 1024 + j * 8);
  }
  SB();
  asm volatile("" : "+s"(w));
  __builtin_amdgcn_s_barrier();
  SB();
  if (__builtin_expect(w != 0, 1)) {
    const int a = w - 1;
    v4u vv[4], kv[4];
    { const rsrc_t r = mk(vc, 8u * SK * 256);
#pragma unroll
      for (int i = 0; i < 4; ++i) vv[i] = ldw(r, l * 16 + i * 1024, kvo);
#pragma unroll
      for (int i = 0; i < KPRE; ++i) kv[i] = ldw(rk, n * 256 + gq * 16 + i * 64, kvo); }
    SB();
    EPOCH();
    STAMP(1);
    if (KPRE < 4) {
      BAR();
#pragma unroll
      for (int i = KPRE; i < 4; ++i) kv[i] = ldw(rk, n * 256 + gq * 16 + i * 64, kvo);
      SB();
    }
    STAMP(2);
#pragma unroll
    for (int i = 0; i < 4; ++i) *(LAS v4u*)(L + L_V + (a * 16 + i * 4 + gq) * VSTR + n * 16) = vv[i];
    v4u qg[4];
    const int qso = WS_QG + g * 16384 + a * 1024;
    if (a < 4) {
#pragma unroll
      for (int i = 0; i < 4; ++i) qg[i] = __builtin_amdgcn_raw_buffer_load_b128(rs, l * 16, qso + i * 4096, QAUX);
    }
    SB();
    STAMP(3);
    BAR();
    __builtin_amdgcn_s_sleep(2);
    v4u wv[3];
    { const rsrc_t r = mk(wo, 4096 * 512 * 2), r2 = mk(w2, FF * 512 * 2);
      const int vo = gq * 1024 + n * 16;
      wv[0] = ldw(r, vo, (gp * 512 + rb_ * 64 + a * 8) * 1024 + kq * 256); wv[1] = ldw(r, vo, (gp * 512 + rb_ * 64 + a * 8 + 4) * 1024 + kq * 256);
      wv[2] = ldw(r2, vo, (gp * 224 + rb_ * 28 + min(a, 6) * 4) * 1024 + kq * 256); }
    SB();
    __builtin_amdgcn_s_setprio(2);
    int mlo = (l & 51) == 0 ? (n >> 2) * 256 : OOB, obo = (n & 3) == 0 ? (n >> 2) * 8192 + gq * 8 : OOB;
    int sbo = l < 32 ? (n >> 3) * 8192 + gq * 64 + (n & 7) * 8 : OOB;
    int obs = WS_OB + g * 32768 + (a >> 2) * 4096 + j * 128 + (a & 3) * 32;
    int sadr = L_S + (a & 1) * 1024 + gq * 256 + (n >> 2) * 64 + (a >> 1) * 16 + (n & 3) * 4, qadr = L_Q + (n >> 2) * QSTR + gq * 16;
    asm volatile("" : "+v"(mlo), "+v"(obo), "+v"(sbo), "+s"(obs), "+v"(sadr), "+v"(qadr));
    v2u vt[8];
    LAS unsigned char* TR = L + L_V + (gq * 4 + (n >> 2)) * VSTR + a * 32 + (n & 3) * 8;
#pragma unroll
    for (int i = 0; i < 8; ++i) vt[i] = trd(TR + i * 16 * VSTR);
    SB();
    if (a < 4) {
      unsigned spins = 0;
      if (__builtin_expect(!__all((GBAD(qg[0]) | GBAD(qg[1]) | GBAD(qg[2]) | GBAD(qg[3])) == 0), 0)) {
        for (;;) {
          bool ok = true, o[4];
#pragma unroll
          for (int i = 0; i < 4; ++i) { o[i] = __all(GBAD(qg[i]) == 0); ok &= o[i]; }
          if (ok || ++spins > SPIN_CAP) break;
          FENCE();
#pragma unroll
          for (int i = 0; i < 4; ++i) if (!o[i]) qg[i] = LD16(l * 16, qso + i * 4096);
          __builtin_amdgcn_s_waitcnt(0x0F70);
        }
      }
      NOTE(11, spins);
      *(LAS unsigned*)(L + L_Q + a * QSTR + l * 4) = pk((gval(qg[0][0]) + gval(qg[1][0])) + (gval(qg[2][0]) + gval(qg[3][0])), (gval(qg[0][2]) + gval(qg[1][2])) + (gval(qg[2][2]) + gval(qg[3][2])));
    }
    STAMP(4);
    BAR();
    { v4f sc = {0.f, 0.f, 0.f, 0.f};
      v4u qf[4];
#pragma unroll
      for (int i = 0; i < 4; ++i) qf[i] = *(LAS v4u*)(L + qadr + i * 64);
      SB();
#pragma unroll
      for (int i = 0; i < 4; ++i) sc = mfma(kv[i], qf[i], sc);
      const float sa = (n & 1) ? sc[1] : sc[0], sb = (n & 1) ? sc[3] : sc[2], s1 = (n & 2) ? sb : sa;
      *(LAS float*)(L + sadr) = rb(rb(s1) * 0.08838834764831845f) * LOG2E; }
    STAMP(5);
    BAR();
    { const v4f s0 = *(LAS v4f*)(L + L_S + l * 16), s1 = *(LAS v4f*)(L + L_S + 1024 + l * 16);
      float m = fmx(fmx(fmx(s0[0], s0[1]), fmx(s0[2], s0[3])), fmx(fmx(s1[0], s1[1]), fmx(s1[2], s1[3])));
      m = fmx(m, dpp<X1>(m)); m = fmx(m, dpp<X2>(m)); m = max16(m); m = max32(m);
      float p[8];
#pragma unroll
      for (int i = 0; i < 4; ++i) { p[i] = ex2(s0[i] - m); p[4 + i] = ex2(s1[i] - m); }
      const v4u pb = {pk(p[0], p[1]), pk(p[2], p[3]), pk(p[4], p[5]), pk(p[6], p[7])};
      if (a == 7) {
        const v4f dl = mfma(v4u{0x3F803F80u, 0x3F803F80u, 0x3F803F80u, 0x3F803F80u}, pb, v4f{0.f, 0.f, 0.f, 0.f});
        float ls = dl[0]; ls += dpp<X1>(ls); ls += dpp<X2>(ls);
        ST8((v2u{nosent(__float_as_uint(m)), nosent(__float_as_uint(ls))}), mlo, WS_ML + g * 1024 + j * 8);
      }
      v4f d[4];
#pragma unroll
      for (int i = 0; i < 4; ++i) d[i] = mfma(cat(vt[2 * i], vt[2 * i + 1]), pb, v4f{0.f, 0.f, 0.f, 0.f});
      float o[4];
#pragma unroll
      for (int r = 0; r < 4; ++r) o[r] = d[0][r] + dpp<0x55>(d[1][r]);
      asm volatile("" : "+v"(o[0]), "+v"(o[1]), "+v"(o[2]), "+v"(o[3]));
#pragma unroll
      for (int r = 0; r < 4; ++r) o[r] += dpp<0xAA>(d[2][r]);
      asm volatile("" : "+v"(o[0]), "+v"(o[1]), "+v"(o[2]), "+v"(o[3]));
#pragma unroll
      for (int r = 0; r < 4; ++r) o[r] += dpp<0xFF>(d[3][r]);
      ST8((v2u{nosent(pk(o[0], o[1])), nosent(pk(o[2], o[3]))}), obo, obs);
    }
    STAMP(6);
#pragma unroll
    for (int i = 0; i < 3; ++i) *(LAS v4u*)(L + L_V + ((i < 2 ? a * 8 + i * 4 : 64 + a * 4) + gq) * VSTR + n * 16) = wv[i];
    STAMP(7);
    if (a < 5) {
      if (DELAY2) __builtin_amdgcn_s_sleep(DELAY2);
      unsigned spins = 0;
      if (a < 4) {
        const int so = WS_OB + gp * 32768 + rb_ * 4096 + a * 1024;
        v4u ov;
        do { FENCE(); ov = LD16(l * 16, so); } while (!__all(max4(ov) != SENT) && ++spins < SPIN_CAP);
        STAMP(8);
        const int s = a * 8 + (l >> 3);
        *(LAS v4u*)(L + L_O + s * 128 + (((l & 7) ^ (s >> 1)) & 7) * 16) = ov;
      } else {
        v2u ml;
        do { FENCE(); ml = LD8((l & 31) * 8, WS_ML + gp * 1024 + (rb_ >> 1) * 256); } while (!__all(max(ml[0], ml[1]) != SENT) && ++spins < SPIN_CAP);
        STAMP(8);
        const float m = __uint_as_float(ml[0]);
        float M = fmx(m, dpp<X1>(m)); M = fmx(M, dpp<X2>(M)); M = fmx(M, dpp<HM>(M)); M = fmx(M, dpp<R8>(M)); M = max16(M);
        const float wgt = ex2(m - M);
        float ls = wgt * __uint_as_float(ml[1]);
        ls += dpp<X1>(ls); ls += dpp<X2>(ls); ls += dpp<HM>(ls); ls += dpp<R8>(ls); ls = fold16(ls, ls);
        *(LAS float*)(L + L_WG + (l & 31) * 4) = wgt * rcp(ls);
      }
      NOTE(12, spins);
    }
    STAMP(9);
    BAR();
    { v2u wt[6];
      LAS unsigned char* TW = L + L_V + (gq * 8 + (n >> 2)) * VSTR + a * 32 + (n & 3) * 8;
#pragma unroll
      for (int i = 0; i < 6; ++i) wt[i] = trd(TW + ((i >> 1) * 32 + (i & 1) * 4) * VSTR);
      v4u of[4];
#pragma unroll
      for (int i = 0; i < 4; ++i) of[i] = *(LAS v4u*)(L + L_O + ((i >> 1) * 16 + n) * 128 + ((((i & 1) * 4 + gq) ^ (n >> 1)) & 7) * 16);
      const v4u hf = *(LAS v4u*)(L + L_H + gq * 16);
      const v4f w0 = *(LAS v4f*)(L + L_WG + gq * 16), w1_ = *(LAS v4f*)(L + L_WG + 64 + gq * 16);
      const v4f z = {0.f, 0.f, 0.f, 0.f};
      v4f d0 = mfma(of[0], cat(wt[0], wt[1]), z), d1 = mfma(of[2], cat(wt[0], wt[1]), z);
      d0 = mfma(of[1], cat(wt[2], wt[3]), d0); d1 = mfma(of[3], cat(wt[2], wt[3]), d1);
      const v4f df = mfma(hf, cat(wt[4], wt[5]), z);
      float s = (fmaf(d0[1], w0[1], d0[0] * w0[0]) + fmaf(d0[3], w0[3], d0[2] * w0[2])) + (fmaf(d1[1], w1_[1], d1[0] * w1_[0]) + fmaf(d1[3], w1_[3], d1[2] * w1_[2]));
      s = fold16(s, s); s = fold32(s, s);
      const float e = gq ? df[0] : s;
      ST8(GRAN(e), sbo, WS_SB + (kq * 16 + a * 2) * 8192 + (g * 8 + rb_) * 128);
    }
    STAMP(10);
    if (b < 64) {
      v4u sv; unsigned spins = 0;
      do { FENCE(); sv = LD16(l * 16, WS_SB + b * 8192 + a * 1024); } while (!__all(GBAD(sv) == 0) && ++spins < SPIN_CAP);
      NOTE(13, spins);
      float v = fold32(gval(sv[0]), gval(sv[2]));
      v = fold16(v, v);
      v += dpp<R8>(v);
      *(LAS float*)(L + L_S + a * 64 + (l & 7) * 8 + (l >> 5) * 4) = v;
      BAR();
    }
    FLUSH();
  } else {
    EPOCH();
    STAMP(1);
    float xf[8];
#pragma unroll
    for (int i = 0; i < 4; ++i) { xf[2 * i] = bflo(xv[i]); xf[2 * i + 1] = bfhi(xv[i]); }
    const float mean = sum64(((xf[0] + xf[1]) + (xf[2] + xf[3])) + ((xf[4] + xf[5]) + (xf[6] + xf[7]))) * (1.f / 512);
    float vs = 0.f;
#pragma unroll
    for (int i = 0; i < 8; ++i) { const float d = xf[i] - mean; vs = fmaf(d, d, vs); }
    const float var = sum64(vs) * (1.f / 512);
    const float mb = rb(mean), rsd = rcp(rb(__builtin_amdgcn_sqrtf(rb(rb(var) + 1e-5f))));
    v4u xn;
#pragma unroll
    for (int i = 0; i < 4; ++i) { const unsigned d = pk(xf[2 * i] - mb, xf[2 * i + 1] - mb); xn[i] = pk(bflo(d) * rsd, bfhi(d) * rsd); }
    *(LAS v4u*)(L + L_XN + l * 16) = xn;
    const v4u xa = *(LAS v4u*)(L + L_XN + kq * 256 + (l >> 3) * 32), xb = *(LAS v4u*)(L + L_XN + kq * 256 + (l >> 3) * 32 + 16);
    const v4u xc = *(LAS v4u*)(L + L_XN + kq * 256 + (l >> 2) * 16);
    STAMP(2);
    { float acc[8] = {};
#pragma unroll
      for (int i = 0; i < 8; ++i) { mac2(acc, r16[2 * i], r16[2 * i + 1], i < 4 ? xa[i] : xb[i - 4]); SB(); }
      const float qv = red8(acc, l);
      asm volatile("" :: "v"(cpf));
      DRAIN();
      ST8(GRAN(qv), ((l & 7) * 8 + (l >> 3)) * 8, WS_QG + gn * 16384 + kq * 4096 + rb_ * 512);
    }
    STAMP(3);
    if (KPRE < 4) BAR();
    BAR();
    const int hc0 = g * 224 + rb_ * 32, hr0 = gp * 224 + rb_ * 28;
    if (j < 28) { const rsrc_t r = mk(w1, 512 * FF * 2);
      const int vo = (l >> 2) * (8 * FF * 2) + (l & 3) * 16, so = kq * (128 * FF * 2) + hc0 * 2;
#pragma unroll
      for (int i = 0; i < 8; ++i) r16[i] = ldw(r, vo, so + i * (FF * 2)); }
    const int hpr = min(n, 13) * 2;
    const float b1v = bflo(b1[hr0 + hpr + (gq & 1)]);
    const int ocol = (b & 63) * 8 + (l & 7);
    const float b2v = bflo(b2[ocol]), xo = bflo(x[ocol]);
    SB();
    __builtin_amdgcn_s_setprio(0);
    STAMP(4);
    BAR();
    BAR();
    STAMP(5);
    if (j < 28) {
      float ac[8] = {};
#pragma unroll
      for (int i = 0; i < 4; ++i) mac2(ac, r16[2 * i], r16[2 * i + 1], xc[i]);
      float hv = red8(ac, l);
      hv += dpp<X1>(dpp<X2>(dpp<HM>(hv)));
      ST8(GRAN(hv), (l & 4) ? OOB : ((l & 3) * 8 + (l >> 3)) * 8, WS_HG + kq * (FF * 8) + hc0 * 8);
    }
    STAMP(6);
    {
      v4u hg; unsigned spins = 0;
      do { FENCE(); hg = LD16(gq * (FF * 8) + hpr * 8, WS_HG + hr0 * 8); } while (!__all(GBAD(hg) == 0) && ++spins < SPIN_CAP);
      NOTE(12, spins);
      float hs = fold16(gval(hg[0]), gval(hg[2])); hs = fold32(hs, hs);
      const float hp_ = rb(rb(hs) + b1v);
      const float u2 = 1.5957691216057308f * fmaf(0.044715f * hp_ * hp_, hp_, hp_);
      const float ge = hp_ * rcp(1.f + ex2(-u2 * LOG2E));
      if (l < 32) *(LAS u16*)(L + L_H + n * 4 + gq * 2) = (n < 14) ? (u16)pk(ge, ge) : (u16)0;
    }
    STAMP(7);
    BAR();
    STAMP(8);
    if (b < 64) {
      BAR();
      STAMP(9);
      float v = *(LAS float*)(L + L_S + l * 4) + *(LAS float*)(L + L_S + 256 + l * 4);
      v = fold16(v, v); v = fold32(v, v);
      const float f = rb(rb(dpp<R8>(v)) + b2v);
      const float gt = rb(f * rcp(1.f + ex2(-f * LOG2E)));
      const float o = xo + rb(rb(v) * gt);
      if (l < 8) out[ocol] = (u16)pk(o, o);
      if (b == 0 && l == 0) *(GAS u64*)ws = ep;
    }
    STAMP(10);
    FLUSH();
  }
}

extern "C" void kernel_launch(void* const* d_in, const int* in_sizes, int n_in, void* d_out, int out_size, void* d_ws, size_t ws_size, hipStream_t stream) {
  fused_decode<<<256, NTHR, 0, stream>>>((const u16*)d_in[0], (const u16*)d_in[1], (const u16*)d_in[2], (const u16*)d_in[3], (const u16*)d_in[4], (const u16*)d_in[6],
                                         (const u16*)d_in[7], (const u16*)d_in[8], (const u16*)d_in[9], (u16*)d_out, (unsigned char*)d_ws);
}
```

```cpp
#define DIAG 1
#include <hip/hip_runtime.h>
#include <hip/hip_bf16.h>

typedef unsigned short u16;
typedef unsigned long long u64;
typedef unsigned v4u __attribute__((ext_vector_type(4)));
typedef unsigned v2u __attribute__((ext_vector_type(2)));
typedef float v4f __attribute__((ext_vector_type(4)));
typedef short s16x4 __attribute__((ext_vector_type(4)));
typedef __bf16 bf16x2 __attribute__((ext_vector_type(2)));
typedef __bf16 bf16x8 __attribute__((ext_vector_type(8)));
typedef __amdgpu_buffer_rsrc_t rsrc_t;
#define GAS __attribute__((address_space(1)))
#define CAS __attribute__((address_space(4)))
#define LAS __attribute__((address_space(3)))
#define SB() __builtin_amdgcn_sched_barrier(0)
#define LD_AUX 2
#define DELAY2 0
#define KPRE 0
#define XOFF 1
#define CODEPF 1
#define QAUX 0

constexpr int SK = 32768, WIN = 4096, FF = 1792;
constexpr int NTHR = 576;
constexpr unsigned WS_QG = 4096;
constexpr unsigned WS_HG = WS_QG + 131072;
constexpr unsigned WS_ML = WS_HG + 57344;
constexpr unsigned WS_OB = WS_ML + 8192;
constexpr unsigned WS_SB = WS_OB + 262144;
constexpr unsigned WS_END = WS_SB + 524288;
constexpr unsigned WS_PROF = 4u << 20;
constexpr int VSTR = 288, L_V = 0, L_Q = 128 * VSTR, QSTR = 272, L_S = L_Q + 4 * QSTR, L_XN = L_S + 2048, L_H = L_XN + 1024, L_WG = L_H + 64, L_O = L_WG + 128, L_END = L_O + 4096;
constexpr unsigned SENT = 0xFFFFFFFFu;
constexpr unsigned SPIN_CAP = 1u << 18;
constexpr float LOG2E = 1.4426950408889634f;
constexpr int OOB = 0x40000000;

__device__ __forceinline__ rsrc_t mk(const void* p, unsigned bytes) { return __builtin_amdgcn_make_buffer_rsrc((void*)p, 0, bytes, 0x00020000); }
__device__ __forceinline__ v4u ldw(rsrc_t r, int voff, int soff) { return __builtin_amdgcn_raw_buffer_load_b128(r, voff, soff, LD_AUX); }
__device__ __forceinline__ float bflo(unsigned u) { return __uint_as_float(u << 16); }
__device__ __forceinline__ float bfhi(unsigned u) { return __uint_as_float(u & 0xFFFF0000u); }
__device__ __forceinline__ unsigned pk(float lo, float hi) { bf16x2 r = {(__bf16)lo, (__bf16)hi}; return __builtin_bit_cast(unsigned, r); }
__device__ __forceinline__ float rb(float f) { return bflo(pk(f, f)); }
__device__ __forceinline__ float dot2(unsigned a, unsigned b, float c) { return __builtin_amdgcn_fdot2_f32_bf16(__builtin_bit_cast(bf16x2, a), __builtin_bit_cast(bf16x2, b), c, false); }
__device__ __forceinline__ v4f mfma(v4u a, v4u b, v4f c) { return __builtin_amdgcn_mfma_f32_16x16x32_bf16(__builtin_bit_cast(bf16x8, a), __builtin_bit_cast(bf16x8, b), c, 0, 0, 0); }
template <int CTRL> __device__ __forceinline__ float dpp(float x) { return __builtin_bit_cast(float, __builtin_amdgcn_mov_dpp(__builtin_bit_cast(int, x), CTRL, 0xf, 0xf, true)); }
constexpr int X1 = 0xB1, X2 = 0x4E, HM = 0x141, R8 = 0x128, R4 = 0x124;
__device__ __forceinline__ float sw16(float a, float b, float& o) { auto r = __builtin_amdgcn_permlane16_swap(__float_as_uint(a), __float_as_uint(b), false, false); o = __uint_as_float(r[1]); return __uint_as_float(r[0]); }
__device__ __forceinline__ float sw32(float a, float b, float& o) { auto r = __builtin_amdgcn_permlane32_swap(__float_as_uint(a), __float_as_uint(b), false, false); o = __uint_as_float(r[1]); return __uint_as_float(r[0]); }
__device__ __forceinline__ float fold16(float a, float b) { float o; float p = sw16(a, b, o); return p + o; }
__device__ __forceinline__ float fold32(float a, float b) { float o; float p = sw32(a, b, o); return p + o; }
__device__ __forceinline__ float fmx(float a, float b) { return __builtin_elementwise_maximum(a, b); }
__device__ __forceinline__ float max16(float a) { float o; float p = sw16(a, a, o); return fmx(p, o); }
__device__ __forceinline__ float max32(float a) { float o; float p = sw32(a, a, o); return fmx(p, o); }
__device__ __forceinline__ float sum64(float v) { v += dpp<X1>(v); v += dpp<X2>(v); v += dpp<HM>(v); v += dpp<R8>(v); v = fold16(v, v); return fold32(v, v); }
__device__ __forceinline__ unsigned nosent(unsigned u) { return min(u, 0xFFFFFFFEu); }
__device__ __forceinline__ unsigned max4(v4u v) { return max(max(v[0], v[1]), max(v[2], v[3])); }
__device__ __forceinline__ v2u trd(LAS unsigned char* p) { return __builtin_bit_cast(v2u, __builtin_amdgcn_ds_read_tr16_b64_v4i16((LAS s16x4*)p)); }
__device__ __forceinline__ v4u cat(v2u a, v2u b) { return v4u{a[0], a[1], b[0], b[1]}; }
__device__ __forceinline__ float ex2(float x) { return __builtin_amdgcn_exp2f(x); }
__device__ __forceinline__ float rcp(float x) { return __builtin_amdgcn_rcpf(x); }
__device__ __forceinline__ float gval(unsigned lo) { return __uint_as_float(lo & 0xFFFFFF00u); }
#define BAR() do { asm volatile("s_waitcnt lgkmcnt(0)" ::: "memory"); __builtin_amdgcn_s_barrier(); asm volatile("" ::: "memory"); } while (0)
#define FENCE() asm volatile("" ::: "memory")
#define DRAIN() asm volatile("s_waitcnt vmcnt(0)" ::: "memory")
#define LD16(off, soff) __builtin_amdgcn_raw_buffer_load_b128(rs, (off), (soff), 16)
#define LD8(off, soff) __builtin_amdgcn_raw_buffer_load_b64(rs, (off), (soff), 16)
#define ST16(v, off, soff) __builtin_amdgcn_raw_buffer_store_b128((v), rs, (off), (soff), 16)
#define ST8(v, off, soff) __builtin_amdgcn_raw_buffer_store_b64((v), rs, (off), (soff), 16)
#define GRAN(f) (v2u{((__float_as_uint(f) + 0x80u) & 0xFFFFFF00u) | tlo, thi})
#define GBAD(v) (((v)[1] ^ thi) | ((v)[3] ^ thi) | ((((v)[0] ^ tlo) | ((v)[2] ^ tlo)) & 0xFFu))
#define EPOCH() const u64 ep = *(const CAS u64*)ws + 1; const unsigned tlo = (unsigned)ep & 0xFFu, thi = (unsigned)(ep >> 8)
#define STAMP(i) do { if ((i) == 0 || (i) == DIAG) { SB(); tst[i] = (unsigned)__builtin_amdgcn_s_memrealtime(); SB(); } } while (0)
#define NOTE(i, v)
#define FLUSH() do { if (w == 0 && b < 8) { const unsigned d_ = 2 * (tst[DIAG] - tst[0]), e_ = (unsigned)__builtin_amdgcn_s_memrealtime(); while ((unsigned)__builtin_amdgcn_s_memrealtime() - e_ < d_ && d_ < 100000u) {} } } while (0)

__device__ __forceinline__ void mac2(float (&acc)[8], v4u a, v4u b, unsigned xx) {
#pragma unroll
  for (int d = 0; d < 4; ++d) {
    acc[2 * d] = dot2(__builtin_amdgcn_perm(b[d], a[d], 0x05040100u), xx, acc[2 * d]);
    acc[2 * d + 1] = dot2(__builtin_amdgcn_perm(b[d], a[d], 0x07060302u), xx, acc[2 * d + 1]);
  }
}
__device__ __forceinline__ float red8(float (&acc)[8], int l) {
  float a4[4], a2[2];
#pragma unroll
  for (int i = 0; i < 4; ++i) a4[i] = fold32(acc[i], acc[i + 4]);
#pragma unroll
  for (int i = 0; i < 2; ++i) a2[i] = fold16(a4[i], a4[i + 2]);
  const float t0 = a2[0] + dpp<R8>(a2[0]), t1 = a2[1] + dpp<R8>(a2[1]);
  return (l & 8) ? t1 : t0;
}

__global__ void __launch_bounds__(NTHR, 3) fused_decode(const u16* __restrict__ x, const u16* __restrict__ kc, const u16* __restrict__ vc, const u16* __restrict__ wq,
                                                        const u16* __restrict__ wo, const u16* __restrict__ w1, const u16* __restrict__ b1, const u16* __restrict__ w2,
                                                        const u16* __restrict__ b2, u16* __restrict__ out, unsigned char* ws) {
  __shared__ __attribute__((aligned(16))) unsigned char lds_[L_END];
  LAS unsigned char* L = (LAS unsigned char*)lds_;
  unsigned tst[16] = {};
  const int b = blockIdx.x, g = b & 7, j = b >> 3, l = threadIdx.x & 63;
  int w = __builtin_amdgcn_readfirstlane(threadIdx.x >> 6);
  STAMP(0);
  asm volatile("" :: "s"(x), "s"(kc), "s"(vc), "s"(wq), "s"(wo), "s"(w1), "s"(b1), "s"(w2), "s"(b2), "s"(out), "s"(ws));
  const rsrc_t rs = mk(ws, WS_END);
  const int kq = j & 3, rb_ = j >> 2, gn = (g + XOFF) & 7, gp = (g - XOFF) & 7;
  const int gq = l >> 4, n = l & 15;
  const int kvo = (g * SK + (SK - WIN) + j * 128 + (w - 1) * 16) * 256;
  const rsrc_t rk = mk(kc, 8u * SK * 256);
  v4u xv, r16[16]; unsigned cpf = 0;
  if (w == 0) {
    __builtin_amdgcn_s_setprio(3);
    xv = *(const GAS v4u*)(x + 8 * l);
    const rsrc_t r = mk(wq, 512 * 4096 * 2);
    const int vo = (l >> 3) * (16 * 8192) + (l & 7) * 16, so = kq * (128 * 8192) + gn * 1024 + rb_ * 128;
    { u64 pc; asm volatile("s_getpc_b64 %0" : "=s"(pc));
      cpf = *(const GAS unsigned*)((pc & ~127ull) + min(j * 2 + (l & 1), 59) * 128); }
#pragma unroll
    for (int i = 0; i < 16; ++i) r16[i] = ldw(r, vo, so + i * 8192);
    SB();
    ST16((v4u{SENT, SENT, SENT, SENT}), (l >> 3) * 4096 + (l & 7) * 16, WS_OB + g * 32768 + j * 128);
    ST8((v2u{SENT, SENT}), (l & 3) * 256, WS_ML + g * 1024 + j * 8);
  }
  SB();
  asm volatile("" : "+s"(w));
  __builtin_amdgcn_s_barrier();
  SB();
  if (__builtin_expect(w != 0, 1)) {
    const int a = w - 1;
    v4u vv[4], kv[4];
    { const rsrc_t r = mk(vc, 8u * SK * 256);
#pragma unroll
      for (int i = 0; i < 4; ++i) vv[i] = ldw(r, l * 16 + i * 1024, kvo);
#pragma unroll
      for (int i = 0; i < KPRE; ++i) kv[i] = ldw(rk, n * 256 + gq * 16 + i * 64, kvo); }
    SB();
    EPOCH();
    STAMP(1);
    if (KPRE < 4) {
      BAR();
#pragma unroll
      for (int i = KPRE; i < 4; ++i) kv[i] = ldw(rk, n * 256 + gq * 16 + i * 64, kvo);
      SB();
    }
    STAMP(2);
#pragma unroll
    for (int i = 0; i < 4; ++i) *(LAS v4u*)(L + L_V + (a * 16 + i * 4 + gq) * VSTR + n * 16) = vv[i];
    v4u qg[4];
    const int qso = WS_QG + g * 16384 + a * 1024;
    if (a < 4) {
#pragma unroll
      for (int i = 0; i < 4; ++i) qg[i] = __builtin_amdgcn_raw_buffer_load_b128(rs, l * 16, qso + i * 4096, QAUX);
    }
    SB();
    STAMP(3);
    BAR();
    __builtin_amdgcn_s_sleep(2);
    v4u wv[3];
    { const rsrc_t r = mk(wo, 4096 * 512 * 2), r2 = mk(w2, FF * 512 * 2);
      const int vo = gq * 1024 + n * 16;
      wv[0] = ldw(r, vo, (gp * 512 + rb_ * 64 + a * 8) * 1024 + kq * 256); wv[1] = ldw(r, vo, (gp * 512 + rb_ * 64 + a * 8 + 4) * 1024 + kq * 256);
      wv[2] = ldw(r2, vo, (gp * 224 + rb_ * 28 + min(a, 6) * 4) * 1024 + kq * 256); }
    SB();
    __builtin_amdgcn_s_setprio(2);
    int mlo = (l & 51) == 0 ? (n >> 2) * 256 : OOB, obo = (n & 3) == 0 ? (n >> 2) * 8192 + gq * 8 : OOB;
    int sbo = l < 32 ? (n >> 3) * 8192 + gq * 64 + (n & 7) * 8 : OOB;
    int obs = WS_OB + g * 32768 + (a >> 2) * 4096 + j * 128 + (a & 3) * 32;
    int sadr = L_S + (a & 1) * 1024 + gq * 256 + (n >> 2) * 64 + (a >> 1) * 16 + (n & 3) * 4, qadr = L_Q + (n >> 2) * QSTR + gq * 16;
    asm volatile("" : "+v"(mlo), "+v"(obo), "+v"(sbo), "+s"(obs), "+v"(sadr), "+v"(qadr));
    v2u vt[8];
    LAS unsigned char* TR = L + L_V + (gq * 4 + (n >> 2)) * VSTR + a * 32 + (n & 3) * 8;
#pragma unroll
    for (int i = 0; i < 8; ++i) vt[i] = trd(TR + i * 16 * VSTR);
    SB();
    if (a < 4) {
      unsigned spins = 0;
      if (__builtin_expect(!__all((GBAD(qg[0]) | GBAD(qg[1]) | GBAD(qg[2]) | GBAD(qg[3])) == 0), 0)) {
        for (;;) {
          bool ok = true, o[4];
#pragma unroll
          for (int i = 0; i < 4; ++i) { o[i] = __all(GBAD(qg[i]) == 0); ok &= o[i]; }
          if (ok || ++spins > SPIN_CAP) break;
          FENCE();
#pragma unroll
          for (int i = 0; i < 4; ++i) if (!o[i]) qg[i] = LD16(l * 16, qso + i * 4096);
          __builtin_amdgcn_s_waitcnt(0x0F70);
        }
      }
      NOTE(11, spins);
      *(LAS unsigned*)(L + L_Q + a * QSTR + l * 4) = pk((gval(qg[0][0]) + gval(qg[1][0])) + (gval(qg[2][0]) + gval(qg[3][0])), (gval(qg[0][2]) + gval(qg[1][2])) + (gval(qg[2][2]) + gval(qg[3][2])));
    }
    STAMP(4);
    BAR();
    { v4f sc = {0.f, 0.f, 0.f, 0.f};
      v4u qf[4];
#pragma unroll
      for (int i = 0; i < 4; ++i) qf[i] = *(LAS v4u*)(L + qadr + i * 64);
      SB();
#pragma unroll
      for (int i = 0; i < 4; ++i) sc = mfma(kv[i], qf[i], sc);
      const float sa = (n & 1) ? sc[1] : sc[0], sb = (n & 1) ? sc[3] : sc[2], s1 = (n & 2) ? sb : sa;
      *(LAS float*)(L + sadr) = rb(rb(s1) * 0.08838834764831845f) * LOG2E; }
    STAMP(5);
    BAR();
    { const v4f s0 = *(LAS v4f*)(L + L_S + l * 16), s1 = *(LAS v4f*)(L + L_S + 1024 + l * 16);
      float m = fmx(fmx(fmx(s0[0], s0[1]), fmx(s0[2], s0[3])), fmx(fmx(s1[0], s1[1]), fmx(s1[2], s1[3])));
      m = fmx(m, dpp<X1>(m)); m = fmx(m, dpp<X2>(m)); m = max16(m); m = max32(m);
      float p[8];
#pragma unroll
      for (int i = 0; i < 4; ++i) { p[i] = ex2(s0[i] - m); p[4 + i] = ex2(s1[i] - m); }
      const v4u pb = {pk(p[0], p[1]), pk(p[2], p[3]), pk(p[4], p[5]), pk(p[6], p[7])};
      if (a == 7) {
        const v4f dl = mfma(v4u{0x3F803F80u, 0x3F803F80u, 0x3F803F80u, 0x3F803F80u}, pb, v4f{0.f, 0.f, 0.f, 0.f});
        float ls = dl[0]; ls += dpp<X1>(ls); ls += dpp<X2>(ls);
        ST8((v2u{nosent(__float_as_uint(m)), nosent(__float_as_uint(ls))}), mlo, WS_ML + g * 1024 + j * 8);
      }
      v4f d[4];
#pragma unroll
      for (int i = 0; i < 4; ++i) d[i] = mfma(cat(vt[2 * i], vt[2 * i + 1]), pb, v4f{0.f, 0.f, 0.f, 0.f});
      float o[4];
#pragma unroll
      for (int r = 0; r < 4; ++r) o[r] = d[0][r] + dpp<0x55>(d[1][r]);
      asm volatile("" : "+v"(o[0]), "+v"(o[1]), "+v"(o[2]), "+v"(o[3]));
#pragma unroll
      for (int r = 0; r < 4; ++r) o[r] += dpp<0xAA>(d[2][r]);
      asm volatile("" : "+v"(o[0]), "+v"(o[1]), "+v"(o[2]), "+v"(o[3]));
#pragma unroll
      for (int r = 0; r < 4; ++r) o[r] += dpp<0xFF>(d[3][r]);
      ST8((v2u{nosent(pk(o[0], o[1])), nosent(pk(o[2], o[3]))}), obo, obs);
    }
    STAMP(6);
#pragma unroll
    for (int i = 0; i < 3; ++i) *(LAS v4u*)(L + L_V + ((i < 2 ? a * 8 + i * 4 : 64 + a * 4) + gq) * VSTR + n * 16) = wv[i];
    STAMP(7);
    if (a < 5) {
      if (DELAY2) __builtin_amdgcn_s_sleep(DELAY2);
      unsigned spins = 0;
      if (a < 4) {
        const int so = WS_OB + gp * 32768 + rb_ * 4096 + a * 1024;
        v4u ov;
        do { FENCE(); ov = LD16(l * 16, so); } while (!__all(max4(ov) != SENT) && ++spins < SPIN_CAP);
        STAMP(8);
        const int s = a * 8 + (l >> 3);
        *(LAS v4u*)(L + L_O + s * 128 + (((l & 7) ^ (s >> 1)) & 7) * 16) = ov;
      } else {
        v2u ml;
        do { FENCE(); ml = LD8((l & 31) * 8, WS_ML + gp * 1024 + (rb_ >> 1) * 256); } while (!__all(max(ml[0], ml[1]) != SENT) && ++spins < SPIN_CAP);
        STAMP(8);
        const float m = __uint_as_float(ml[0]);
        float M = fmx(m, dpp<X1>(m)); M = fmx(M, dpp<X2>(M)); M = fmx(M, dpp<HM>(M)); M = fmx(M, dpp<R8>(M)); M = max16(M);
        const float wgt = ex2(m - M);
        float ls = wgt * __uint_as_float(ml[1]);
        ls += dpp<X1>(ls); ls += dpp<X2>(ls); ls += dpp<HM>(ls); ls += dpp<R8>(ls); ls = fold16(ls, ls);
        *(LAS float*)(L + L_WG + (l & 31) * 4) = wgt * rcp(ls);
      }
      NOTE(12, spins);
    }
    STAMP(9);
    BAR();
    { v2u wt[6];
      LAS unsigned char* TW = L + L_V + (gq * 8 + (n >> 2)) * VSTR + a * 32 + (n & 3) * 8;
#pragma unroll
      for (int i = 0; i < 6; ++i) wt[i] = trd(TW + ((i >> 1) * 32 + (i & 1) * 4) * VSTR);
      v4u of[4];
#pragma unroll
      for (int i = 0; i < 4; ++i) of[i] = *(LAS v4u*)(L + L_O + ((i >> 1) * 16 + n) * 128 + ((((i & 1) * 4 + gq) ^ (n >> 1)) & 7) * 16);
      const v4u hf = *(LAS v4u*)(L + L_H + gq * 16);
      const v4f w0 = *(LAS v4f*)(L + L_WG + gq * 16), w1_ = *(LAS v4f*)(L + L_WG + 64 + gq * 16);
      const v4f z = {0.f, 0.f, 0.f, 0.f};
      v4f d0 = mfma(of[0], cat(wt[0], wt[1]), z), d1 = mfma(of[2], cat(wt[0], wt[1]), z);
      d0 = mfma(of[1], cat(wt[2], wt[3]), d0); d1 = mfma(of[3], cat(wt[2], wt[3]), d1);
      const v4f df = mfma(hf, cat(wt[4], wt[5]), z);
      float s = (fmaf(d0[1], w0[1], d0[0] * w0[0]) + fmaf(d0[3], w0[3], d0[2] * w0[2])) + (fmaf(d1[1], w1_[1], d1[0] * w1_[0]) + fmaf(d1[3], w1_[3], d1[2] * w1_[2]));
      s = fold16(s, s); s = fold32(s, s);
      const float e = gq ? df[0] : s;
      ST8(GRAN(e), sbo, WS_SB + (kq * 16 + a * 2) * 8192 + (g * 8 + rb_) * 128);
    }
    STAMP(10);
    if (b < 64) {
      v4u sv; unsigned spins = 0;
      do { FENCE(); sv = LD16(l * 16, WS_SB + b * 8192 + a * 1024); } while (!__all(GBAD(sv) == 0) && ++spins < SPIN_CAP);
      NOTE(13, spins);
      float v = fold32(gval(sv[0]), gval(sv[2]));
      v = fold16(v, v);
      v += dpp<R8>(v);
      *(LAS float*)(L + L_S + a * 64 + (l & 7) * 8 + (l >> 5) * 4) = v;
      BAR();
    }
    FLUSH();
  } else {
    EPOCH();
    STAMP(1);
    float xf[8];
#pragma unroll
    for (int i = 0; i < 4; ++i) { xf[2 * i] = bflo(xv[i]); xf[2 * i + 1] = bfhi(xv[i]); }
    const float mean = sum64(((xf[0] + xf[1]) + (xf[2] + xf[3])) + ((xf[4] + xf[5]) + (xf[6] + xf[7]))) * (1.f / 512);
    float vs = 0.f;
#pragma unroll
    for (int i = 0; i < 8; ++i) { const float d = xf[i] - mean; vs = fmaf(d, d, vs); }
    const float var = sum64(vs) * (1.f / 512);
    const float mb = rb(mean), rsd = rcp(rb(__builtin_amdgcn_sqrtf(rb(rb(var) + 1e-5f))));
    v4u xn;
#pragma unroll
    for (int i = 0; i < 4; ++i) { const unsigned d = pk(xf[2 * i] - mb, xf[2 * i + 1] - mb); xn[i] = pk(bflo(d) * rsd, bfhi(d) * rsd); }
    *(LAS v4u*)(L + L_XN + l * 16) = xn;
    const v4u xa = *(LAS v4u*)(L + L_XN + kq * 256 + (l >> 3) * 32), xb = *(LAS v4u*)(L + L_XN + kq * 256 + (l >> 3) * 32 + 16);
    const v4u xc = *(LAS v4u*)(L + L_XN + kq * 256 + (l >> 2) * 16);
    STAMP(2);
    { float acc[8] = {};
#pragma unroll
      for (int i = 0; i < 8; ++i) { mac2(acc, r16[2 * i], r16[2 * i + 1], i < 4 ? xa[i] : xb[i - 4]); SB(); }
      const float qv = red8(acc, l);
      asm volatile("" :: "v"(cpf));
      DRAIN();
      ST8(GRAN(qv), ((l & 7) * 8 + (l >> 3)) * 8, WS_QG + gn * 16384 + kq * 4096 + rb_ * 512);
    }
    STAMP(3);
    if (KPRE < 4) BAR();
    BAR();
    const int hc0 = g * 224 + rb_ * 32, hr0 = gp * 224 + rb_ * 28;
    if (j < 28) { const rsrc_t r = mk(w1, 512 * FF * 2);
      const int vo = (l >> 2) * (8 * FF * 2) + (l & 3) * 16, so = kq * (128 * FF * 2) + hc0 * 2;
#pragma unroll
      for (int i = 0; i < 8; ++i) r16[i] = ldw(r, vo, so + i * (FF * 2)); }
    const int hpr = min(n, 13) * 2;
    const float b1v = bflo(b1[hr0 + hpr + (gq & 1)]);
    const int ocol = (b & 63) * 8 + (l & 7);
    const float b2v = bflo(b2[ocol]), xo = bflo(x[ocol]);
    SB();
    __builtin_amdgcn_s_setprio(0);
    STAMP(4);
    BAR();
    BAR();
    STAMP(5);
    if (j < 28) {
      float ac[8] = {};
#pragma unroll
      for (int i = 0; i < 4; ++i) mac2(ac, r16[2 * i], r16[2 * i + 1], xc[i]);
      float hv = red8(ac, l);
      hv += dpp<X1>(dpp<X2>(dpp<HM>(hv)));
      ST8(GRAN(hv), (l & 4) ? OOB : ((l & 3) * 8 + (l >> 3)) * 8, WS_HG + kq * (FF * 8) + hc0 * 8);
    }
    STAMP(6);
    {
      v4u hg; unsigned spins = 0;
      do { FENCE(); hg = LD16(gq * (FF * 8) + hpr * 8, WS_HG + hr0 * 8); } while (!__all(GBAD(hg) == 0) && ++spins < SPIN_CAP);
      NOTE(12, spins);
      float hs = fold16(gval(hg[0]), gval(hg[2])); hs = fold32(hs, hs);
      const float hp_ = rb(rb(hs) + b1v);
      const float u2 = 1.5957691216057308f * fmaf(0.044715f * hp_ * hp_, hp_, hp_);
      const float ge = hp_ * rcp(1.f + ex2(-u2 * LOG2E));
      if (l < 32) *(LAS u16*)(L + L_H + n * 4 + gq * 2) = (n < 14) ? (u16)pk(ge, ge) : (u16)0;
    }
    STAMP(7);
    BAR();
    STAMP(8);
    if (b < 64) {
      BAR();
      STAMP(9);
      float v = *(LAS float*)(L + L_S + l * 4) + *(LAS float*)(L + L_S + 256 + l * 4);
      v = fold16(v, v); v = fold32(v, v);
      const float f = rb(rb(dpp<R8>(v)) + b2v);
      const float gt = rb(f * rcp(1.f + ex2(-f * LOG2E)));
      const float o = xo + rb(rb(v) * gt);
      if (l < 8) out[ocol] = (u16)pk(o, o);
      if (b == 0 && l == 0) *(GAS u64*)ws = ep;
    }
    STAMP(10);
    FLUSH();
  }
}

extern "C" void kernel_launch(void* const* d_in, const int* in_sizes, int n_in, void* d_out, int out_size, void* d_ws, size_t ws_size, hipStream_t stream) {
  fused_decode<<<256, NTHR, 0, stream>>>((const u16*)d_in[0], (const u16*)d_in[1], (const u16*)d_in[2], (const u16*)d_in[3], (const u16*)d_in[4], (const u16*)d_in[6],
                                         (const u16*)d_in[7], (const u16*)d_in[8], (const u16*)d_in[9], (u16*)d_out, (unsigned char*)d_ws);
}
```

```cpp
#include <hip/hip_runtime.h>
#include <hip/hip_bf16.h>

typedef unsigned short u16;
typedef unsigned long long u64;
typedef unsigned v4u __attribute__((ext_vector_type(4)));
typedef unsigned v2u __attribute__((ext_vector_type(2)));
typedef float v4f __attribute__((ext_vector_type(4)));
typedef short s16x4 __attribute__((ext_vector_type(4)));
typedef __bf16 bf16x2 __attribute__((ext_vector_type(2)));
typedef __bf16 bf16x8 __attribute__((ext_vector_type(8)));
typedef __amdgpu_buffer_rsrc_t rsrc_t;
#define GAS __attribute__((address_space(1)))
#define CAS __attribute__((address_space(4)))
#define LAS __attribute__((address_space(3)))
#define SB() __builtin_amdgcn_sched_barrier(0)
#define LD_AUX 2
#define SPRIO 3
#define TRIG 0
#define W1E 1
#define LSM 1
#define MASKP 0
#define DELAY3 10
#define XOFF 1
#define CODEPF 1
#define QAUX 0

constexpr int SK = 32768, WIN = 4096, FF = 1792;
constexpr int NTHR = 576;
constexpr unsigned WS_QG = 4096;
constexpr unsigned WS_HG = WS_QG + 131072;
constexpr unsigned WS_ML = WS_HG + 114688;
constexpr unsigned WS_OB = WS_ML + 16384;
constexpr unsigned WS_SB = WS_OB + 524288;
constexpr unsigned WS_END = WS_SB + 524288;
constexpr unsigned WS_PROF = 4u << 20;
constexpr int VSTR = 288, L_V = 0, L_Q = 128 * VSTR, QSTR = 320, L_S = L_Q + 4 * QSTR, L_XN = L_S + 2048, L_MX = L_XN + 1024, L_H = L_MX + 128, L_WG = L_H + 64, L_O = (L_WG + 128 + 127) / 128 * 128, L_W = L_O + 4096, L_END = L_W + 96 * VSTR;
constexpr unsigned SPIN_CAP = 1u << 18;
constexpr float LOG2E = 1.4426950408889634f;
constexpr int OOB = 0x40000000;

__device__ __forceinline__ rsrc_t mk(const void* p, unsigned bytes) { return __builtin_amdgcn_make_buffer_rsrc((void*)p, 0, bytes, 0x00020000); }
__device__ __forceinline__ v4u ldw(rsrc_t r, int voff, int soff) { return __builtin_amdgcn_raw_buffer_load_b128(r, voff, soff, LD_AUX); }
__device__ __forceinline__ float bflo(unsigned u) { return __uint_as_float(u << 16); }
__device__ __forceinline__ float bfhi(unsigned u) { return __uint_as_float(u & 0xFFFF0000u); }
__device__ __forceinline__ unsigned pk(float lo, float hi) { bf16x2 r = {(__bf16)lo, (__bf16)hi}; return __builtin_bit_cast(unsigned, r); }
__device__ __forceinline__ float rb(float f) { return bflo(pk(f, f)); }
__device__ __forceinline__ float dot2(unsigned a, unsigned b, float c) { return __builtin_amdgcn_fdot2_f32_bf16(__builtin_bit_cast(bf16x2, a), __builtin_bit_cast(bf16x2, b), c, false); }
__device__ __forceinline__ v4f mfma(v4u a, v4u b, v4f c) { return __builtin_amdgcn_mfma_f32_16x16x32_bf16(__builtin_bit_cast(bf16x8, a), __builtin_bit_cast(bf16x8, b), c, 0, 0, 0); }
template <int CTRL> __device__ __forceinline__ float dpp(float x) { return __builtin_bit_cast(float, __builtin_amdgcn_mov_dpp(__builtin_bit_cast(int, x), CTRL, 0xf, 0xf, true)); }
constexpr int X1 = 0xB1, X2 = 0x4E, HM = 0x141, R8 = 0x128, R4 = 0x124;
__device__ __forceinline__ float sw16(float a, float b, float& o) { auto r = __builtin_amdgcn_permlane16_swap(__float_as_uint(a), __float_as_uint(b), false, false); o = __uint_as_float(r[1]); return __uint_as_float(r[0]); }
__device__ __forceinline__ float sw32(float a, float b, float& o) { auto r = __builtin_amdgcn_permlane32_swap(__float_as_uint(a), __float_as_uint(b), false, false); o = __uint_as_float(r[1]); return __uint_as_float(r[0]); }
__device__ __forceinline__ float fold16(float a, float b) { float o; float p = sw16(a, b, o); return p + o; }
__device__ __forceinline__ float fold32(float a, float b) { float o; float p = sw32(a, b, o); return p + o; }
__device__ __forceinline__ float fmx(float a, float b) { return __builtin_elementwise_maximum(a, b); }
__device__ __forceinline__ float max16(float a) { float o; float p = sw16(a, a, o); return fmx(p, o); }
__device__ __forceinline__ float max32(float a) { float o; float p = sw32(a, a, o); return fmx(p, o); }
__device__ __forceinline__ float sum64(float v) { v += dpp<X1>(v); v += dpp<X2>(v); v += dpp<HM>(v); v += dpp<R8>(v); v = fold16(v, v); return fold32(v, v); }
__device__ __forceinline__ v2u trd(LAS unsigned char* p) { return __builtin_bit_cast(v2u, __builtin_amdgcn_ds_read_tr16_b64_v4i16((LAS s16x4*)p)); }
__device__ __forceinline__ v4u cat(v2u a, v2u b) { return v4u{a[0], a[1], b[0], b[1]}; }
__device__ __forceinline__ float ex2(float x) { return __builtin_amdgcn_exp2f(x); }
__device__ __forceinline__ float rcp(float x) { return __builtin_amdgcn_rcpf(x); }
__device__ __forceinline__ float gval(unsigned lo) { return __uint_as_float(lo & 0xFFFFFF00u); }
#define BAR() do { asm volatile("s_waitcnt lgkmcnt(0)" ::: "memory"); __builtin_amdgcn_s_barrier(); asm volatile("" ::: "memory"); } while (0)
#define FENCE() asm volatile("" ::: "memory")
#define DRAIN() asm volatile("s_waitcnt vmcnt(0)" ::: "memory")
#define LD16(off, soff) __builtin_amdgcn_raw_buffer_load_b128(rs, (off), (soff), 16)
#define LD8(off, soff) __builtin_amdgcn_raw_buffer_load_b64(rs, (off), (soff), 16)
#define ST16(v, off, soff) __builtin_amdgcn_raw_buffer_store_b128((v), rs, (off), (soff), 16)
#define ST8(v, off, soff) __builtin_amdgcn_raw_buffer_store_b64((v), rs, (off), (soff), 16)
#define GRAN(f) (v2u{((__float_as_uint(f) + 0x80u) & 0xFFFFFF00u) | tlo, thi})
#define TBAD(v) (((v)[1] ^ thi) | ((v)[3] ^ thi))
#define GBAD(v) (((v)[1] ^ thi) | ((v)[3] ^ thi) | ((((v)[0] ^ tlo) | ((v)[2] ^ tlo)) & 0xFFu))
#define EPOCH() asm volatile("" : "+s"(ep0)); const u64 ep = ep0 + 1; const unsigned thi = (unsigned)ep, tlo = (unsigned)(ep >> 32) & 0xFFu
#define WAITV0() __builtin_amdgcn_s_waitcnt(0x0F70)
#define STAMP0()
#define CS(i)
#define STAMP(i)
#define NOTE(i, v)
#define FLUSH()

__device__ __forceinline__ void mksel(unsigned (&sel)[8], int l) {
#pragma unroll
  for (int d = 0; d < 4; ++d) { const bool on = ((l & 7) >> 1) == d;
    sel[d] = on ? ((l & 1) ? 0x01000c0cu : 0x0c0c0100u) : 0x0c0c0c0cu; sel[4 + d] = on ? ((l & 1) ? 0x03020c0cu : 0x0c0c0302u) : 0x0c0c0c0cu; }
}
__device__ __forceinline__ v4u diag(unsigned pair, const unsigned (&sel)[8], int hi) {
  return v4u{__builtin_amdgcn_perm(0u, pair, sel[4 * hi]), __builtin_amdgcn_perm(0u, pair, sel[4 * hi + 1]), __builtin_amdgcn_perm(0u, pair, sel[4 * hi + 2]), __builtin_amdgcn_perm(0u, pair, sel[4 * hi + 3])};
}
template <int CTRL, int RM, int BM> __device__ __forceinline__ float upd(float old, float src) {
  return __builtin_bit_cast(float, __builtin_amdgcn_update_dpp(__builtin_bit_cast(int, old), __builtin_bit_cast(int, src), CTRL, RM, BM, false));
}
__device__ __forceinline__ float diag_out(v4f d) {
  float t0 = upd<R8, 0x3, 0xC>(d[0], d[2]), t1 = upd<R8, 0x3, 0xC>(d[1], d[3]);
  t0 = upd<R8, 0xC, 0x3>(t0, d[0]); t1 = upd<R8, 0xC, 0x3>(t1, d[1]);
  t0 = upd<0xE4, 0xC, 0xC>(t0, d[2]); t1 = upd<0xE4, 0xC, 0xC>(t1, d[3]);
  return fold32(t0, t1);
}

#define POLL(v, BAD, vo, so, note) do { unsigned spins_ = 0; for (;;) { const bool ok_ = BAD(v) == 0; if (__all(ok_) || ++spins_ > SPIN_CAP) break; FENCE(); \
    const v4u nv_ = LD16((MASKP && ok_) ? OOB : (vo), (so)); v = (MASKP && ok_) ? v : nv_; } NOTE(note, spins_); } while (0)

__global__ void __launch_bounds__(NTHR, 3) fused_decode(const u16* __restrict__ x, const u16* __restrict__ kc, const u16* __restrict__ vc, const u16* __restrict__ wq,
                                                        const u16* __restrict__ wo, const u16* __restrict__ w1, const u16* __restrict__ b1, const u16* __restrict__ w2,
                                                        const u16* __restrict__ b2, u16* __restrict__ out, unsigned char* ws) {
  __shared__ __attribute__((aligned(16))) unsigned char lds_[L_END];
  LAS unsigned char* L = (LAS unsigned char*)lds_;
  const int b = blockIdx.x, g = b & 7, j = b >> 3, l = threadIdx.x & 63;
  int w = __builtin_amdgcn_readfirstlane(threadIdx.x >> 6);
  STAMP0();
  asm volatile("" :: "s"(x), "s"(kc), "s"(vc), "s"(wq), "s"(wo), "s"(w1), "s"(b1), "s"(w2), "s"(b2), "s"(out), "s"(ws));
  u64 ep0 = *(const CAS u64*)ws;
  const rsrc_t rs = mk(ws, WS_END);
  const int kq = j & 3, rb_ = j >> 2, gn = (g + XOFF) & 7, gp = (g - XOFF) & 7;
  const int gq = l >> 4, n = l & 15;
  const rsrc_t rk = mk(kc, 8u * SK * 256);
  const int kvo = (g * SK + (SK - WIN) + j * 128 + (w - 1) * 16) * 256, kof = (n >> 2) * 256 + gq * 64 + (n & 3) * 16;
  const rsrc_t rv = mk(vc, 8u * SK * 256);
  v4u vv[4];
  if (w != 0) {
    __builtin_amdgcn_s_barrier();
    SB();
#pragma unroll
    for (int i = 0; i < 4; ++i) vv[i] = ldw(rv, l * 16 + i * 1024, kvo);
    SB();
  }
  asm volatile("" : "+s"(w));
  if (__builtin_expect(w == 0, 1)) {
    v4u r16[16], f8[8]; unsigned cpf = 0;
    __builtin_amdgcn_s_setprio(3);
    const v4u xv = *(const GAS v4u*)(x + 8 * l);
#define LOADW1() { const rsrc_t r1 = mk(w1, 512 * FF * 2); const int vo1 = j < 28 ? (l >> 3) * (8 * FF * 2) + (l & 7) * 16 : OOB, so1 = g * (64 * FF * 2) + j * 128; \
    _Pragma("unroll") for (int i = 0; i < 8; ++i) f8[i] = ldw(r1, vo1, so1 + i * (FF * 2)); }
    { const rsrc_t r = mk(wq, 512 * 4096 * 2);
      const int vo = (l >> 3) * (16 * 8192) + (l & 7) * 16, so = kq * (128 * 8192) + gn * 1024 + rb_ * 128;
      { u64 pc; asm volatile("s_getpc_b64 %0" : "=s"(pc));
        cpf = *(const GAS unsigned*)((pc & ~127ull) + min(j * 2 + (l & 1), 53) * 128); }
#pragma unroll
      for (int i = 0; i < 16; ++i) r16[i] = ldw(r, vo, so + i * 8192);
      if (W1E == 1) LOADW1(); }
    SB();
    __builtin_amdgcn_s_barrier();
    SB();
    STAMP(1);
    float xf[8];
#pragma unroll
    for (int i = 0; i < 4; ++i) { xf[2 * i] = bflo(xv[i]); xf[2 * i + 1] = bfhi(xv[i]); }
    const float mean = sum64(((xf[0] + xf[1]) + (xf[2] + xf[3])) + ((xf[4] + xf[5]) + (xf[6] + xf[7]))) * (1.f / 512);
    float vs = 0.f;
#pragma unroll
    for (int i = 0; i < 8; ++i) { const float d = xf[i] - mean; vs = fmaf(d, d, vs); }
    const float var = sum64(vs) * (1.f / 512);
    const float mb = rb(mean), rsd = rcp(rb(__builtin_amdgcn_sqrtf(rb(rb(var) + 1e-5f))));
    v4u xn;
#pragma unroll
    for (int i = 0; i < 4; ++i) { const unsigned d = pk(xf[2 * i] - mb, xf[2 * i + 1] - mb); xn[i] = pk(bflo(d) * rsd, bfhi(d) * rsd); }
    *(LAS v4u*)(L + L_XN + l * 16) = xn;
    const v4u xa = *(LAS v4u*)(L + L_XN + kq * 256 + (l >> 3) * 32), xb = *(LAS v4u*)(L + L_XN + kq * 256 + (l >> 3) * 32 + 16);
    const v4u xc = *(LAS v4u*)(L + L_XN + g * 128 + (l >> 3) * 16);
    EPOCH();
    unsigned sel[8]; mksel(sel, l);
    const int gvo = ((l & 24) * 2 + (l >> 5) * 8 + (l & 7)) * 8;
    { v4u bx[8];
#pragma unroll
      for (int i = 0; i < 8; ++i) bx[i] = diag(xa[i >> 1], sel, i & 1);
      SB();
      STAMP(2);
      v4f d = {0.f, 0.f, 0.f, 0.f};
#pragma unroll
      for (int i = 0; i < 8; ++i) { d = mfma(r16[i], bx[i], d); bx[i] = diag(xb[i >> 1], sel, i & 1); SB(); }
#pragma unroll
      for (int i = 0; i < 8; ++i) d = mfma(r16[8 + i], bx[i], d);
      const float qv = diag_out(d);
      asm volatile("" :: "v"(cpf));
      ST8(GRAN(qv), gvo, WS_QG + gn * 16384 + kq * 4096 + rb_ * 512);
    }
    STAMP(3);
    if (W1E == 0) { LOADW1(); SB(); }
    { v4f d = {0.f, 0.f, 0.f, 0.f};
#pragma unroll
      for (int i = 0; i < 8; ++i) d = mfma(f8[i], diag(xc[i >> 1], sel, i & 1), d);
      const float hv = diag_out(d);
      ST8(GRAN(hv), j < 28 ? gvo : OOB, WS_HG + g * (FF * 8) + j * 512); }
    const int hr0 = gp * 224 + rb_ * 28, hpr = min(n, 13) * 2, ocol = (b & 63) * 8 + (l & 7);
    int mlo = (l & 51) == 0 ? (n >> 2) * 512 : OOB, mls = WS_ML + g * 2048 + j * 16, hvo = gq * (FF * 8) + hpr * 8, hso = WS_HG + hr0 * 8;
    int pvo = (l & 31) * 16, pso = WS_ML + gp * 2048 + (rb_ >> 1) * 512;
    asm volatile("" : "+v"(mlo), "+s"(mls), "+v"(hvo), "+s"(hso), "+v"(pvo), "+s"(pso));
    __builtin_amdgcn_s_setprio(SPRIO);
    STAMP(4);
    BAR();
    BAR();
    const float b1v = bflo(b1[hr0 + hpr + (gq & 1)]), b2v = bflo(b2[ocol]), xo = bflo(x[ocol]);
    SB();
    BAR();
    { const v4f s0 = *(LAS v4f*)(L + L_S + l * 16), s1 = *(LAS v4f*)(L + L_S + 1024 + l * 16);
      const v2u mm = *(LAS v2u*)(L + L_MX + n * 8);
      float m = fmx(__uint_as_float(mm[0]), __uint_as_float(mm[1]));
      m = fmx(fmx(m, dpp<X1>(m)), fmx(dpp<X2>(m), dpp<0x1B>(m)));
      float ls = fmaf((s0[0] + s0[1]) + (s0[2] + s0[3]), ex2(__uint_as_float(mm[0]) - m), ((s1[0] + s1[1]) + (s1[2] + s1[3])) * ex2(__uint_as_float(mm[1]) - m));
      ls += dpp<X1>(ls); ls += dpp<X2>(ls); ls = fold16(ls, ls); ls = fold32(ls, ls);
      ST16((v4u{__float_as_uint(m), thi, __float_as_uint(ls), thi}), mlo, mls);
    }
    STAMP(6);
    v4u hg = LD16(hvo, hso), hh = LD16(hvo, hso + 4 * FF * 8);
    v4u pv = LD16(pvo, pso);
    SB();
    BAR();
    {
      POLL(hg, GBAD, hvo, hso, 14); POLL(hh, GBAD, hvo, hso + 4 * FF * 8, 14);
      float hs = fold16(gval(hg[0]) + gval(hh[0]), gval(hg[2]) + gval(hh[2])); hs = fold32(hs, hs);
      const float hp_ = rb(rb(hs) + b1v);
      const float u2 = 1.5957691216057308f * fmaf(0.044715f * hp_ * hp_, hp_, hp_);
      const float ge = hp_ * rcp(1.f + ex2(-u2 * LOG2E));
      if (l < 32) *(LAS u16*)(L + L_H + n * 4 + gq * 2) = (n < 14) ? (u16)pk(ge, ge) : (u16)0;
    }
    STAMP(7);
    {
      POLL(pv, TBAD, pvo, pso, 12);
      const float m = __uint_as_float(pv[0]);
      float M = fmx(fmx(m, dpp<X1>(m)), fmx(dpp<X2>(m), dpp<0x1B>(m))); M = fmx(fmx(M, dpp<R4>(M)), fmx(dpp<R8>(M), dpp<0x12C>(M))); M = max16(M);
      const float e = ex2(m - M);
      float ls = e * __uint_as_float(pv[2]);
      ls += dpp<X1>(ls); ls += dpp<X2>(ls); ls += dpp<HM>(ls); ls += dpp<R8>(ls); ls = fold16(ls, ls);
      *(LAS float*)(L + L_WG + (l & 31) * 4) = e * rcp(ls);
    }
    STAMP(8);
    BAR();
    if (b < 64) {
      BAR();
      STAMP(9);
      float v = *(LAS float*)(L + L_S + l * 4) + *(LAS float*)(L + L_S + 256 + l * 4);
      v = fold16(v, v); v = fold32(v, v);
      const float f = rb(rb(dpp<R8>(v)) + b2v);
      const float gt = rb(f * rcp(1.f + ex2(-f * LOG2E)));
      const float o = xo + rb(rb(v) * gt);
      if (l < 8) out[ocol] = (u16)pk(o, o);
      if (b == 0 && l == 0) *(GAS u64*)ws = ep;
    }
    STAMP(10);
    FLUSH();
  } else {
    const int a = w - 1;
    __builtin_amdgcn_s_setprio(2);
    v4u wv[3];
    EPOCH();
    int obo = (n & 3) == 0 ? (n >> 2) * 16384 + gq * 16 : OOB;
    int sbo = l < 32 ? (n >> 3) * 8192 + gq * 64 + (n & 7) * 8 : OOB;
    int obs = WS_OB + g * 65536 + (a >> 2) * 8192 + j * 256 + (a & 3) * 64, sbs = WS_SB + (kq * 16 + a * 2) * 8192 + (g * 8 + rb_) * 128;
    int sadr = L_S + (a & 1) * 1024 + (n >> 2) * 256 + (n & 3) * 64 + (a >> 1) * 16 + gq * 4, qadr = L_Q + (n & 3) * QSTR + gq * 64 + (n >> 2) * 16;
    int wos = (gp * 512 + rb_ * 64 + a * 8) * 1024 + kq * 256, w2s = (gp * 224 + rb_ * 28 + min(a, 6) * 4) * 1024 + kq * 256, wvo = gq * 1024 + n * 16;
    int pso = WS_OB + gp * 65536 + rb_ * 8192 + a * 1024;
    int oadr = L_O + n * 128 + ((gq ^ (n >> 1)) & 7) * 16, owr = L_O + (a * 4 + gq) * 128 + ((((n >> 1) ^ ((a * 4 + gq) >> 1)) & 7) * 16) + (n & 1) * 8;
    int fso = WS_SB + b * 8192 + a * 1024, fadr = L_S + a * 64 + (l & 7) * 8 + (l >> 5) * 4, vwr = L_V + (a * 16 + gq) * VSTR + n * 16;
    asm volatile("" : "+v"(obo), "+v"(sbo), "+v"(sadr), "+v"(qadr), "+v"(wvo), "+v"(oadr), "+v"(owr), "+v"(fadr), "+v"(vwr));
    asm volatile("" : "+s"(obs), "+s"(sbs), "+s"(wos), "+s"(w2s), "+s"(pso), "+s"(fso));
    const rsrc_t ro = mk(wo, 4096 * 512 * 2), r2 = mk(w2, FF * 512 * 2);
    SB();
    int tadr = L_V + (gq * 4 + (n >> 2)) * VSTR + a * 32 + (n & 3) * 8, qso = WS_QG + g * 16384 + (a & 3) * 1024, qvo = a >= 4 ? l * 16 : OOB;
    int mxw = L_MX + (n & 3) * 32 + a * 4, qwr = L_Q + (a & 3) * QSTR + l * 4;
    asm volatile("" : "+v"(tadr), "+s"(qso), "+v"(qvo), "+v"(mxw), "+v"(qwr));
    v4u kv[4], qg[4];
#pragma unroll
    for (int i = 0; i < 4; ++i) {
      asm volatile("" : "+v"(vv[i]));
      if (i == TRIG) {
#pragma unroll
        for (int k = 0; k < 4; ++k) kv[k] = ldw(rk, kof + k * 1024, kvo);
#pragma unroll
        for (int k = 0; k < 4; ++k) qg[k] = __builtin_amdgcn_raw_buffer_load_b128(rs, qvo, qso + k * 4096, QAUX);
        STAMP(1);
      }
      *(LAS v4u*)(L + vwr + i * 4 * VSTR) = vv[i];
      SB();
    }
    STAMP(2);
    BAR();
    v2u vt[8];
#pragma unroll
    for (int i = 0; i < 8; ++i) vt[i] = trd(L + tadr + i * 16 * VSTR);
    SB();
#define QSUM() *(LAS unsigned*)(L + qwr) = pk((gval(qg[0][0]) + gval(qg[1][0])) + (gval(qg[2][0]) + gval(qg[3][0])), (gval(qg[0][2]) + gval(qg[1][2])) + (gval(qg[2][2]) + gval(qg[3][2])))
    if (a >= 4) {
      unsigned spins = 0;
      QSUM();
      SB();
      if (__builtin_expect(!__all((GBAD(qg[0]) | GBAD(qg[1]) | GBAD(qg[2]) | GBAD(qg[3])) == 0), 0)) {
        for (;;) {
          bool ok = true, o[4];
#pragma unroll
          for (int i = 0; i < 4; ++i) { o[i] = __all(GBAD(qg[i]) == 0); ok &= o[i]; }
          if (ok || ++spins > SPIN_CAP) break;
          FENCE();
#pragma unroll
          for (int i = 0; i < 4; ++i) if (!o[i]) qg[i] = LD16(l * 16, qso + i * 4096);
          WAITV0();
        }
        QSUM();
      }
      NOTE(11, spins);
    }
    WAITV0();
    STAMP(4);
    CS(1);
    BAR();
    CS(2);
    {
      const v4u qf = *(LAS v4u*)(L + qadr);
      SB();
      wv[0] = ldw(ro, wvo, wos); wv[1] = ldw(ro, wvo + 4096, wos); wv[2] = ldw(r2, wvo, w2s);
      SB();
      CS(3);
      v4f sc[4];
#pragma unroll
      for (int i = 0; i < 4; ++i) sc[i] = mfma(kv[i], qf, v4f{0.f, 0.f, 0.f, 0.f});
      float t0 = sc[0][0] + dpp<0x12C>(sc[0][1]), t1 = sc[1][1] + dpp<0x124>(sc[1][0]), t2 = sc[2][2] + dpp<0x128>(sc[2][0]), t3 = sc[3][3] + dpp<0x12C>(sc[3][0]);
      asm volatile("" : "+v"(t0), "+v"(t1)); asm volatile("" : "+v"(t2), "+v"(t3));
      t0 += dpp<0x128>(sc[0][2]); t1 += dpp<0x12C>(sc[1][2]); t2 += dpp<0x124>(sc[2][1]); t3 += dpp<0x128>(sc[3][1]);
      asm volatile("" : "+v"(t0), "+v"(t1)); asm volatile("" : "+v"(t2), "+v"(t3));
      t0 += dpp<0x124>(sc[0][3]); t1 += dpp<0x128>(sc[1][3]); t2 += dpp<0x12C>(sc[2][3]); t3 += dpp<0x124>(sc[3][2]);
      asm volatile("" : "+v"(t0), "+v"(t1)); asm volatile("" : "+v"(t2), "+v"(t3));
      const float s1 = (n & 8) ? ((n & 4) ? t3 : t2) : ((n & 4) ? t1 : t0);
      float s2 = rb(rb(s1) * 0.08838834764831845f) * LOG2E;
      asm volatile("" : "+v"(s2));
      CS(4);
      float m = fmx(fmx(s2, dpp<R4>(s2)), fmx(dpp<R8>(s2), dpp<0x12C>(s2))); m = max16(m); m = max32(m);
      *(LAS float*)(L + mxw) = m;
      *(LAS float*)(L + sadr) = ex2(s2 - m);
    }
    STAMP(5);
    CS(5);
    BAR();
    CS(6);
    {
      const v2u mm = *(LAS v2u*)(L + L_MX + n * 8);
      const v4f s0 = *(LAS v4f*)(L + L_S + l * 16), s1 = *(LAS v4f*)(L + L_S + 1024 + l * 16);
      float m = fmx(__uint_as_float(mm[0]), __uint_as_float(mm[1]));
      m = fmx(fmx(m, dpp<X1>(m)), fmx(dpp<X2>(m), dpp<0x1B>(m)));
      const v4f p0 = s0 * ex2(__uint_as_float(mm[0]) - m), p1 = s1 * ex2(__uint_as_float(mm[1]) - m);
      const v4u pb = {pk(p0[0], p0[1]), pk(p0[2], p0[3]), pk(p1[0], p1[1]), pk(p1[2], p1[3])};
      CS(7);
      v4f d[4];
#pragma unroll
      for (int i = 0; i < 4; ++i) d[i] = mfma(cat(vt[2 * i], vt[2 * i + 1]), pb, v4f{0.f, 0.f, 0.f, 0.f});
      SB();
      float o[4];
#pragma unroll
      for (int r = 0; r < 4; ++r) o[r] = d[0][r] + dpp<0x55>(d[1][r]);
      asm volatile("" : "+v"(o[0]), "+v"(o[1]), "+v"(o[2]), "+v"(o[3]));
#pragma unroll
      for (int r = 0; r < 4; ++r) o[r] += dpp<0xAA>(d[2][r]);
      asm volatile("" : "+v"(o[0]), "+v"(o[1]), "+v"(o[2]), "+v"(o[3]));
#pragma unroll
      for (int r = 0; r < 4; ++r) o[r] += dpp<0xFF>(d[3][r]);
      asm volatile("" : "+v"(o[0]), "+v"(o[1]), "+v"(o[2]), "+v"(o[3]));
      CS(8);
      ST16((v4u{pk(o[0], o[1]), thi, pk(o[2], o[3]), thi}), obo, obs);
      CS(9);
    }
    STAMP(6);
    v4u pv = LD16(l * 16, pso);
    SB();
#pragma unroll
    for (int i = 0; i < 3; ++i) *(LAS v4u*)(L + L_W + ((i < 2 ? a * 8 + i * 4 : 64 + a * 4) + gq) * VSTR + n * 16) = wv[i];
    BAR();
    v2u wt[6];
#pragma unroll
    for (int i = 0; i < 6; ++i) wt[i] = trd(L + L_W + (gq * 8 + (n >> 2)) * VSTR + a * 32 + (n & 3) * 8 + ((i >> 1) * 32 + (i & 1) * 4) * VSTR);
    SB();
    POLL(pv, TBAD, l * 16, pso, 12);
    STAMP(8);
    *(LAS v2u*)(L + owr) = v2u{pv[0], pv[2]};
    STAMP(9);
    BAR();
    { v4u of[4];
      of[0] = *(LAS v4u*)(L + oadr); of[2] = *(LAS v4u*)(L + oadr + 2048); of[1] = *(LAS v4u*)(L + (oadr ^ 64)); of[3] = *(LAS v4u*)(L + (oadr ^ 64) + 2048);
      const v4f w0 = *(LAS v4f*)(L + L_WG + gq * 16), w1_ = *(LAS v4f*)(L + L_WG + 64 + gq * 16);
      const v4u hf = *(LAS v4u*)(L + L_H + gq * 16);
      SB();
      const v4f z = {0.f, 0.f, 0.f, 0.f};
      v4f d0 = mfma(of[0], cat(wt[0], wt[1]), z), d1 = mfma(of[2], cat(wt[0], wt[1]), z);
      d0 = mfma(of[1], cat(wt[2], wt[3]), d0); d1 = mfma(of[3], cat(wt[2], wt[3]), d1);
      const v4f df = mfma(hf, cat(wt[4], wt[5]), z);
      float s = (fmaf(d0[1], w0[1], d0[0] * w0[0]) + fmaf(d0[3], w0[3], d0[2] * w0[2])) + (fmaf(d1[1], w1_[1], d1[0] * w1_[0]) + fmaf(d1[3], w1_[3], d1[2] * w1_[2]));
      s = fold32(s, s);
      const float e = fold16(s, 0.5f * df[0]);
      ST8(GRAN(e), sbo, sbs);
    }
    STAMP(10);
    if (b < 64) {
      if (DELAY3 && a == g) __builtin_amdgcn_s_sleep(DELAY3);
      v4u sv = LD16(l * 16, fso);
      POLL(sv, GBAD, l * 16, fso, 13);
      float v = fold32(gval(sv[0]), gval(sv[2]));
      v = fold16(v, v);
      v += dpp<R8>(v);
      *(LAS float*)(L + fadr) = v;
      BAR();
    }
    FLUSH();
  }
}

extern "C" void kernel_launch(void* const* d_in, const int* in_sizes, int n_in, void* d_out, int out_size, void* d_ws, size_t ws_size, hipStream_t stream) {
  fused_decode<<<256, NTHR, 0, stream>>>((const u16*)d_in[0], (const u16*)d_in[1], (const u16*)d_in[2], (const u16*)d_in[3], (const u16*)d_in[4], (const u16*)d_in[6],
                                         (const u16*)d_in[7], (const u16*)d_in[8], (const u16*)d_in[9], (u16*)d_out, (unsigned char*)d_ws);
}
```

```cpp
#include <hip/hip_runtime.h>
#include <hip/hip_bf16.h>

typedef unsigned short u16;
typedef unsigned long long u64;
typedef unsigned v4u __attribute__((ext_vector_type(4)));
typedef unsigned v2u __attribute__((ext_vector_type(2)));
typedef float v4f __attribute__((ext_vector_type(4)));
typedef short s16x4 __attribute__((ext_vector_type(4)));
typedef __bf16 bf16x2 __attribute__((ext_vector_type(2)));
typedef __bf16 bf16x8 __attribute__((ext_vector_type(8)));
typedef __amdgpu_buffer_rsrc_t rsrc_t;
#define GAS __attribute__((address_space(1)))
#define CAS __attribute__((address_space(4)))
#define LAS __attribute__((address_space(3)))
#define SB() __builtin_amdgcn_sched_barrier(0)
#define LD_AUX 2
#define SPRIO 3
#define TRIG 0
#define QTRIG 4
#define KTRIG 0
#define QT 250
#define PFLINES 58
#define P2I 2
#define HS1 5
#define HS2 12
#define QSLEEP 0
#define DELAY3 14
#define XOFF 1
#define CODEPF 1
#define QAUX 0

constexpr int SK = 32768, WIN = 4096, FF = 1792;
constexpr int NTHR = 576;
constexpr unsigned WS_QG = 4096;
constexpr unsigned WS_HG = WS_QG + 65536;
constexpr unsigned WS_ML = WS_HG + 57344;
constexpr unsigned WS_OB = WS_ML + 16384;
constexpr unsigned WS_SB = WS_OB + 524288;
constexpr unsigned WS_END = WS_SB + 524288;
constexpr unsigned WS_PROF = 4u << 20;
constexpr int VSTR = 288, L_V = 0, L_O = 128 * VSTR, L_Q = L_O + 4096, QSTR = 320, L_S = L_Q + 4 * QSTR, L_XN = L_S + 2048, L_MX = L_XN + 1024, L_H = L_MX + 128,
              L_WG = L_H + 64, L_W = L_WG + 128, WBLK = 1088, L_END = L_W + 24 * WBLK;
static_assert(L_O % 128 == 0, "the o image is addressed with ^ 64");
constexpr unsigned SPIN_CAP = 1u << 18;
constexpr float LOG2E = 1.4426950408889634f;
constexpr int OOB = 0x40000000;

__device__ __forceinline__ rsrc_t mk(const void* p, unsigned bytes) { return __builtin_amdgcn_make_buffer_rsrc((void*)p, 0, bytes, 0x00020000); }
__device__ __forceinline__ v4u ldw(rsrc_t r, int voff, int soff) { return __builtin_amdgcn_raw_buffer_load_b128(r, voff, soff, LD_AUX); }
__device__ __forceinline__ float bflo(unsigned u) { return __uint_as_float(u << 16); }
__device__ __forceinline__ float bfhi(unsigned u) { return __uint_as_float(u & 0xFFFF0000u); }
__device__ __forceinline__ unsigned pk(float lo, float hi) { bf16x2 r = {(__bf16)lo, (__bf16)hi}; return __builtin_bit_cast(unsigned, r); }
__device__ __forceinline__ float rb(float f) { return bflo(pk(f, f)); }
__device__ __forceinline__ v4f mfma(v4u a, v4u b, v4f c) { return __builtin_amdgcn_mfma_f32_16x16x32_bf16(__builtin_bit_cast(bf16x8, a), __builtin_bit_cast(bf16x8, b), c, 0, 0, 0); }
template <int CTRL> __device__ __forceinline__ unsigned dppu(unsigned x) { return (unsigned)__builtin_amdgcn_mov_dpp((int)x, CTRL, 0xf, 0xf, true); }
template <int CTRL> __device__ __forceinline__ float dpp(float x) { return __uint_as_float(dppu<CTRL>(__float_as_uint(x))); }
constexpr int X1 = 0xB1, X2 = 0x4E, X3 = 0x1B, HM = 0x141, R4 = 0x124, R8 = 0x128, R12 = 0x12C;
__device__ __forceinline__ float sw16(float a, float b, float& o) { auto r = __builtin_amdgcn_permlane16_swap(__float_as_uint(a), __float_as_uint(b), false, false); o = __uint_as_float(r[1]); return __uint_as_float(r[0]); }
__device__ __forceinline__ float sw32(float a, float b, float& o) { auto r = __builtin_amdgcn_permlane32_swap(__float_as_uint(a), __float_as_uint(b), false, false); o = __uint_as_float(r[1]); return __uint_as_float(r[0]); }
__device__ __forceinline__ float fold16(float a, float b) { float o; float p = sw16(a, b, o); return p + o; }
__device__ __forceinline__ float fold32(float a, float b) { float o; float p = sw32(a, b, o); return p + o; }
__device__ __forceinline__ float fmx(float a, float b) { return __builtin_elementwise_maximum(a, b); }
__device__ __forceinline__ float max16(float a) { float o; float p = sw16(a, a, o); return fmx(p, o); }
__device__ __forceinline__ float max32(float a) { float o; float p = sw32(a, a, o); return fmx(p, o); }
__device__ __forceinline__ float maxq(float m) { return fmx(fmx(m, dpp<X1>(m)), fmx(dpp<X2>(m), dpp<X3>(m))); }
__device__ __forceinline__ float maxb(float m) { return fmx(fmx(m, dpp<R4>(m)), fmx(dpp<R8>(m), dpp<R12>(m))); }
__device__ __forceinline__ float sum64(float v) { v += dpp<X1>(v); v += dpp<X2>(v); v += dpp<HM>(v); v += dpp<R8>(v); v = fold16(v, v); return fold32(v, v); }
__device__ __forceinline__ v2u trd(LAS unsigned char* p) { return __builtin_bit_cast(v2u, __builtin_amdgcn_ds_read_tr16_b64_v4i16((LAS s16x4*)p)); }
__device__ __forceinline__ v4u cat(v2u a, v2u b) { return v4u{a[0], a[1], b[0], b[1]}; }
__device__ __forceinline__ float ex2(float x) { return __builtin_amdgcn_exp2f(x); }
__device__ __forceinline__ float rcp(float x) { return __builtin_amdgcn_rcpf(x); }
__device__ __forceinline__ float gval(unsigned d) { return __uint_as_float(d & 0xFFFFFF00u); }
__device__ __forceinline__ unsigned d4(float f, unsigned tagbyte) { return ((__float_as_uint(f) + 0x80u) & 0xFFFFFF00u) | tagbyte; }
#define LANES(mask) __builtin_amdgcn_inverse_ballot_w64(mask)
#define BAR() do { asm volatile("s_waitcnt lgkmcnt(0)" ::: "memory"); __builtin_amdgcn_s_barrier(); asm volatile("" ::: "memory"); } while (0)
#define FENCE() asm volatile("" ::: "memory")
#define WAITV0() __builtin_amdgcn_s_waitcnt(0x0F70)
#define LD16(off, soff) __builtin_amdgcn_raw_buffer_load_b128(rs, (off), (soff), 16)
#define ST16(v, off, soff) __builtin_amdgcn_raw_buffer_store_b128((v), rs, (off), (soff), 16)
#define ST8(v, off, soff) __builtin_amdgcn_raw_buffer_store_b64((v), rs, (off), (soff), 16)
#define TBAD(v) (((v)[1] ^ tag) | ((v)[3] ^ tag))
#define DBAD(v) ((__builtin_amdgcn_perm((v)[1], (v)[0], 0x0c0c0400u) | __builtin_amdgcn_perm((v)[3], (v)[2], 0x04000c0cu)) ^ tag)
#define EPOCH() asm volatile("" : "+s"(ep0)); const unsigned tag = ((ep0 & 0x7F7F7F7Fu) + 0x01010101u) ^ (ep0 & 0x80808080u)
#define STAMP0()
#define CS(i)
#define STAMP(i)
#define NOTE(i, v)
#define FLUSH()

__device__ __forceinline__ void mksel(unsigned (&sel)[8], int l) {
#pragma unroll
  for (int d = 0; d < 4; ++d) { const bool on = ((l & 7) >> 1) == d;
    sel[d] = on ? ((l & 1) ? 0x01000c0cu : 0x0c0c0100u) : 0x0c0c0c0cu; sel[4 + d] = on ? ((l & 1) ? 0x03020c0cu : 0x0c0c0302u) : 0x0c0c0c0cu; }
}
__device__ __forceinline__ v4u diag(unsigned pair, const unsigned (&sel)[8], int hi) {
  return v4u{__builtin_amdgcn_perm(0u, pair, sel[4 * hi]), __builtin_amdgcn_perm(0u, pair, sel[4 * hi + 1]), __builtin_amdgcn_perm(0u, pair, sel[4 * hi + 2]), __builtin_amdgcn_perm(0u, pair, sel[4 * hi + 3])};
}
__device__ __forceinline__ float upr8(float v) { return __builtin_bit_cast(float, __builtin_amdgcn_update_dpp(__builtin_bit_cast(int, v), __builtin_bit_cast(int, v), R8, 0xC, 0xF, false)); }
__device__ __forceinline__ v2u diag_out(v4f d, unsigned tb0, unsigned tb1) {
  const float t0 = upr8(d[0]), t1 = upr8(d[1]), t2 = upr8(d[2]), t3 = upr8(d[3]);
  return v2u{d4(fold32(t0, t2), tb0), d4(fold32(t1, t3), tb1)};
}

#define POLL(v, BAD, vo, so, note) do { unsigned spins_ = 0; _Pragma("nounroll") while (!__all(BAD(v) == 0) && ++spins_ < SPIN_CAP) { FENCE(); v = LD16((vo), (so)); } NOTE(note, spins_); } while (0)
#define POLL2(v, u, BAD, vo, so, so2, note) do { unsigned spins_ = 0; _Pragma("nounroll") while (!__all((BAD(v) | BAD(u)) == 0) && ++spins_ < SPIN_CAP) { FENCE(); v = LD16((vo), (so)); u = LD16((vo), (so2)); WAITV0(); } NOTE(note, spins_); } while (0)

__global__ void __launch_bounds__(NTHR, 3) fused_decode(const u16* __restrict__ x, const u16* __restrict__ kc, const u16* __restrict__ vc, const u16* __restrict__ wq,
                                                        const u16* __restrict__ wo, const u16* __restrict__ w1, const u16* __restrict__ b1, const u16* __restrict__ w2,
                                                        const u16* __restrict__ b2, u16* __restrict__ out, unsigned char* ws) {
  __shared__ __attribute__((aligned(128))) unsigned char lds_[L_END];
  LAS unsigned char* L = (LAS unsigned char*)lds_;
  const int b = blockIdx.x, g = b & 7, j = b >> 3, l = threadIdx.x & 63;
  int w = __builtin_amdgcn_readfirstlane(threadIdx.x >> 6);
  STAMP0();
  unsigned cpf;
  { u64 pc;
    asm volatile("s_cmp_lg_u32 %2, 4\n\ts_cbranch_scc1 1f\n\ts_getpc_b64 %0\n\tv_min_u32 %1, %4, %3\n\tv_lshlrev_b32 %1, 7, %1\n\tglobal_load_dword %1, %1, %0\n1:"
                 : "=&s"(pc), "=&v"(cpf) : "s"(w), "v"(l), "i"(PFLINES) : "scc", "memory"); }
#define PADTO(k, nxt) do { SB(); asm volatile(".Lpad" #k ":\n\ts_cbranch_execz " nxt); SB(); } while (0)
#define HOPS() asm volatile("s_sleep %1\n\ts_cmp_lg_u32 %0, 8\n\ts_cbranch_scc1 1f\n\ts_mov_b64 exec, 0\n\ts_branch .Lpad4\n1:\ts_sleep %2\n.Lpadret:\n\ts_mov_b64 exec, -1" :: "s"(w), "i"(HS1), "i"(HS2) : "scc")
  asm volatile("" :: "s"(x), "s"(kc), "s"(vc), "s"(wq), "s"(wo), "s"(w1), "s"(b1), "s"(w2), "s"(b2), "s"(out), "s"(ws));
  unsigned ep0 = *(const CAS unsigned*)ws;
  const unsigned tstart = QTRIG == 6 ? (unsigned)__builtin_amdgcn_s_memrealtime() : 0u;
  const rsrc_t rs = mk(ws, WS_END);
  const int kq = j & 3, rb_ = j >> 2, gn = (g + XOFF) & 7, gp = (g - XOFF) & 7;
  const int gq = l >> 4, n = l & 15;
  const int kvo = (g * SK + (SK - WIN) + j * 128 + (w - 1) * 16) * 256;
  v4u vv[4];
  if (w != 0) {
    const rsrc_t rv = mk(vc, 8u * SK * 256);
    __builtin_amdgcn_s_barrier();
    SB();
#pragma unroll
    for (int i = 0; i < 4; ++i) vv[i] = ldw(rv, l * 16 + i * 1024, kvo);
    SB();
    HOPS();
  }
  asm volatile("" : "+s"(w));
  if (__builtin_expect(w == 0, 1)) {
    v4u r16[16], f8[8];
    __builtin_amdgcn_s_setprio(3);
    const v4u xv = *(const GAS v4u*)(x + 8 * l);
    { const rsrc_t r = mk(wq, 512 * 4096 * 2), r1 = mk(w1, 512 * FF * 2);
      const int vo = (l >> 3) * (16 * 8192) + (l & 7) * 16, so = kq * (128 * 8192) + gn * 1024 + rb_ * 128;
      const int vo1 = j < 28 ? (l >> 3) * (8 * FF * 2) + (l & 7) * 16 : OOB, so1 = g * (64 * FF * 2) + j * 128;
#pragma unroll
      for (int i = 0; i < 24; ++i) {
        if (i < 16) r16[i] = ldw(r, vo, so + i * 8192); else f8[i - 16] = ldw(r1, vo1, so1 + (i - 16) * (FF * 2)); }
    }
    SB();
    __builtin_amdgcn_s_barrier();
    SB();
    STAMP(1);
    unsigned sel[8]; mksel(sel, l);
    int gvo = (l & 8) ? OOB : (l & 7) * 32 + (l & 16) + (l >> 5) * 8, xra = L_XN + kq * 256 + (l >> 3) * 32, xrc = L_XN + g * 128 + (l >> 3) * 16;
    asm volatile("" : "+v"(sel[0]), "+v"(sel[1]), "+v"(sel[2]), "+v"(sel[3]), "+v"(sel[4]), "+v"(sel[5]), "+v"(sel[6]), "+v"(sel[7]), "+v"(gvo), "+v"(xra), "+v"(xrc));
    SB();
    float xf[8];
#pragma unroll
    for (int i = 0; i < 4; ++i) { xf[2 * i] = bflo(xv[i]); xf[2 * i + 1] = bfhi(xv[i]); }
    const float mean = sum64(((xf[0] + xf[1]) + (xf[2] + xf[3])) + ((xf[4] + xf[5]) + (xf[6] + xf[7]))) * (1.f / 512);
    float vs = 0.f;
#pragma unroll
    for (int i = 0; i < 8; ++i) { const float d = xf[i] - mean; vs = fmaf(d, d, vs); }
    const float var = sum64(vs) * (1.f / 512);
    const float mb = rb(mean), rsd = rcp(rb(__builtin_amdgcn_sqrtf(rb(rb(var) + 1e-5f))));
    v4u xn;
#pragma unroll
    for (int i = 0; i < 4; ++i) { const unsigned d = pk(xf[2 * i] - mb, xf[2 * i + 1] - mb); xn[i] = pk(bflo(d) * rsd, bfhi(d) * rsd); }
    *(LAS v4u*)(L + L_XN + l * 16) = xn;
    const v4u xa = *(LAS v4u*)(L + xra), xb = *(LAS v4u*)(L + xra + 16), xc = *(LAS v4u*)(L + xrc);
    EPOCH();
    const unsigned tb0 = (tag >> ((l >> 5) * 16)) & 0xFFu, tb1 = (tag >> ((l >> 5) * 16 + 8)) & 0xFFu;
    { v4u bx[8];
#pragma unroll
      for (int i = 0; i < 8; ++i) bx[i] = diag(xa[i >> 1], sel, i & 1);
      SB();
      STAMP(2);
      v4f d = {0.f, 0.f, 0.f, 0.f};
#pragma unroll
      for (int i = 0; i < 8; ++i) { d = mfma(bx[i], r16[i], d); bx[i] = diag(xb[i >> 1], sel, i & 1); SB(); if (i == P2I) PADTO(2, ".Lpad8"); }
#pragma unroll
      for (int i = 0; i < 8; ++i) d = mfma(bx[i], r16[8 + i], d);
      ST8(diag_out(d, tb0, tb1), gvo, WS_QG + gn * 8192 + kq * 2048 + rb_ * 256);
    }
    STAMP(3);
    { v4f d = {0.f, 0.f, 0.f, 0.f};
#pragma unroll
      for (int i = 0; i < 8; ++i) d = mfma(diag(xc[i >> 1], sel, i & 1), f8[i], d);
      ST8(diag_out(d, tb0, tb1), j < 28 ? gvo : OOB, WS_HG + g * (FF * 4) + j * 256); }
    PADTO(3, ".Lpad2");
    const int hr0 = gp * 224 + rb_ * 28, hu = min(l & 7, 6), ocol = (b & 63) * 8 + (l & 7);
    int mlo = (l & 51) == 0 ? (n >> 2) * 512 : OOB, mls = WS_ML + g * 2048 + j * 16, hvo = (l >> 3) * (FF * 4) + hu * 16, hso = WS_HG + hr0 * 4;
    int pvo = (l & 31) * 16, pso = WS_ML + gp * 2048 + (rb_ >> 1) * 512;
    asm volatile("" : "+v"(mlo), "+s"(mls), "+v"(hvo), "+s"(hso), "+v"(pvo), "+s"(pso));
    __builtin_amdgcn_s_setprio(SPRIO);
    STAMP(4);
    BAR();
    BAR();
    const float b1v = bflo(b1[hr0 + hu * 4 + gq]), b2v = bflo(b2[ocol]), xo = bflo(x[ocol]);
    v4u hg = LD16(hvo, hso);
    SB();
    BAR();
    { const v2u mm = *(LAS v2u*)(L + L_MX + n * 8);
      const v4f s0 = *(LAS v4f*)(L + L_S + l * 16), s1 = *(LAS v4f*)(L + L_S + 1024 + l * 16);
      const float m = maxq(fmx(__uint_as_float(mm[0]), __uint_as_float(mm[1])));
      float ls = fmaf((s0[0] + s0[1]) + (s0[2] + s0[3]), ex2(__uint_as_float(mm[0]) - m), ((s1[0] + s1[1]) + (s1[2] + s1[3])) * ex2(__uint_as_float(mm[1]) - m));
      ls += dpp<X1>(ls); ls += dpp<X2>(ls); ls = fold16(ls, ls); ls = fold32(ls, ls);
      ST16((v4u{__float_as_uint(m), tag, __float_as_uint(ls), tag}), mlo, mls);
    }
    STAMP(6);
    v4u pv = LD16(pvo, pso);
    SB();
    BAR();
    {
      POLL(hg, DBAD, hvo, hso, 14);
      float hs = fold16(fold32(gval(hg[0]), gval(hg[2])), fold32(gval(hg[1]), gval(hg[3]))); hs += dpp<R8>(hs);
      const float hp_ = rb(rb(hs) + b1v);
      const float u2 = 1.5957691216057308f * fmaf(0.044715f * hp_ * hp_, hp_, hp_);
      const float ge = hp_ * rcp(1.f + ex2(-u2 * LOG2E));
      *(LAS u16*)(L + L_H + (l & 7) * 8 + gq * 2) = ((l & 7) < 7) ? (u16)pk(ge, ge) : (u16)0;
    }
    STAMP(7);
    {
      POLL(pv, TBAD, pvo, pso, 12);
      const float m = __uint_as_float(pv[0]);
      const float M = max16(maxb(maxq(m)));
      const float e = ex2(m - M);
      float ls = e * __uint_as_float(pv[2]);
      ls += dpp<X1>(ls); ls += dpp<X2>(ls); ls += dpp<HM>(ls); ls += dpp<R8>(ls); ls = fold16(ls, ls);
      *(LAS float*)(L + L_WG + (l & 31) * 4) = e * rcp(ls);
    }
    STAMP(8);
    PADTO(4, ".Lpad3");
    BAR();
    if (b < 64) {
      BAR();
      STAMP(9);
      float v = *(LAS float*)(L + L_S + l * 4) + *(LAS float*)(L + L_S + 256 + l * 4);
      v = fold16(v, v); v = fold32(v, v);
      const float f = rb(rb(dpp<R8>(v)) + b2v);
      const float gt = rb(f * rcp(1.f + ex2(-f * LOG2E)));
      const float o = xo + rb(rb(v) * gt);
      if (l < 8) out[ocol] = (u16)pk(o, o);
      if (b == 0 && l == 0) *(GAS unsigned*)ws = tag;
    }
    STAMP(10);
    FLUSH();
  } else {
    const int a = w - 1, a3 = a & 3;
    __builtin_amdgcn_s_setprio(2);
    EPOCH();
    int obo = (n & 3) == 0 ? (n >> 2) * 16384 + gq * 16 : OOB;
    int sbo = l < 32 ? (n >> 3) * 8192 + gq * 64 + (n & 7) * 8 : OOB;
    int obs = WS_OB + g * 65536 + (a3 >> 1) * 8192 + j * 256 + (a & 1) * 128, sbs = WS_SB + (kq * 16 + a * 2) * 8192 + (g * 8 + rb_) * 128;
    int sadr = L_S + (a & 1) * 1024 + (n >> 2) * 256 + (n & 3) * 64 + (a >> 1) * 16 + gq * 4, qadr = L_Q + (n & 3) * QSTR + gq * 64 + (n >> 2) * 16;
    int pso = WS_OB + gp * 65536 + rb_ * 8192 + a3 * 2048, pvo = a >= 4 ? l * 16 : OOB;
    int oadr = L_O + n * 128 + ((gq ^ (n >> 1)) & 7) * 16, owr = L_O + (a3 * 8 + gq) * 128 + ((((n >> 1) ^ ((a3 * 8 + gq) >> 1)) & 7) * 16) + (n & 1) * 8;
    int fso = WS_SB + b * 8192 + a * 1024, fadr = L_S + a * 64 + (l & 7) * 8 + (l >> 5) * 4, vwr = L_V + (a * 16 + gq) * VSTR + n * 16;
    int qso = WS_QG + g * 8192 + a3 * 512, qvo = a >= 4 ? (l >> 5) * 4096 + (l & 31) * 16 : OOB;
    int mxw = L_MX + (n & 3) * 32 + a * 4, qwr = L_Q + a3 * QSTR + (l & 31) * 8 + (l >> 5) * 4, kof = (n >> 2) * 256 + gq * 64 + (n & 3) * 16;
    asm volatile("" : "+v"(obo), "+v"(sbo), "+v"(sadr), "+v"(qadr), "+v"(oadr), "+v"(owr), "+v"(fadr), "+v"(vwr), "+v"(qvo), "+v"(mxw), "+v"(qwr), "+v"(kof), "+v"(pvo));
    asm volatile("" : "+s"(obs), "+s"(sbs), "+s"(pso), "+s"(fso), "+s"(qso));
    int wos = (gp * 512 + rb_ * 64 + a3 * 16) * 1024 + kq * 256, w2s = (gp * 224 + rb_ * 28 + a3 * 8) * 1024 + kq * 256, wvo = gq * 1024 + (n ^ (gq * 2)) * 16;
    int w2t = w2s + (a3 == 3 ? 0 : 4096);
    int wl0 = L_W + a3 * 4 * WBLK, wl1 = L_W + (16 + a3 * 2) * WBLK;
    v4u so_ = {(unsigned)(u64)wo, (unsigned)((u64)wo >> 32) & 0xFFFFu, 4096u * 512 * 2, 0x20000u}, s2_ = {(unsigned)(u64)w2, (unsigned)((u64)w2 >> 32) & 0xFFFFu, FF * 512u * 2, 0x20000u};
    asm volatile("" : "+s"(wos), "+s"(w2s), "+v"(wvo), "+s"(so_), "+s"(s2_), "+s"(wl0), "+s"(wl1), "+s"(w2t));
    const rsrc_t rk = mk(kc, 8u * SK * 256);
    SB();
    v4u kv[4], q0, q1;
#pragma unroll
    for (int i = 0; i < 4; ++i) {
      asm volatile("" : "+v"(vv[i]) : "v"(cpf));
      if (i == TRIG) {
#pragma unroll
        for (int k = 0; k < 4; ++k) kv[k] = ldw(rk, kof + k * 1024, kvo);
      }
      if (i == QTRIG) {
        q0 = __builtin_amdgcn_raw_buffer_load_b128(rs, qvo, qso, QAUX); q1 = __builtin_amdgcn_raw_buffer_load_b128(rs, qvo, qso + 2048, QAUX);
        STAMP(1);
      }
      *(LAS v4u*)(L + vwr + i * 4 * VSTR) = vv[i];
      SB();
    }
    STAMP(2);
    PADTO(5, ".Lpadret");
    if (QTRIG == 6) { while ((unsigned)__builtin_amdgcn_s_memrealtime() - tstart < QT) __builtin_amdgcn_s_sleep(1);
      q0 = __builtin_amdgcn_raw_buffer_load_b128(rs, qvo, qso, QAUX); q1 = __builtin_amdgcn_raw_buffer_load_b128(rs, qvo, qso + 2048, QAUX); STAMP(1); SB(); }
    asm volatile("" : "+v"(kv[KTRIG]));
    BAR();
    if (QTRIG == 4) { if (QSLEEP) __builtin_amdgcn_s_sleep(QSLEEP); q0 = __builtin_amdgcn_raw_buffer_load_b128(rs, qvo, qso, QAUX); q1 = __builtin_amdgcn_raw_buffer_load_b128(rs, qvo, qso + 2048, QAUX); STAMP(1); }
    v2u vt[16];
    if (a < 4) {
#pragma unroll
      for (int i = 0; i < 16; ++i) vt[i] = trd(L + L_V + (gq * 4 + (n >> 2)) * VSTR + a * 64 + (n & 3) * 8 + (i & 7) * 16 * VSTR + (i >> 3) * 32);
    }
    SB();
    if (QTRIG == 5) { asm volatile("" : "+v"(kv[0])); q0 = __builtin_amdgcn_raw_buffer_load_b128(rs, qvo, qso, QAUX); q1 = __builtin_amdgcn_raw_buffer_load_b128(rs, qvo, qso + 2048, QAUX); STAMP(1); }
    WAITV0();
    if (a < 4) {
      unsigned keep;
#define DMA1(SRD, SO) "buffer_load_dwordx4 %3, " SRD ", " SO " offen nt lds\n\ts_add_u32 m0, m0, %8\n\ts_add_u32 " SO ", " SO ", 0x1000\n\t"
      asm volatile("s_mov_b32 %0, m0\n\ts_mov_b32 m0, %6\n\ts_nop 0\n\t" DMA1("%4", "%1") DMA1("%4", "%1") DMA1("%4", "%1") DMA1("%4", "%1")
                   "s_mov_b32 m0, %7\n\ts_nop 0\n\t" DMA1("%5", "%2") "buffer_load_dwordx4 %3, %5, %9 offen nt lds\n\ts_mov_b32 m0, %0"
                   : "=&s"(keep), "+s"(wos), "+s"(w2s) : "v"(wvo), "s"(so_), "s"(s2_), "s"(wl0), "s"(wl1), "i"(WBLK), "s"(w2t) : "memory", "scc");
    } else {
#define QSUM() *(LAS unsigned*)(L + qwr) = pk(fold32(gval(q0[0]) + gval(q1[0]), gval(q0[2]) + gval(q1[2])), fold32(gval(q0[1]) + gval(q1[1]), gval(q0[3]) + gval(q1[3])))
      QSUM();
      SB();
      if (__builtin_expect(!__all((DBAD(q0) | DBAD(q1)) == 0), 0)) { POLL2(q0, q1, DBAD, qvo, qso, qso + 2048, 11); QSUM(); }
    }
    STAMP(4);
    CS(1);
    PADTO(6, ".Lpad5");
    BAR();
    CS(2);
    {
      const v4u qf = *(LAS v4u*)(L + qadr);
      v4f sc[4];
#pragma unroll
      for (int i = 0; i < 4; ++i) sc[i] = mfma(kv[i], qf, v4f{0.f, 0.f, 0.f, 0.f});
      SB();
      CS(3);
      float t0 = sc[0][0] + dpp<R12>(sc[0][1]);
      asm volatile("" : "+v"(t0)); t0 += dpp<R8>(sc[0][2]);
      float t1 = sc[1][1] + dpp<R4>(sc[1][0]);
      asm volatile("" : "+v"(t0), "+v"(t1)); t0 += dpp<R4>(sc[0][3]); t1 += dpp<R12>(sc[1][2]);
      float t2 = sc[2][2] + dpp<R8>(sc[2][0]);
      asm volatile("" : "+v"(t0), "+v"(t1), "+v"(t2)); t1 += dpp<R8>(sc[1][3]); t2 += dpp<R4>(sc[2][1]);
      float t3 = sc[3][3] + dpp<R12>(sc[3][0]);
      asm volatile("" : "+v"(t1), "+v"(t2), "+v"(t3)); t2 += dpp<R12>(sc[2][3]); t3 += dpp<R8>(sc[3][1]);
      t0 = LANES(0x00F000F000F000F0ull) ? t1 : t0;
      asm volatile("" : "+v"(t2), "+v"(t3)); t3 += dpp<R4>(sc[3][2]);
      t0 = LANES(0x0F000F000F000F00ull) ? t2 : t0;
      const float s1 = LANES(0xF000F000F000F000ull) ? t3 : t0;
      float s2 = rb(rb(s1) * 0.08838834764831845f) * LOG2E;
      asm volatile("" : "+v"(s2));
      CS(4);
      const float m = max32(max16(maxb(s2)));
      *(LAS float*)(L + mxw) = m;
      *(LAS float*)(L + sadr) = ex2(s2 - m);
    }
    STAMP(5);
    CS(5);
    BAR();
    CS(6);
    if (a < 4) {
      const v2u mm = *(LAS v2u*)(L + L_MX + n * 8);
      const v4f s0 = *(LAS v4f*)(L + L_S + l * 16), s1 = *(LAS v4f*)(L + L_S + 1024 + l * 16);
      const float m = maxq(fmx(__uint_as_float(mm[0]), __uint_as_float(mm[1])));
      const v4f p0 = s0 * ex2(__uint_as_float(mm[0]) - m), p1 = s1 * ex2(__uint_as_float(mm[1]) - m);
      const v4u pb = {pk(p0[0], p0[1]), pk(p0[2], p0[3]), pk(p1[0], p1[1]), pk(p1[2], p1[3])};
      CS(7);
      const v4f z = {0.f, 0.f, 0.f, 0.f};
      v4f d0 = mfma(cat(vt[0], vt[1]), pb, z), d1 = mfma(cat(vt[8], vt[9]), pb, z);
      const v4u pb1 = {dppu<0x55>(pb[0]), dppu<0x55>(pb[1]), dppu<0x55>(pb[2]), dppu<0x55>(pb[3])};
      d0 = mfma(cat(vt[2], vt[3]), pb1, d0); d1 = mfma(cat(vt[10], vt[11]), pb1, d1);
      const v4u pb2 = {dppu<0xAA>(pb[0]), dppu<0xAA>(pb[1]), dppu<0xAA>(pb[2]), dppu<0xAA>(pb[3])};
      d0 = mfma(cat(vt[4], vt[5]), pb2, d0); d1 = mfma(cat(vt[12], vt[13]), pb2, d1);
      const v4u pb3 = {dppu<0xFF>(pb[0]), dppu<0xFF>(pb[1]), dppu<0xFF>(pb[2]), dppu<0xFF>(pb[3])};
      d0 = mfma(cat(vt[6], vt[7]), pb3, d0); d1 = mfma(cat(vt[14], vt[15]), pb3, d1);
      ST16((v4u{pk(d0[0], d0[1]), tag, pk(d0[2], d0[3]), tag}), obo, obs);
      CS(8);
      ST16((v4u{pk(d1[0], d1[1]), tag, pk(d1[2], d1[3]), tag}), obo + 64, obs);
      CS(9);
      STAMP(6);
      asm volatile("s_waitcnt vmcnt(2)" ::: "memory");
    }
    SB();
    v4u pv = LD16(pvo, pso), pu = LD16(pvo, pso + 1024);
    PADTO(7, ".Lpad6");
    BAR();
    v2u wt[6];
#pragma unroll
    for (int i = 0; i < 6; ++i) wt[i] = trd(L + L_W + ((i >> 1) * 8 + gq * 2 + (i & 1)) * WBLK + (n >> 2) * 256 + (a ^ (n >> 2)) * 32 + (n & 3) * 8);
    SB();
    STAMP(7);
    if (a >= 4) {
      POLL2(pv, pu, TBAD, pvo, pso, pso + 1024, 12);
      STAMP(8);
      *(LAS v2u*)(L + owr) = v2u{pv[0], pv[2]}; *(LAS v2u*)(L + (owr ^ 32) + 512) = v2u{pu[0], pu[2]};
    }
    STAMP(9);
    BAR();
    { v4u of[4];
      of[0] = *(LAS v4u*)(L + oadr); of[2] = *(LAS v4u*)(L + oadr + 2048); of[1] = *(LAS v4u*)(L + (oadr ^ 64)); of[3] = *(LAS v4u*)(L + (oadr ^ 64) + 2048);
      const v4f w0 = *(LAS v4f*)(L + L_WG + gq * 16), w1_ = *(LAS v4f*)(L + L_WG + 64 + gq * 16);
      const v4u hf = *(LAS v4u*)(L + L_H + gq * 16);
      SB();
      const v4f z = {0.f, 0.f, 0.f, 0.f};
      v4f d0 = mfma(of[0], cat(wt[0], wt[1]), z), d1 = mfma(of[2], cat(wt[0], wt[1]), z);
      d0 = mfma(of[1], cat(wt[2], wt[3]), d0); d1 = mfma(of[3], cat(wt[2], wt[3]), d1);
      const v4f df = mfma(hf, cat(wt[4], wt[5]), z);
      float s = (fmaf(d0[1], w0[1], d0[0] * w0[0]) + fmaf(d0[3], w0[3], d0[2] * w0[2])) + (fmaf(d1[1], w1_[1], d1[0] * w1_[0]) + fmaf(d1[3], w1_[3], d1[2] * w1_[2]));
      s = fold32(s, s);
      const float e = fold16(s, 0.5f * df[0]);
      ST8((v2u{__float_as_uint(e), tag}), sbo, sbs);
    }
    STAMP(10);
    PADTO(8, ".Lpad7");
    if (b < 64) {
      if (DELAY3 && a == g) __builtin_amdgcn_s_sleep(DELAY3);
      v4u sv = LD16(l * 16, fso);
      POLL(sv, TBAD, l * 16, fso, 13);
      float v = fold32(__uint_as_float(sv[0]), __uint_as_float(sv[2]));
      v = fold16(v, v);
      v += dpp<R8>(v);
      *(LAS float*)(L + fadr) = v;
      BAR();
    }
    FLUSH();
  }
}

extern "C" void kernel_launch(void* const* d_in, const int* in_sizes, int n_in, void* d_out, int out_size, void* d_ws, size_t ws_size, hipStream_t stream) {
  fused_decode<<<256, NTHR, 0, stream>>>((const u16*)d_in[0], (const u16*)d_in[1], (const u16*)d_in[2], (const u16*)d_in[3], (const u16*)d_in[4], (const u16*)d_in[6],
                                         (const u16*)d_in[7], (const u16*)d_in[8], (const u16*)d_in[9], (u16*)d_out, (unsigned char*)d_ws);
}
```

```cpp
#include <hip/hip_runtime.h>
#include <hip/hip_bf16.h>

typedef unsigned short u16;
typedef unsigned long long u64;
typedef unsigned v4u __attribute__((ext_vector_type(4)));
typedef unsigned v2u __attribute__((ext_vector_type(2)));
typedef float v4f __attribute__((ext_vector_type(4)));
typedef short s16x4 __attribute__((ext_vector_type(4)));
typedef __bf16 bf16x2 __attribute__((ext_vector_type(2)));
typedef __bf16 bf16x8 __attribute__((ext_vector_type(8)));
typedef __amdgpu_buffer_rsrc_t rsrc_t;
#define GAS __attribute__((address_space(1)))
#define CAS __attribute__((address_space(4)))
#define LAS __attribute__((address_space(3)))
#define SB() __builtin_amdgcn_sched_barrier(0)
#define LD_AUX 2
#define SPRIO 3
#define TRIG 0
#define QTRIG 4
#define KTRIG 0
#define QSLEEP 0
#define DELAY3 14
#define XOFF 1
#define CODEPF 1
#define QAUX 0

constexpr int SK = 32768, WIN = 4096, FF = 1792;
constexpr int NTHR = 576;
constexpr unsigned WS_QG = 4096;
constexpr unsigned WS_HG = WS_QG + 65536;
constexpr unsigned WS_ML = WS_HG + 57344;
constexpr unsigned WS_OB = WS_ML + 16384;
constexpr unsigned WS_SB = WS_OB + 524288;
constexpr unsigned WS_END = WS_SB + 524288;
constexpr unsigned WS_PROF = 4u << 20;
constexpr int VSTR = 288, L_V = 0, L_O = 128 * VSTR, L_Q = L_O + 4096, QSTR = 320, L_S = L_Q + 4 * QSTR, L_XN = L_S + 2048, L_MX = L_XN + 1024, L_H = L_MX + 128,
              L_WG = L_H + 64, L_W = L_WG + 128, WBLK = 1088, L_END = L_W + 24 * WBLK;
static_assert(L_O % 128 == 0, "the o image is addressed with ^ 64");
constexpr unsigned SPIN_CAP = 1u << 18;
constexpr float LOG2E = 1.4426950408889634f;
constexpr int OOB = 0x40000000;

__device__ __forceinline__ rsrc_t mk(const void* p, unsigned bytes) { return __builtin_amdgcn_make_buffer_rsrc((void*)p, 0, bytes, 0x00020000); }
__device__ __forceinline__ v4u ldw(rsrc_t r, int voff, int soff) { return __builtin_amdgcn_raw_buffer_load_b128(r, voff, soff, LD_AUX); }
__device__ __forceinline__ float bflo(unsigned u) { return __uint_as_float(u << 16); }
__device__ __forceinline__ float bfhi(unsigned u) { return __uint_as_float(u & 0xFFFF0000u); }
__device__ __forceinline__ unsigned pk(float lo, float hi) { bf16x2 r = {(__bf16)lo, (__bf16)hi}; return __builtin_bit_cast(unsigned, r); }
__device__ __forceinline__ float rb(float f) { return bflo(pk(f, f)); }
__device__ __forceinline__ v4f mfma(v4u a, v4u b, v4f c) { return __builtin_amdgcn_mfma_f32_16x16x32_bf16(__builtin_bit_cast(bf16x8, a), __builtin_bit_cast(bf16x8, b), c, 0, 0, 0); }
template <int CTRL> __device__ __forceinline__ unsigned dppu(unsigned x) { return (unsigned)__builtin_amdgcn_mov_dpp((int)x, CTRL, 0xf, 0xf, true); }
template <int CTRL> __device__ __forceinline__ float dpp(float x) { return __uint_as_float(dppu<CTRL>(__float_as_uint(x))); }
constexpr int X1 = 0xB1, X2 = 0x4E, X3 = 0x1B, HM = 0x141, R4 = 0x124, R8 = 0x128, R12 = 0x12C;
__device__ __forceinline__ float sw16(float a, float b, float& o) { auto r = __builtin_amdgcn_permlane16_swap(__float_as_uint(a), __float_as_uint(b), false, false); o = __uint_as_float(r[1]); return __uint_as_float(r[0]); }
__device__ __forceinline__ float sw32(float a, float b, float& o) { auto r = __builtin_amdgcn_permlane32_swap(__float_as_uint(a), __float_as_uint(b), false, false); o = __uint_as_float(r[1]); return __uint_as_float(r[0]); }
__device__ __forceinline__ float fold16(float a, float b) { float o; float p = sw16(a, b, o); return p + o; }
__device__ __forceinline__ float fold32(float a, float b) { float o; float p = sw32(a, b, o); return p + o; }
__device__ __forceinline__ float fmx(float a, float b) { return __builtin_elementwise_maximum(a, b); }
__device__ __forceinline__ float max16(float a) { float o; float p = sw16(a, a, o); return fmx(p, o); }
__device__ __forceinline__ float max32(float a) { float o; float p = sw32(a, a, o); return fmx(p, o); }
__device__ __forceinline__ float maxq(float m) { return fmx(fmx(m, dpp<X1>(m)), fmx(dpp<X2>(m), dpp<X3>(m))); }
__device__ __forceinline__ float maxb(float m) { return fmx(fmx(m, dpp<R4>(m)), fmx(dpp<R8>(m), dpp<R12>(m))); }
__device__ __forceinline__ float sum64(float v) { v += dpp<X1>(v); v += dpp<X2>(v); v += dpp<HM>(v); v += dpp<R8>(v); v = fold16(v, v); return fold32(v, v); }
__device__ __forceinline__ v2u trd(LAS unsigned char* p) { return __builtin_bit_cast(v2u, __builtin_amdgcn_ds_read_tr16_b64_v4i16((LAS s16x4*)p)); }
__device__ __forceinline__ v4u cat(v2u a, v2u b) { return v4u{a[0], a[1], b[0], b[1]}; }
__device__ __forceinline__ float ex2(float x) { return __builtin_amdgcn_exp2f(x); }
__device__ __forceinline__ float rcp(float x) { return __builtin_amdgcn_rcpf(x); }
__device__ __forceinline__ float gval(unsigned d) { return __uint_as_float(d & 0xFFFFFF00u); }
__device__ __forceinline__ unsigned d4(float f, unsigned tagbyte) { return ((__float_as_uint(f) + 0x80u) & 0xFFFFFF00u) | tagbyte; }
#define LANES(mask) __builtin_amdgcn_inverse_ballot_w64(mask)
#define BAR() do { asm volatile("s_waitcnt lgkmcnt(0)" ::: "memory"); __builtin_amdgcn_s_barrier(); asm volatile("" ::: "memory"); } while (0)
#define FENCE() asm volatile("" ::: "memory")
#define WAITV0() __builtin_amdgcn_s_waitcnt(0x0F70)
#define LD16(off, soff) __builtin_amdgcn_raw_buffer_load_b128(rs, (off), (soff), 16)
#define ST16(v, off, soff) __builtin_amdgcn_raw_buffer_store_b128((v), rs, (off), (soff), 16)
#define ST8(v, off, soff) __builtin_amdgcn_raw_buffer_store_b64((v), rs, (off), (soff), 16)
#define TBAD(v) (((v)[1] ^ tag) | ((v)[3] ^ tag))
#define DBAD(v) ((__builtin_amdgcn_perm((v)[1], (v)[0], 0x0c0c0400u) | __builtin_amdgcn_perm((v)[3], (v)[2], 0x04000c0cu)) ^ tag)
#define EPOCH() asm volatile("" : "+s"(ep0)); const unsigned tag = ((ep0 & 0x7F7F7F7Fu) + 0x01010101u) ^ (ep0 & 0x80808080u)
#define STAMP0()
#define CS(i)
#define STAMP(i)
#define NOTE(i, v)
#define FLUSH()

__device__ __forceinline__ void mksel(unsigned (&sel)[8], int l) {
#pragma unroll
  for (int d = 0; d < 4; ++d) { const bool on = ((l & 7) >> 1) == d;
    sel[d] = on ? ((l & 1) ? 0x01000c0cu : 0x0c0c0100u) : 0x0c0c0c0cu; sel[4 + d] = on ? ((l & 1) ? 0x03020c0cu : 0x0c0c0302u) : 0x0c0c0c0cu; }
}
__device__ __forceinline__ v4u diag(unsigned pair, const unsigned (&sel)[8], int hi) {
  return v4u{__builtin_amdgcn_perm(0u, pair, sel[4 * hi]), __builtin_amdgcn_perm(0u, pair, sel[4 * hi + 1]), __builtin_amdgcn_perm(0u, pair, sel[4 * hi + 2]), __builtin_amdgcn_perm(0u, pair, sel[4 * hi + 3])};
}
__device__ __forceinline__ float upr8(float v) { return __builtin_bit_cast(float, __builtin_amdgcn_update_dpp(__builtin_bit_cast(int, v), __builtin_bit_cast(int, v), R8, 0xC, 0xF, false)); }
__device__ __forceinline__ v2u diag_out(v4f d, unsigned tb0, unsigned tb1) {
  const float t0 = upr8(d[0]), t1 = upr8(d[1]), t2 = upr8(d[2]), t3 = upr8(d[3]);
  return v2u{d4(fold32(t0, t2), tb0), d4(fold32(t1, t3), tb1)};
}

#define POLL(v, BAD, vo, so, note) do { unsigned spins_ = 0; while (!__all(BAD(v) == 0) && ++spins_ < SPIN_CAP) { FENCE(); v = LD16((vo), (so)); } NOTE(note, spins_); } while (0)
#define POLL2(v, u, BAD, vo, so, so2, note) do { unsigned spins_ = 0; while (!__all((BAD(v) | BAD(u)) == 0) && ++spins_ < SPIN_CAP) { FENCE(); v = LD16((vo), (so)); u = LD16((vo), (so2)); WAITV0(); } NOTE(note, spins_); } while (0)

__global__ void __launch_bounds__(NTHR, 3) fused_decode(const u16* __restrict__ x, const u16* __restrict__ kc, const u16* __restrict__ vc, const u16* __restrict__ wq,
                                                        const u16* __restrict__ wo, const u16* __restrict__ w1, const u16* __restrict__ b1, const u16* __restrict__ w2,
                                                        const u16* __restrict__ b2, u16* __restrict__ out, unsigned char* ws) {
  __shared__ __attribute__((aligned(128))) unsigned char lds_[L_END];
  LAS unsigned char* L = (LAS unsigned char*)lds_;
  const int b = blockIdx.x, g = b & 7, j = b >> 3, l = threadIdx.x & 63;
  int w = __builtin_amdgcn_readfirstlane(threadIdx.x >> 6);
  STAMP0();
  unsigned cpf = 0;
  { u64 pc; asm volatile("s_getpc_b64 %0" : "=s"(pc));
    cpf = *(const GAS unsigned*)((pc & ~127ull) + (((j * 9 + w) & 63) * 59 >> 6) * 128 + 128 + (l & 1) * 64); }
  asm volatile("" :: "s"(x), "s"(kc), "s"(vc), "s"(wq), "s"(wo), "s"(w1), "s"(b1), "s"(w2), "s"(b2), "s"(out), "s"(ws));
  unsigned ep0 = *(const CAS unsigned*)ws;
  const rsrc_t rs = mk(ws, WS_END);
  const int kq = j & 3, rb_ = j >> 2, gn = (g + XOFF) & 7, gp = (g - XOFF) & 7;
  const int gq = l >> 4, n = l & 15;
  const int kvo = (g * SK + (SK - WIN) + j * 128 + (w - 1) * 16) * 256;
  v4u vv[4];
  if (w != 0) {
    const rsrc_t rv = mk(vc, 8u * SK * 256);
    __builtin_amdgcn_s_barrier();
    SB();
#pragma unroll
    for (int i = 0; i < 4; ++i) vv[i] = ldw(rv, l * 16 + i * 1024, kvo);
    SB();
  }
  asm volatile("" : "+s"(w));
  if (__builtin_expect(w == 0, 1)) {
    v4u r16[16], f8[8];
    __builtin_amdgcn_s_setprio(3);
    const v4u xv = *(const GAS v4u*)(x + 8 * l);
    { const rsrc_t r = mk(wq, 512 * 4096 * 2), r1 = mk(w1, 512 * FF * 2);
      const int vo = (l >> 3) * (16 * 8192) + (l & 7) * 16, so = kq * (128 * 8192) + gn * 1024 + rb_ * 128;
      const int vo1 = j < 28 ? (l >> 3) * (8 * FF * 2) + (l & 7) * 16 : OOB, so1 = g * (64 * FF * 2) + j * 128;
#pragma unroll
      for (int i = 0; i < 24; ++i) {
        if (i < 16) r16[i] = ldw(r, vo, so + i * 8192); else f8[i - 16] = ldw(r1, vo1, so1 + (i - 16) * (FF * 2)); }
    }
    SB();
    __builtin_amdgcn_s_barrier();
    SB();
    STAMP(1);
    unsigned sel[8]; mksel(sel, l);
    int gvo = (l & 8) ? OOB : (l & 7) * 32 + (l & 16) + (l >> 5) * 8, xra = L_XN + kq * 256 + (l >> 3) * 32, xrc = L_XN + g * 128 + (l >> 3) * 16;
    asm volatile("" : "+v"(sel[0]), "+v"(sel[1]), "+v"(sel[2]), "+v"(sel[3]), "+v"(sel[4]), "+v"(sel[5]), "+v"(sel[6]), "+v"(sel[7]), "+v"(gvo), "+v"(xra), "+v"(xrc));
    SB();
    float xf[8];
#pragma unroll
    for (int i = 0; i < 4; ++i) { xf[2 * i] = bflo(xv[i]); xf[2 * i + 1] = bfhi(xv[i]); }
    const float mean = sum64(((xf[0] + xf[1]) + (xf[2] + xf[3])) + ((xf[4] + xf[5]) + (xf[6] + xf[7]))) * (1.f / 512);
    float vs = 0.f;
#pragma unroll
    for (int i = 0; i < 8; ++i) { const float d = xf[i] - mean; vs = fmaf(d, d, vs); }
    const float var = sum64(vs) * (1.f / 512);
    const float mb = rb(mean), rsd = rcp(rb(__builtin_amdgcn_sqrtf(rb(rb(var) + 1e-5f))));
    v4u xn;
#pragma unroll
    for (int i = 0; i < 4; ++i) { const unsigned d = pk(xf[2 * i] - mb, xf[2 * i + 1] - mb); xn[i] = pk(bflo(d) * rsd, bfhi(d) * rsd); }
    *(LAS v4u*)(L + L_XN + l * 16) = xn;
    const v4u xa = *(LAS v4u*)(L + xra), xb = *(LAS v4u*)(L + xra + 16), xc = *(LAS v4u*)(L + xrc);
    EPOCH();
    const unsigned tb0 = (tag >> ((l >> 5) * 16)) & 0xFFu, tb1 = (tag >> ((l >> 5) * 16 + 8)) & 0xFFu;
    { v4u bx[8];
#pragma unroll
      for (int i = 0; i < 8; ++i) bx[i] = diag(xa[i >> 1], sel, i & 1);
      SB();
      STAMP(2);
      v4f d = {0.f, 0.f, 0.f, 0.f};
#pragma unroll
      for (int i = 0; i < 8; ++i) { d = mfma(bx[i], r16[i], d); bx[i] = diag(xb[i >> 1], sel, i & 1); SB(); }
#pragma unroll
      for (int i = 0; i < 8; ++i) d = mfma(bx[i], r16[8 + i], d);
      asm volatile("" :: "v"(cpf));
      ST8(diag_out(d, tb0, tb1), gvo, WS_QG + gn * 8192 + kq * 2048 + rb_ * 256);
    }
    STAMP(3);
    { v4f d = {0.f, 0.f, 0.f, 0.f};
#pragma unroll
      for (int i = 0; i < 8; ++i) d = mfma(diag(xc[i >> 1], sel, i & 1), f8[i], d);
      ST8(diag_out(d, tb0, tb1), j < 28 ? gvo : OOB, WS_HG + g * (FF * 4) + j * 256); }
    const int hr0 = gp * 224 + rb_ * 28, hu = min(l & 7, 6), ocol = (b & 63) * 8 + (l & 7);
    int mlo = (l & 51) == 0 ? (n >> 2) * 512 : OOB, mls = WS_ML + g * 2048 + j * 16, hvo = (l >> 3) * (FF * 4) + hu * 16, hso = WS_HG + hr0 * 4;
    int pvo = (l & 31) * 16, pso = WS_ML + gp * 2048 + (rb_ >> 1) * 512;
    asm volatile("" : "+v"(mlo), "+s"(mls), "+v"(hvo), "+s"(hso), "+v"(pvo), "+s"(pso));
    __builtin_amdgcn_s_setprio(SPRIO);
    STAMP(4);
    BAR();
    BAR();
    const float b1v = bflo(b1[hr0 + hu * 4 + gq]), b2v = bflo(b2[ocol]), xo = bflo(x[ocol]);
    v4u hg = LD16(hvo, hso);
    SB();
    BAR();
    { const v2u mm = *(LAS v2u*)(L + L_MX + n * 8);
      const v4f s0 = *(LAS v4f*)(L + L_S + l * 16), s1 = *(LAS v4f*)(L + L_S + 1024 + l * 16);
      const float m = maxq(fmx(__uint_as_float(mm[0]), __uint_as_float(mm[1])));
      float ls = fmaf((s0[0] + s0[1]) + (s0[2] + s0[3]), ex2(__uint_as_float(mm[0]) - m), ((s1[0] + s1[1]) + (s1[2] + s1[3])) * ex2(__uint_as_float(mm[1]) - m));
      ls += dpp<X1>(ls); ls += dpp<X2>(ls); ls = fold16(ls, ls); ls = fold32(ls, ls);
      ST16((v4u{__float_as_uint(m), tag, __float_as_uint(ls), tag}), mlo, mls);
    }
    STAMP(6);
    v4u pv = LD16(pvo, pso);
    SB();
    BAR();
    {
      POLL(hg, DBAD, hvo, hso, 14);
      float hs = fold16(fold32(gval(hg[0]), gval(hg[2])), fold32(gval(hg[1]), gval(hg[3]))); hs += dpp<R8>(hs);
      const float hp_ = rb(rb(hs) + b1v);
      const float u2 = 1.5957691216057308f * fmaf(0.044715f * hp_ * hp_, hp_, hp_);
      const float ge = hp_ * rcp(1.f + ex2(-u2 * LOG2E));
      *(LAS u16*)(L + L_H + (l & 7) * 8 + gq * 2) = ((l & 7) < 7) ? (u16)pk(ge, ge) : (u16)0;
    }
    STAMP(7);
    {
      POLL(pv, TBAD, pvo, pso, 12);
      const float m = __uint_as_float(pv[0]);
      const float M = max16(maxb(maxq(m)));
      const float e = ex2(m - M);
      float ls = e * __uint_as_float(pv[2]);
      ls += dpp<X1>(ls); ls += dpp<X2>(ls); ls += dpp<HM>(ls); ls += dpp<R8>(ls); ls = fold16(ls, ls);
      *(LAS float*)(L + L_WG + (l & 31) * 4) = e * rcp(ls);
    }
    STAMP(8);
    BAR();
    if (b < 64) {
      BAR();
      STAMP(9);
      float v = *(LAS float*)(L + L_S + l * 4) + *(LAS float*)(L + L_S + 256 + l * 4);
      v = fold16(v, v); v = fold32(v, v);
      const float f = rb(rb(dpp<R8>(v)) + b2v);
      const float gt = rb(f * rcp(1.f + ex2(-f * LOG2E)));
      const float o = xo + rb(rb(v) * gt);
      if (l < 8) out[ocol] = (u16)pk(o, o);
      if (b == 0 && l == 0) *(GAS unsigned*)ws = tag;
    }
    STAMP(10);
    FLUSH();
  } else {
    const int a = w - 1, a3 = a & 3;
    __builtin_amdgcn_s_setprio(2);
    EPOCH();
    int obo = (n & 3) == 0 ? (n >> 2) * 16384 + gq * 16 : OOB;
    int sbo = l < 32 ? (n >> 3) * 8192 + gq * 64 + (n & 7) * 8 : OOB;
    int obs = WS_OB + g * 65536 + (a3 >> 1) * 8192 + j * 256 + (a & 1) * 128, sbs = WS_SB + (kq * 16 + a * 2) * 8192 + (g * 8 + rb_) * 128;
    int sadr = L_S + (a & 1) * 1024 + (n >> 2) * 256 + (n & 3) * 64 + (a >> 1) * 16 + gq * 4, qadr = L_Q + (n & 3) * QSTR + gq * 64 + (n >> 2) * 16;
    int pso = WS_OB + gp * 65536 + rb_ * 8192 + a3 * 2048, pvo = a >= 4 ? l * 16 : OOB;
    int oadr = L_O + n * 128 + ((gq ^ (n >> 1)) & 7) * 16, owr = L_O + (a3 * 8 + gq) * 128 + ((((n >> 1) ^ ((a3 * 8 + gq) >> 1)) & 7) * 16) + (n & 1) * 8;
    int fso = WS_SB + b * 8192 + a * 1024, fadr = L_S + a * 64 + (l & 7) * 8 + (l >> 5) * 4, vwr = L_V + (a * 16 + gq) * VSTR + n * 16;
    int qso = WS_QG + g * 8192 + a3 * 512, qvo = a >= 4 ? (l >> 5) * 4096 + (l & 31) * 16 : OOB;
    int mxw = L_MX + (n & 3) * 32 + a * 4, qwr = L_Q + a3 * QSTR + (l & 31) * 8 + (l >> 5) * 4, kof = (n >> 2) * 256 + gq * 64 + (n & 3) * 16;
    asm volatile("" : "+v"(obo), "+v"(sbo), "+v"(sadr), "+v"(qadr), "+v"(oadr), "+v"(owr), "+v"(fadr), "+v"(vwr), "+v"(qvo), "+v"(mxw), "+v"(qwr), "+v"(kof), "+v"(pvo));
    asm volatile("" : "+s"(obs), "+s"(sbs), "+s"(pso), "+s"(fso), "+s"(qso));
    int wos = (gp * 512 + rb_ * 64 + a3 * 16) * 1024 + kq * 256, w2s = (gp * 224 + rb_ * 28 + a3 * 8) * 1024 + kq * 256, wvo = gq * 1024 + (n ^ (gq * 2)) * 16;
    int w2t = w2s + (a3 == 3 ? 0 : 4096);
    int wl0 = L_W + a3 * 4 * WBLK, wl1 = L_W + (16 + a3 * 2) * WBLK;
    v4u so_ = {(unsigned)(u64)wo, (unsigned)((u64)wo >> 32) & 0xFFFFu, 4096u * 512 * 2, 0x20000u}, s2_ = {(unsigned)(u64)w2, (unsigned)((u64)w2 >> 32) & 0xFFFFu, FF * 512u * 2, 0x20000u};
    asm volatile("" : "+s"(wos), "+s"(w2s), "+v"(wvo), "+s"(so_), "+s"(s2_), "+s"(wl0), "+s"(wl1), "+s"(w2t));
    const rsrc_t rk = mk(kc, 8u * SK * 256);
    SB();
    v4u kv[4], q0, q1;
#pragma unroll
    for (int i = 0; i < 4; ++i) {
      asm volatile("" : "+v"(vv[i]) : "v"(cpf));
      if (i == TRIG) {
#pragma unroll
        for (int k = 0; k < 4; ++k) kv[k] = ldw(rk, kof + k * 1024, kvo);
      }
      if (i == QTRIG) {
        q0 = __builtin_amdgcn_raw_buffer_load_b128(rs, qvo, qso, QAUX); q1 = __builtin_amdgcn_raw_buffer_load_b128(rs, qvo, qso + 2048, QAUX);
        STAMP(1);
      }
      *(LAS v4u*)(L + vwr + i * 4 * VSTR) = vv[i];
      SB();
    }
    STAMP(2);
    asm volatile("" : "+v"(kv[KTRIG]));
    BAR();
    if (QTRIG == 4) { if (QSLEEP) __builtin_amdgcn_s_sleep(QSLEEP); q0 = __builtin_amdgcn_raw_buffer_load_b128(rs, qvo, qso, QAUX); q1 = __builtin_amdgcn_raw_buffer_load_b128(rs, qvo, qso + 2048, QAUX); STAMP(1); }
    v2u vt[16];
    if (a < 4) {
#pragma unroll
      for (int i = 0; i < 16; ++i) vt[i] = trd(L + L_V + (gq * 4 + (n >> 2)) * VSTR + a * 64 + (n & 3) * 8 + (i & 7) * 16 * VSTR + (i >> 3) * 32);
    }
    SB();
    if (QTRIG == 5) { asm volatile("" : "+v"(kv[0])); q0 = __builtin_amdgcn_raw_buffer_load_b128(rs, qvo, qso, QAUX); q1 = __builtin_amdgcn_raw_buffer_load_b128(rs, qvo, qso + 2048, QAUX); STAMP(1); }
    WAITV0();
    if (a < 4) {
      unsigned keep;
#define DMA1(SRD, SO) "buffer_load_dwordx4 %3, " SRD ", " SO " offen nt lds\n\ts_add_u32 m0, m0, %8\n\ts_add_u32 " SO ", " SO ", 0x1000\n\t"
      asm volatile("s_mov_b32 %0, m0\n\ts_mov_b32 m0, %6\n\ts_nop 0\n\t" DMA1("%4", "%1") DMA1("%4", "%1") DMA1("%4", "%1") DMA1("%4", "%1")
                   "s_mov_b32 m0, %7\n\ts_nop 0\n\t" DMA1("%5", "%2") "buffer_load_dwordx4 %3, %5, %9 offen nt lds\n\ts_mov_b32 m0, %0"
                   : "=&s"(keep), "+s"(wos), "+s"(w2s) : "v"(wvo), "s"(so_), "s"(s2_), "s"(wl0), "s"(wl1), "i"(WBLK), "s"(w2t) : "memory", "scc");
    } else {
#define QSUM() *(LAS unsigned*)(L + qwr) = pk(fold32(gval(q0[0]) + gval(q1[0]), gval(q0[2]) + gval(q1[2])), fold32(gval(q0[1]) + gval(q1[1]), gval(q0[3]) + gval(q1[3])))
      QSUM();
      SB();
      if (__builtin_expect(!__all((DBAD(q0) | DBAD(q1)) == 0), 0)) { POLL2(q0, q1, DBAD, qvo, qso, qso + 2048, 11); QSUM(); }
    }
    STAMP(4);
    CS(1);
    BAR();
    CS(2);
    {
      const v4u qf = *(LAS v4u*)(L + qadr);
      v4f sc[4];
#pragma unroll
      for (int i = 0; i < 4; ++i) sc[i] = mfma(kv[i], qf, v4f{0.f, 0.f, 0.f, 0.f});
      SB();
      CS(3);
      float t0 = sc[0][0] + dpp<R12>(sc[0][1]);
      asm volatile("" : "+v"(t0)); t0 += dpp<R8>(sc[0][2]);
      float t1 = sc[1][1] + dpp<R4>(sc[1][0]);
      asm volatile("" : "+v"(t0), "+v"(t1)); t0 += dpp<R4>(sc[0][3]); t1 += dpp<R12>(sc[1][2]);
      float t2 = sc[2][2] + dpp<R8>(sc[2][0]);
      asm volatile("" : "+v"(t0), "+v"(t1), "+v"(t2)); t1 += dpp<R8>(sc[1][3]); t2 += dpp<R4>(sc[2][1]);
      float t3 = sc[3][3] + dpp<R12>(sc[3][0]);
      asm volatile("" : "+v"(t1), "+v"(t2), "+v"(t3)); t2 += dpp<R12>(sc[2][3]); t3 += dpp<R8>(sc[3][1]);
      t0 = LANES(0x00F000F000F000F0ull) ? t1 : t0;
      asm volatile("" : "+v"(t2), "+v"(t3)); t3 += dpp<R4>(sc[3][2]);
      t0 = LANES(0x0F000F000F000F00ull) ? t2 : t0;
      const float s1 = LANES(0xF000F000F000F000ull) ? t3 : t0;
      float s2 = rb(rb(s1) * 0.08838834764831845f) * LOG2E;
      asm volatile("" : "+v"(s2));
      CS(4);
      const float m = max32(max16(maxb(s2)));
      *(LAS float*)(L + mxw) = m;
      *(LAS float*)(L + sadr) = ex2(s2 - m);
    }
    STAMP(5);
    CS(5);
    BAR();
    CS(6);
    if (a < 4) {
      const v2u mm = *(LAS v2u*)(L + L_MX + n * 8);
      const v4f s0 = *(LAS v4f*)(L + L_S + l * 16), s1 = *(LAS v4f*)(L + L_S + 1024 + l * 16);
      const float m = maxq(fmx(__uint_as_float(mm[0]), __uint_as_float(mm[1])));
      const v4f p0 = s0 * ex2(__uint_as_float(mm[0]) - m), p1 = s1 * ex2(__uint_as_float(mm[1]) - m);
      const v4u pb = {pk(p0[0], p0[1]), pk(p0[2], p0[3]), pk(p1[0], p1[1]), pk(p1[2], p1[3])};
      CS(7);
      const v4f z = {0.f, 0.f, 0.f, 0.f};
      v4f d0 = mfma(cat(vt[0], vt[1]), pb, z), d1 = mfma(cat(vt[8], vt[9]), pb, z);
      const v4u pb1 = {dppu<0x55>(pb[0]), dppu<0x55>(pb[1]), dppu<0x55>(pb[2]), dppu<0x55>(pb[3])};
      d0 = mfma(cat(vt[2], vt[3]), pb1, d0); d1 = mfma(cat(vt[10], vt[11]), pb1, d1);
      const v4u pb2 = {dppu<0xAA>(pb[0]), dppu<0xAA>(pb[1]), dppu<0xAA>(pb[2]), dppu<0xAA>(pb[3])};
      d0 = mfma(cat(vt[4], vt[5]), pb2, d0); d1 = mfma(cat(vt[12], vt[13]), pb2, d1);
      const v4u pb3 = {dppu<0xFF>(pb[0]), dppu<0xFF>(pb[1]), dppu<0xFF>(pb[2]), dppu<0xFF>(pb[3])};
      d0 = mfma(cat(vt[6], vt[7]), pb3, d0); d1 = mfma(cat(vt[14], vt[15]), pb3, d1);
      ST16((v4u{pk(d0[0], d0[1]), tag, pk(d0[2], d0[3]), tag}), obo, obs);
      CS(8);
      ST16((v4u{pk(d1[0], d1[1]), tag, pk(d1[2], d1[3]), tag}), obo + 64, obs);
      CS(9);
      STAMP(6);
      asm volatile("s_waitcnt vmcnt(2)" ::: "memory");
    }
    SB();
    v4u pv = LD16(pvo, pso), pu = LD16(pvo, pso + 1024);
    SB();
    BAR();
    v2u wt[6];
#pragma unroll
    for (int i = 0; i < 6; ++i) wt[i] = trd(L + L_W + ((i >> 1) * 8 + gq * 2 + (i & 1)) * WBLK + (n >> 2) * 256 + (a ^ (n >> 2)) * 32 + (n & 3) * 8);
    SB();
    STAMP(7);
    if (a >= 4) {
      POLL2(pv, pu, TBAD, pvo, pso, pso + 1024, 12);
      STAMP(8);
      *(LAS v2u*)(L + owr) = v2u{pv[0], pv[2]}; *(LAS v2u*)(L + (owr ^ 32) + 512) = v2u{pu[0], pu[2]};
    }
    STAMP(9);
    BAR();
    { v4u of[4];
      of[0] = *(LAS v4u*)(L + oadr); of[2] = *(LAS v4u*)(L + oadr + 2048); of[1] = *(LAS v4u*)(L + (oadr ^ 64)); of[3] = *(LAS v4u*)(L + (oadr ^ 64) + 2048);
      const v4f w0 = *(LAS v4f*)(L + L_WG + gq * 16), w1_ = *(LAS v4f*)(L + L_WG + 64 + gq * 16);
      const v4u hf = *(LAS v4u*)(L + L_H + gq * 16);
      SB();
      const v4f z = {0.f, 0.f, 0.f, 0.f};
      v4f d0 = mfma(of[0], cat(wt[0], wt[1]), z), d1 = mfma(of[2], cat(wt[0], wt[1]), z);
      d0 = mfma(of[1], cat(wt[2], wt[3]), d0); d1 = mfma(of[3], cat(wt[2], wt[3]), d1);
      const v4f df = mfma(hf, cat(wt[4], wt[5]), z);
      float s = (fmaf(d0[1], w0[1], d0[0] * w0[0]) + fmaf(d0[3], w0[3], d0[2] * w0[2])) + (fmaf(d1[1], w1_[1], d1[0] * w1_[0]) + fmaf(d1[3], w1_[3], d1[2] * w1_[2]));
      s = fold32(s, s);
      const float e = fold16(s, 0.5f * df[0]);
      ST8((v2u{__float_as_uint(e), tag}), sbo, sbs);
    }
    STAMP(10);
    if (b < 64) {
      if (DELAY3 && a == g) __builtin_amdgcn_s_sleep(DELAY3);
      v4u sv = LD16(l * 16, fso);
      POLL(sv, TBAD, l * 16, fso, 13);
      float v = fold32(__uint_as_float(sv[0]), __uint_as_float(sv[2]));
      v = fold16(v, v);
      v += dpp<R8>(v);
      *(LAS float*)(L + fadr) = v;
      BAR();
    }
    FLUSH();
  }
}

extern "C" void kernel_launch(void* const* d_in, const int* in_sizes, int n_in, void* d_out, int out_size, void* d_ws, size_t ws_size, hipStream_t stream) {
  fused_decode<<<256, NTHR, 0, stream>>>((const u16*)d_in[0], (const u16*)d_in[1], (const u16*)d_in[2], (const u16*)d_in[3], (const u16*)d_in[4], (const u16*)d_in[6],
                                         (const u16*)d_in[7], (const u16*)d_in[8], (const u16*)d_in[9], (u16*)d_out, (unsigned char*)d_ws);
}
```

```cpp
#include <hip/hip_runtime.h>
#include <hip/hip_bf16.h>

typedef unsigned short u16;
typedef unsigned long long u64;
typedef unsigned v4u __attribute__((ext_vector_type(4)));
typedef unsigned v2u __attribute__((ext_vector_type(2)));
typedef float v4f __attribute__((ext_vector_type(4)));
typedef short s16x4 __attribute__((ext_vector_type(4)));
typedef __bf16 bf16x2 __attribute__((ext_vector_type(2)));
typedef __bf16 bf16x8 __attribute__((ext_vector_type(8)));
typedef __amdgpu_buffer_rsrc_t rsrc_t;
#define GAS __attribute__((address_space(1)))
#define CAS __attribute__((address_space(4)))
#define LAS __attribute__((address_space(3)))
#define SB() __builtin_amdgcn_sched_barrier(0)
#define LD_AUX 2
#define DMAPOL "nt"
#define WSOFF 0
#define SPR 0
#define KOLD 3
#define W1SK 1
#define PFPOS 0
#define PFN 4
#define SPRIO 3
#define TRIG 0
#define QTRIG 4
#define KTRIG 0
#define QT 250
#define PFLINES 58
#define P2I 3
#define HS1 5
#define HS2 12
#define QSLEEP 0
#define DELAY3 14
#define XOFF 1
#define CODEPF 1
#define QAUX 0

constexpr int SK = 32768, WIN = 4096, FF = 1792;
constexpr int NTHR = 576;
constexpr unsigned WS_QG = 4096;
constexpr unsigned WS_HG = WS_QG + 65536;
constexpr unsigned WS_ML = WS_HG + 57344;
constexpr unsigned WS_OB = WS_ML + 16384;
constexpr unsigned WS_SB = WS_OB + 524288;
constexpr unsigned WS_END = WS_SB + 524288;
constexpr unsigned WS_PROF = 4u << 20;
constexpr int VSTR = 288, L_V = 0, L_O = 128 * VSTR, L_Q = L_O + 4096, QSTR = 320, L_S = L_Q + 4 * QSTR, L_XN = L_S + 2048, L_MX = L_XN + 1024, L_H = L_MX + 128,
              L_WG = L_H + 64, L_W = L_WG + 128, WBLK = 1088, L_END = L_W + 24 * WBLK;
static_assert(L_O % 128 == 0, "the o image is addressed with ^ 64");
constexpr unsigned SPIN_CAP = 1u << 18;
constexpr float LOG2E = 1.4426950408889634f;
constexpr int OOB = 0x40000000;

__device__ __forceinline__ rsrc_t mk(const void* p, unsigned bytes) { return __builtin_amdgcn_make_buffer_rsrc((void*)p, 0, bytes, 0x00020000); }
__device__ __forceinline__ v4u ldw(rsrc_t r, int voff, int soff) { return __builtin_amdgcn_raw_buffer_load_b128(r, voff, soff, LD_AUX); }
__device__ __forceinline__ float bflo(unsigned u) { return __uint_as_float(u << 16); }
__device__ __forceinline__ float bfhi(unsigned u) { return __uint_as_float(u & 0xFFFF0000u); }
__device__ __forceinline__ unsigned pk(float lo, float hi) { bf16x2 r = {(__bf16)lo, (__bf16)hi}; return __builtin_bit_cast(unsigned, r); }
__device__ __forceinline__ float rb(float f) { return bflo(pk(f, f)); }
__device__ __forceinline__ v4f mfma(v4u a, v4u b, v4f c) { return __builtin_amdgcn_mfma_f32_16x16x32_bf16(__builtin_bit_cast(bf16x8, a), __builtin_bit_cast(bf16x8, b), c, 0, 0, 0); }
template <int CTRL> __device__ __forceinline__ unsigned dppu(unsigned x) { return (unsigned)__builtin_amdgcn_mov_dpp((int)x, CTRL, 0xf, 0xf, true); }
template <int CTRL> __device__ __forceinline__ float dpp(float x) { return __uint_as_float(dppu<CTRL>(__float_as_uint(x))); }
constexpr int X1 = 0xB1, X2 = 0x4E, X3 = 0x1B, HM = 0x141, R4 = 0x124, R8 = 0x128, R12 = 0x12C;
__device__ __forceinline__ float sw16(float a, float b, float& o) { auto r = __builtin_amdgcn_permlane16_swap(__float_as_uint(a), __float_as_uint(b), false, false); o = __uint_as_float(r[1]); return __uint_as_float(r[0]); }
__device__ __forceinline__ float sw32(float a, float b, float& o) { auto r = __builtin_amdgcn_permlane32_swap(__float_as_uint(a), __float_as_uint(b), false, false); o = __uint_as_float(r[1]); return __uint_as_float(r[0]); }
__device__ __forceinline__ float fold16(float a, float b) { float o; float p = sw16(a, b, o); return p + o; }
__device__ __forceinline__ float fold32(float a, float b) { float o; float p = sw32(a, b, o); return p + o; }
__device__ __forceinline__ float fmx(float a, float b) { return __builtin_elementwise_maximum(a, b); }
__device__ __forceinline__ float max16(float a) { float o; float p = sw16(a, a, o); return fmx(p, o); }
__device__ __forceinline__ float max32(float a) { float o; float p = sw32(a, a, o); return fmx(p, o); }
__device__ __forceinline__ float maxq(float m) { return fmx(fmx(m, dpp<X1>(m)), fmx(dpp<X2>(m), dpp<X3>(m))); }
__device__ __forceinline__ float maxb(float m) { return fmx(fmx(m, dpp<R4>(m)), fmx(dpp<R8>(m), dpp<R12>(m))); }
__device__ __forceinline__ float sum64(float v) { v += dpp<X1>(v); v += dpp<X2>(v); v += dpp<HM>(v); v += dpp<R8>(v); v = fold16(v, v); return fold32(v, v); }
__device__ __forceinline__ v2u trd(LAS unsigned char* p) { return __builtin_bit_cast(v2u, __builtin_amdgcn_ds_read_tr16_b64_v4i16((LAS s16x4*)p)); }
__device__ __forceinline__ v4u cat(v2u a, v2u b) { return v4u{a[0], a[1], b[0], b[1]}; }
__device__ __forceinline__ float ex2(float x) { return __builtin_amdgcn_exp2f(x); }
__device__ __forceinline__ float rcp(float x) { return __builtin_amdgcn_rcpf(x); }
__device__ __forceinline__ float gval(unsigned d) { return __uint_as_float(d & 0xFFFFFF00u); }
__device__ __forceinline__ unsigned d4(float f, unsigned tagbyte) { return ((__float_as_uint(f) + 0x80u) & 0xFFFFFF00u) | tagbyte; }
#define LANES(mask) __builtin_amdgcn_inverse_ballot_w64(mask)
#define BAR() do { asm volatile("s_waitcnt lgkmcnt(0)" ::: "memory"); __builtin_amdgcn_s_barrier(); asm volatile("" ::: "memory"); } while (0)
#define FENCE() asm volatile("" ::: "memory")
#define WAITV0() __builtin_amdgcn_s_waitcnt(0x0F70)
#define LD16(off, soff) __builtin_amdgcn_raw_buffer_load_b128(rs, (off), (soff), 16)
#define ST16(v, off, soff) __builtin_amdgcn_raw_buffer_store_b128((v), rs, (off), (soff), 16)
#define ST8(v, off, soff) __builtin_amdgcn_raw_buffer_store_b64((v), rs, (off), (soff), 16)
#define TBAD(v) (((v)[1] ^ tag) | ((v)[3] ^ tag))
#define DBAD(v) ((__builtin_amdgcn_perm((v)[1], (v)[0], 0x0c0c0400u) | __builtin_amdgcn_perm((v)[3], (v)[2], 0x04000c0cu)) ^ tag)
#define EPOCH() asm volatile("" : "+s"(ep0)); const unsigned tag = ((ep0 & 0x7F7F7F7Fu) + 0x01010101u) ^ (ep0 & 0x80808080u)
#define STAMP0()
#define CT(i)
#define CS(i)
#define STAMP(i)
#define NOTE(i, v)
#define FLUSH()

__device__ __forceinline__ void mksel(unsigned (&sel)[8], int l) {
#pragma unroll
  for (int d = 0; d < 4; ++d) { const bool on = ((l & 7) >> 1) == d;
    sel[d] = on ? ((l & 1) ? 0x01000c0cu : 0x0c0c0100u) : 0x0c0c0c0cu; sel[4 + d] = on ? ((l & 1) ? 0x03020c0cu : 0x0c0c0302u) : 0x0c0c0c0cu; }
}
__device__ __forceinline__ v4u diag(unsigned pair, const unsigned (&sel)[8], int hi) {
  return v4u{__builtin_amdgcn_perm(0u, pair, sel[4 * hi]), __builtin_amdgcn_perm(0u, pair, sel[4 * hi + 1]), __builtin_amdgcn_perm(0u, pair, sel[4 * hi + 2]), __builtin_amdgcn_perm(0u, pair, sel[4 * hi + 3])};
}
__device__ __forceinline__ float upr8(float v) { return __builtin_bit_cast(float, __builtin_amdgcn_update_dpp(__builtin_bit_cast(int, v), __builtin_bit_cast(int, v), R8, 0xC, 0xF, false)); }
__device__ __forceinline__ v2u diag_out(v4f d, unsigned tb0, unsigned tb1) {
  const float t0 = upr8(d[0]), t1 = upr8(d[1]), t2 = upr8(d[2]), t3 = upr8(d[3]);
  return v2u{d4(fold32(t0, t2), tb0), d4(fold32(t1, t3), tb1)};
}

#define POLL(v, BAD, vo, so, note) do { unsigned spins_ = 0; _Pragma("nounroll") while (!__all(BAD(v) == 0) && ++spins_ < SPIN_CAP) { FENCE(); v = LD16((vo), (so)); } NOTE(note, spins_); } while (0)
#define POLL2(v, u, BAD, vo, so, so2, note) do { unsigned spins_ = 0; _Pragma("nounroll") while (!__all((BAD(v) | BAD(u)) == 0) && ++spins_ < SPIN_CAP) { FENCE(); v = LD16((vo), (so)); u = LD16((vo), (so2)); WAITV0(); } NOTE(note, spins_); } while (0)

__global__ void __launch_bounds__(NTHR, 3) fused_decode(const u16* __restrict__ x, const u16* __restrict__ kc, const u16* __restrict__ vc, const u16* __restrict__ wq,
                                                        const u16* __restrict__ wo, const u16* __restrict__ w1, const u16* __restrict__ b1, const u16* __restrict__ w2,
                                                        const u16* __restrict__ b2, u16* __restrict__ out, unsigned char* ws) {
  __shared__ __attribute__((aligned(128))) unsigned char lds_[L_END];
  LAS unsigned char* L = (LAS unsigned char*)lds_;
  const int b = blockIdx.x, g = b & 7, j = b >> 3, l = threadIdx.x & 63;
  int w = __builtin_amdgcn_readfirstlane(threadIdx.x >> 6);
  STAMP0();
  unsigned cpf = 0;
#define L2PF() { u64 pc; \
    asm volatile("s_cmp_lg_u32 %2, 4\n\ts_cbranch_scc1 1f\n\ts_getpc_b64 %0\n\tv_min_u32 %1, %4, %3\n\tv_lshlrev_b32 %1, 7, %1\n\tglobal_load_dword %1, %1, %0\n1:" \
                 : "=&s"(pc), "=&v"(cpf) : "s"(w), "v"(PFN == 64 ? l : (j * PFN + (l & (PFN - 1))) & 63), "i"(PFLINES) : "scc", "memory"); }
  if (PFPOS == 0) L2PF();
#define PADTO(k, nxt) do { SB(); asm volatile(".Lpad" #k ":\n\ts_cbranch_execz " nxt); SB(); } while (0)
#define HOPS() asm volatile("s_sleep %1\n\ts_cmp_lg_u32 %0, 8\n\ts_cbranch_scc1 1f\n\ts_mov_b64 exec, 0\n\ts_branch .Lpad4\n1:\ts_sleep %2\n.Lpadret:\n\ts_mov_b64 exec, -1" :: "s"(w), "i"(HS1), "i"(HS2) : "scc")
  const int kq = j & 3, rb_ = j >> 2, gn = (g + XOFF) & 7, gp = (g - XOFF) & 7;
  const int gq = l >> 4, n = l & 15;
  const bool hasw1 = rb_ != 3; const int jw = rb_ == 7 ? j - 16 : j;
  int kvo = (g * SK + (SK - WIN) + j * 128 + (w - 1) * 16) * 256, l16 = l * 16;
  int vo = (l >> 3) * (16 * 8192) + (l & 7) * 16, so = kq * (128 * 8192) + gn * 1024 + rb_ * 128;
  int vo1 = hasw1 ? (l >> 3) * (8 * FF * 2) + (l & 7) * 16 : OOB, so1 = g * (64 * FF * 2) + jw * 128;
  asm volatile("" : "+s"(kvo), "+s"(so), "+s"(so1));
  asm volatile("s_setprio 3" : "+v"(l16), "+v"(vo), "+v"(vo1));
  asm volatile("" :: "s"(x), "s"(kc), "s"(vc), "s"(wq), "s"(wo), "s"(w1), "s"(b1), "s"(w2), "s"(b2), "s"(out), "s"(ws));
  unsigned ep0 = *(const CAS unsigned*)ws;
  const rsrc_t rs = mk(ws, WS_END);
  v4u vv[4];
  if (w != 0) {
    const rsrc_t rv = mk(vc, 8u * SK * 256);
    __builtin_amdgcn_s_setprio(2);
    __builtin_amdgcn_s_barrier();
    SB();
#pragma unroll
    for (int i = 0; i < 4; ++i) vv[i] = ldw(rv, l16 + i * 1024, kvo);
    SB();
    if (PFPOS == 1) L2PF();
    HOPS();
  }
  asm volatile("" : "+s"(w));
  if (__builtin_expect(w == 0, 1)) {
    v4u r16[16], f8[8];
    const v4u xv = *(const GAS v4u*)((const GAS unsigned char*)x + (unsigned)l16);
    { const rsrc_t r = mk(wq, 512 * 4096 * 2), r1 = mk(w1, 512 * FF * 2);
#pragma unroll
      for (int i = 0; i < 24; ++i) {
        if (i < 16) r16[i] = ldw(r, vo, so + i * 8192); else f8[i - 16] = ldw(r1, vo1, so1 + (i - 16) * (FF * 2)); }
    }
    SB();
    __builtin_amdgcn_s_barrier();
    SB();
    STAMP(1);
    unsigned sel[8]; mksel(sel, l);
    int gvo = (l & 8) ? OOB : (l & 7) * 32 + (l & 16) + (l >> 5) * 8, xra = L_XN + kq * 256 + (l >> 3) * 32, xrc = L_XN + g * 128 + (l >> 3) * 16;
    asm volatile("" : "+v"(sel[0]), "+v"(sel[1]), "+v"(sel[2]), "+v"(sel[3]), "+v"(sel[4]), "+v"(sel[5]), "+v"(sel[6]), "+v"(sel[7]), "+v"(gvo), "+v"(xra), "+v"(xrc));
    SB();
    float xf[8];
#pragma unroll
    for (int i = 0; i < 4; ++i) { xf[2 * i] = bflo(xv[i]); xf[2 * i + 1] = bfhi(xv[i]); }
    const float mean = sum64(((xf[0] + xf[1]) + (xf[2] + xf[3])) + ((xf[4] + xf[5]) + (xf[6] + xf[7]))) * (1.f / 512);
    float vs = 0.f;
#pragma unroll
    for (int i = 0; i < 8; ++i) { const float d = xf[i] - mean; vs = fmaf(d, d, vs); }
    const float var = sum64(vs) * (1.f / 512);
    const float mb = rb(mean), rsd = rcp(rb(__builtin_amdgcn_sqrtf(rb(rb(var) + 1e-5f))));
    v4u xn;
#pragma unroll
    for (int i = 0; i < 4; ++i) { const unsigned d = pk(xf[2 * i] - mb, xf[2 * i + 1] - mb); xn[i] = pk(bflo(d) * rsd, bfhi(d) * rsd); }
    *(LAS v4u*)(L + L_XN + l * 16) = xn;
    const v4u xa = *(LAS v4u*)(L + xra), xb = *(LAS v4u*)(L + xra + 16), xc = *(LAS v4u*)(L + xrc);
    EPOCH();
    const unsigned tb0 = (tag >> ((l >> 5) * 16)) & 0xFFu, tb1 = (tag >> ((l >> 5) * 16 + 8)) & 0xFFu;
    { v4u bx[8];
#pragma unroll
      for (int i = 0; i < 8; ++i) bx[i] = diag(xa[i >> 1], sel, i & 1);
      SB();
      STAMP(2);
      v4f d = {0.f, 0.f, 0.f, 0.f};
#pragma unroll
      for (int i = 0; i < 8; ++i) { d = mfma(bx[i], r16[i], d); bx[i] = diag(xb[i >> 1], sel, i & 1); SB(); if (i == P2I) PADTO(2, ".Lpad8"); }
#pragma unroll
      for (int i = 0; i < 8; ++i) d = mfma(bx[i], r16[8 + i], d);
      ST8(diag_out(d, tb0, tb1), gvo, WS_QG + gn * 8192 + kq * 2048 + rb_ * 256);
    }
    STAMP(3);
    { v4f d = {0.f, 0.f, 0.f, 0.f};
#pragma unroll
      for (int i = 0; i < 8; ++i) d = mfma(diag(xc[i >> 1], sel, i & 1), f8[i], d);
      ST8(diag_out(d, tb0, tb1), hasw1 ? gvo : OOB, WS_HG + g * (FF * 4) + jw * 256); }
    PADTO(3, ".Lpad2");
    const int hr0 = gp * 224 + rb_ * 28, hu = min(l & 7, 6), ocol = (b & 63) * 8 + (l & 7);
    int mlo = (l & 51) == 0 ? (n >> 2) * 512 : OOB, mls = WS_ML + g * 2048 + j * 16, hvo = (l >> 3) * (FF * 4) + hu * 16, hso = WS_HG + hr0 * 4;
    int pvo = (l & 31) * 16, pso = WS_ML + gp * 2048 + (rb_ >> 1) * 512;
    asm volatile("" : "+v"(mlo), "+s"(mls), "+v"(hvo), "+s"(hso), "+v"(pvo), "+s"(pso));
    __builtin_amdgcn_s_setprio(SPRIO);
    STAMP(4);
    BAR();
    BAR();
    const float b1v = bflo(b1[hr0 + hu * 4 + gq]), b2v = bflo(b2[ocol]), xo = bflo(x[ocol]);
    v4u hg = LD16(hvo, hso);
    SB();
    BAR();
    { const v2u mm = *(LAS v2u*)(L + L_MX + n * 8);
      const v4f s0 = *(LAS v4f*)(L + L_S + l * 16), s1 = *(LAS v4f*)(L + L_S + 1024 + l * 16);
      const float m = maxq(fmx(__uint_as_float(mm[0]), __uint_as_float(mm[1])));
      float ls = fmaf((s0[0] + s0[1]) + (s0[2] + s0[3]), ex2(__uint_as_float(mm[0]) - m), ((s1[0] + s1[1]) + (s1[2] + s1[3])) * ex2(__uint_as_float(mm[1]) - m));
      ls += dpp<X1>(ls); ls += dpp<X2>(ls); ls = fold16(ls, ls); ls = fold32(ls, ls);
      ST16((v4u{__float_as_uint(m), tag, __float_as_uint(ls), tag}), mlo, mls);
    }
    STAMP(6);
    v4u pv = LD16(pvo, pso);
    SB();
    BAR();
    {
      POLL(hg, DBAD, hvo, hso, 14);
      float hs = fold16(fold32(gval(hg[0]), gval(hg[2])), fold32(gval(hg[1]), gval(hg[3]))); hs += dpp<R8>(hs);
      const float hp_ = rb(rb(hs) + b1v);
      const float u2 = 1.5957691216057308f * fmaf(0.044715f * hp_ * hp_, hp_, hp_);
      const float ge = hp_ * rcp(1.f + ex2(-u2 * LOG2E));
      *(LAS u16*)(L + L_H + (l & 7) * 8 + gq * 2) = ((l & 7) < 7) ? (u16)pk(ge, ge) : (u16)0;
    }
    STAMP(7);
    {
      POLL(pv, TBAD, pvo, pso, 12);
      const float m = __uint_as_float(pv[0]);
      const float M = max16(maxb(maxq(m)));
      const float e = ex2(m - M);
      float ls = e * __uint_as_float(pv[2]);
      ls += dpp<X1>(ls); ls += dpp<X2>(ls); ls += dpp<HM>(ls); ls += dpp<R8>(ls); ls = fold16(ls, ls);
      *(LAS float*)(L + L_WG + (l & 31) * 4) = e * rcp(ls);
    }
    STAMP(8);
    PADTO(4, ".Lpad3");
    BAR();
    if (b < 64) {
      BAR();
      STAMP(9);
      float v = *(LAS float*)(L + L_S + l * 4) + *(LAS float*)(L + L_S + 256 + l * 4);
      v = fold16(v, v); v = fold32(v, v);
      const float f = rb(rb(dpp<R8>(v)) + b2v);
      const float gt = rb(f * rcp(1.f + ex2(-f * LOG2E)));
      const float o = xo + rb(rb(v) * gt);
      if (l < 8) out[ocol] = (u16)pk(o, o);
      if (b == 0 && l == 0) *(GAS unsigned*)ws = tag;
    }
    STAMP(10);
    FLUSH();
  } else {
    const int a = w - 1, a3 = a & 3;
    int obo = (n & 3) == 0 ? (n >> 2) * 16384 + gq * 16 : OOB;
    int sbo = l < 32 ? (n >> 3) * 8192 + gq * 64 + (n & 7) * 8 : OOB;
    int obs = WS_OB + g * 65536 + (a3 >> 1) * 8192 + j * 256 + (a & 1) * 128, sbs = WS_SB + (kq * 16 + a * 2) * 8192 + (g * 8 + rb_) * 128;
    int sadr = L_S + (a & 1) * 1024 + (n >> 2) * 256 + (n & 3) * 64 + (a >> 1) * 16 + gq * 4, qadr = L_Q + (n & 3) * QSTR + gq * 64 + (n >> 2) * 16;
    int pso = WS_OB + gp * 65536 + rb_ * 8192 + a3 * 2048, pvo = a >= 4 ? l * 16 : OOB;
    int oadr = L_O + n * 128 + ((gq ^ (n >> 1)) & 7) * 16, owr = L_O + (a3 * 8 + gq) * 128 + ((((n >> 1) ^ ((a3 * 8 + gq) >> 1)) & 7) * 16) + (n & 1) * 8;
    int fso = WS_SB + b * 8192 + a * 1024, fadr = L_S + a * 64 + (l & 7) * 8 + (l >> 5) * 4, vwr = L_V + (a * 16 + gq) * VSTR + n * 16;
    int qso = WS_QG + g * 8192 + a3 * 512, qvo = a >= 4 ? (l >> 5) * 4096 + (l & 31) * 16 : OOB;
    int mxw = L_MX + (n & 3) * 32 + a * 4, qwr = L_Q + a3 * QSTR + (l & 31) * 8 + (l >> 5) * 4, kof = (n >> 2) * 256 + gq * 64 + (n & 3) * 16;
    asm volatile("" : "+v"(obo), "+v"(sbo), "+v"(sadr), "+v"(qadr), "+v"(oadr), "+v"(owr), "+v"(fadr), "+v"(vwr), "+v"(qvo), "+v"(mxw), "+v"(qwr), "+v"(kof), "+v"(pvo));
    asm volatile("" : "+s"(obs), "+s"(sbs), "+s"(pso), "+s"(fso), "+s"(qso));
    int wos = (gp * 512 + rb_ * 64 + a3 * 16) * 1024 + kq * 256, w2s = (gp * 224 + rb_ * 28 + a3 * 8) * 1024 + kq * 256, wvo = gq * 1024 + (n ^ (gq * 2)) * 16;
    int w2t = w2s + (a3 == 3 ? 0 : 4096);
    int wl0 = L_W + a3 * 4 * WBLK, wl1 = L_W + (16 + a3 * 2) * WBLK;
    v4u so_ = {(unsigned)(u64)wo, (unsigned)((u64)wo >> 32) & 0xFFFFu, 4096u * 512 * 2, 0x20000u}, s2_ = {(unsigned)(u64)w2, (unsigned)((u64)w2 >> 32) & 0xFFFFu, FF * 512u * 2, 0x20000u};
    asm volatile("" : "+s"(wos), "+s"(w2s), "+v"(wvo), "+s"(so_), "+s"(s2_), "+s"(wl0), "+s"(wl1), "+s"(w2t));
    const rsrc_t rk = mk(kc, 8u * SK * 256);
    SB();
    v4u kv[4], q0, q1;
#pragma unroll
    for (int i = 0; i < 4; ++i) {
      asm volatile("" : "+v"(vv[i]) : "v"(cpf));
      if (i == TRIG) {
#pragma unroll
        for (int k = 0; k < 4; ++k) kv[k] = ldw(rk, kof + k * 1024, kvo);
      }
      *(LAS v4u*)(L + vwr + i * 4 * VSTR) = vv[i];
      SB();
    }
    STAMP(2);
    EPOCH();
    PADTO(5, ".Lpadret");
    if (KOLD < 0) asm volatile("" : "+v"(kv[KTRIG]));
    else if (a < 4) { if (KOLD == 3) WAITV0(); else __builtin_amdgcn_s_waitcnt(0x0F70 | (3 - KOLD)); }
    BAR();
    if (QTRIG == 4) { if (QSLEEP) __builtin_amdgcn_s_sleep(QSLEEP); q0 = __builtin_amdgcn_raw_buffer_load_b128(rs, qvo, qso, QAUX); q1 = __builtin_amdgcn_raw_buffer_load_b128(rs, qvo, qso + 2048, QAUX); STAMP(1); }
    v2u vt[16];
    if (a < 4) {
#pragma unroll
      for (int i = 0; i < 16; ++i) vt[i] = trd(L + L_V + (gq * 4 + (n >> 2)) * VSTR + a * 64 + (n & 3) * 8 + (i & 7) * 16 * VSTR + (i >> 3) * 32);
    }
    SB();
    WAITV0();
    if (a < 4) {
      unsigned keep;
#define DMA1(SRD, SO) "buffer_load_dwordx4 %3, " SRD ", " SO " offen " DMAPOL " lds\n\ts_add_u32 m0, m0, %8\n\ts_add_u32 " SO ", " SO ", 0x1000\n\t"
      asm volatile("s_mov_b32 %0, m0\n\ts_mov_b32 m0, %6\n\ts_nop 0\n\t" DMA1("%4", "%1") DMA1("%4", "%1") DMA1("%4", "%1") DMA1("%4", "%1")
                   "s_mov_b32 m0, %7\n\ts_nop 0\n\t" DMA1("%5", "%2") "buffer_load_dwordx4 %3, %5, %9 offen " DMAPOL " lds\n\ts_mov_b32 m0, %0"
                   : "=&s"(keep), "+s"(wos), "+s"(w2s) : "v"(wvo), "s"(so_), "s"(s2_), "s"(wl0), "s"(wl1), "i"(WBLK), "s"(w2t) : "memory", "scc");
    } else {
#define QSUM() *(LAS unsigned*)(L + qwr) = pk(fold32(gval(q0[0]) + gval(q1[0]), gval(q0[2]) + gval(q1[2])), fold32(gval(q0[1]) + gval(q1[1]), gval(q0[3]) + gval(q1[3])))
      QSUM();
      SB();
      if (__builtin_expect(!__all((DBAD(q0) | DBAD(q1)) == 0), 0)) { POLL2(q0, q1, DBAD, qvo, qso, qso + 2048, 11); QSUM(); }
    }
    STAMP(4);
    CS(1);
    PADTO(6, ".Lpad5");
    BAR();
    CS(2);
    {
      const v4u qf = *(LAS v4u*)(L + qadr);
      v4f sc[4];
#pragma unroll
      for (int i = 0; i < 4; ++i) sc[i] = mfma(kv[i], qf, v4f{0.f, 0.f, 0.f, 0.f});
      SB();
      CS(3);
      float t0 = sc[0][0] + dpp<R12>(sc[0][1]);
      asm volatile("" : "+v"(t0)); t0 += dpp<R8>(sc[0][2]);
      float t1 = sc[1][1] + dpp<R4>(sc[1][0]);
      asm volatile("" : "+v"(t0), "+v"(t1)); t0 += dpp<R4>(sc[0][3]); t1 += dpp<R12>(sc[1][2]);
      float t2 = sc[2][2] + dpp<R8>(sc[2][0]);
      asm volatile("" : "+v"(t0), "+v"(t1), "+v"(t2)); t1 += dpp<R8>(sc[1][3]); t2 += dpp<R4>(sc[2][1]);
      float t3 = sc[3][3] + dpp<R12>(sc[3][0]);
      asm volatile("" : "+v"(t1), "+v"(t2), "+v"(t3)); t2 += dpp<R12>(sc[2][3]); t3 += dpp<R8>(sc[3][1]);
      t0 = LANES(0x00F000F000F000F0ull) ? t1 : t0;
      asm volatile("" : "+v"(t2), "+v"(t3)); t3 += dpp<R4>(sc[3][2]);
      t0 = LANES(0x0F000F000F000F00ull) ? t2 : t0;
      const float s1 = LANES(0xF000F000F000F000ull) ? t3 : t0;
      float s2 = rb(rb(s1) * 0.08838834764831845f) * LOG2E;
      asm volatile("" : "+v"(s2));
      CS(4);
      const float m = max32(max16(maxb(s2)));
      *(LAS float*)(L + mxw) = m;
      *(LAS float*)(L + sadr) = ex2(s2 - m);
    }
    STAMP(5);
    CS(5);
    BAR();
    CS(6);
    if (a < 4) {
      const v2u mm = *(LAS v2u*)(L + L_MX + n * 8);
      const v4f s0 = *(LAS v4f*)(L + L_S + l * 16), s1 = *(LAS v4f*)(L + L_S + 1024 + l * 16);
      const float m = maxq(fmx(__uint_as_float(mm[0]), __uint_as_float(mm[1])));
      const v4f p0 = s0 * ex2(__uint_as_float(mm[0]) - m), p1 = s1 * ex2(__uint_as_float(mm[1]) - m);
      const v4u pb = {pk(p0[0], p0[1]), pk(p0[2], p0[3]), pk(p1[0], p1[1]), pk(p1[2], p1[3])};
      CS(7);
      const v4f z = {0.f, 0.f, 0.f, 0.f};
      v4f d0 = mfma(cat(vt[0], vt[1]), pb, z), d1 = mfma(cat(vt[8], vt[9]), pb, z);
      const v4u pb1 = {dppu<0x55>(pb[0]), dppu<0x55>(pb[1]), dppu<0x55>(pb[2]), dppu<0x55>(pb[3])};
      d0 = mfma(cat(vt[2], vt[3]), pb1, d0); d1 = mfma(cat(vt[10], vt[11]), pb1, d1);
      const v4u pb2 = {dppu<0xAA>(pb[0]), dppu<0xAA>(pb[1]), dppu<0xAA>(pb[2]), dppu<0xAA>(pb[3])};
      d0 = mfma(cat(vt[4], vt[5]), pb2, d0); d1 = mfma(cat(vt[12], vt[13]), pb2, d1);
      const v4u pb3 = {dppu<0xFF>(pb[0]), dppu<0xFF>(pb[1]), dppu<0xFF>(pb[2]), dppu<0xFF>(pb[3])};
      d0 = mfma(cat(vt[6], vt[7]), pb3, d0); d1 = mfma(cat(vt[14], vt[15]), pb3, d1);
      ST16((v4u{pk(d0[0], d0[1]), tag, pk(d0[2], d0[3]), tag}), obo, obs);
      CS(8);
      ST16((v4u{pk(d1[0], d1[1]), tag, pk(d1[2], d1[3]), tag}), obo + 64, obs);
      CS(9);
      STAMP(6);
      asm volatile("s_waitcnt vmcnt(2)" ::: "memory");
    }
    SB();
    v4u pv = LD16(pvo, pso), pu = LD16(pvo, pso + 1024);
    PADTO(7, ".Lpad6");
    BAR();
    v2u wt[6];
#pragma unroll
    for (int i = 0; i < 6; ++i) wt[i] = trd(L + L_W + ((i >> 1) * 8 + gq * 2 + (i & 1)) * WBLK + (n >> 2) * 256 + (a ^ (n >> 2)) * 32 + (n & 3) * 8);
    SB();
    STAMP(7);
    if (a >= 4) {
      POLL2(pv, pu, TBAD, pvo, pso, pso + 1024, 12);
      STAMP(8);
      *(LAS v2u*)(L + owr) = v2u{pv[0], pv[2]}; *(LAS v2u*)(L + (owr ^ 32) + 512) = v2u{pu[0], pu[2]};
    }
    STAMP(9);
    CT(1);
    BAR();
    CT(2);
    { v4u of[4];
      of[0] = *(LAS v4u*)(L + oadr); of[2] = *(LAS v4u*)(L + oadr + 2048); of[1] = *(LAS v4u*)(L + (oadr ^ 64)); of[3] = *(LAS v4u*)(L + (oadr ^ 64) + 2048);
      const v4f w0 = *(LAS v4f*)(L + L_WG + gq * 16), w1_ = *(LAS v4f*)(L + L_WG + 64 + gq * 16);
      const v4u hf = *(LAS v4u*)(L + L_H + gq * 16);
      SB();
      const v4f z = {0.f, 0.f, 0.f, 0.f};
      v4f d0 = mfma(of[0], cat(wt[0], wt[1]), z), d1 = mfma(of[2], cat(wt[0], wt[1]), z);
      d0 = mfma(of[1], cat(wt[2], wt[3]), d0); d1 = mfma(of[3], cat(wt[2], wt[3]), d1);
      const v4f df = mfma(hf, cat(wt[4], wt[5]), z);
      float s = (fmaf(d0[1], w0[1], d0[0] * w0[0]) + fmaf(d0[3], w0[3], d0[2] * w0[2])) + (fmaf(d1[1], w1_[1], d1[0] * w1_[0]) + fmaf(d1[3], w1_[3], d1[2] * w1_[2]));
      s = fold32(s, s);
      const float e = fold16(s, 0.5f * df[0]);
      ST8((v2u{__float_as_uint(e), tag}), sbo, sbs);
      CT(7); CT(8); CT(9);
    }
    STAMP(10);
    PADTO(8, ".Lpad7");
    if (b < 64) {
      if (DELAY3 && a == g) __builtin_amdgcn_s_sleep(DELAY3);
      v4u sv = LD16(l * 16, fso);
      POLL(sv, TBAD, l * 16, fso, 13);
      float v = fold32(__uint_as_float(sv[0]), __uint_as_float(sv[2]));
      v = fold16(v, v);
      v += dpp<R8>(v);
      *(LAS float*)(L + fadr) = v;
      BAR();
    }
    FLUSH();
  }
}

extern "C" void kernel_launch(void* const* d_in, const int* in_sizes, int n_in, void* d_out, int out_size, void* d_ws, size_t ws_size, hipStream_t stream) {
  fused_decode<<<256, NTHR, 0, stream>>>((const u16*)d_in[0], (const u16*)d_in[1], (const u16*)d_in[2], (const u16*)d_in[3], (const u16*)d_in[4], (const u16*)d_in[6],
                                         (const u16*)d_in[7], (const u16*)d_in[8], (const u16*)d_in[9], (u16*)d_out, (unsigned char*)d_ws + WSOFF);
}
```

```cpp
#include <hip/hip_runtime.h>
#include <hip/hip_bf16.h>

typedef unsigned short u16;
typedef unsigned long long u64;
typedef unsigned v4u __attribute__((ext_vector_type(4)));
typedef unsigned v2u __attribute__((ext_vector_type(2)));
typedef float v4f __attribute__((ext_vector_type(4)));
typedef short s16x4 __attribute__((ext_vector_type(4)));
typedef __bf16 bf16x2 __attribute__((ext_vector_type(2)));
typedef __bf16 bf16x8 __attribute__((ext_vector_type(8)));
typedef __amdgpu_buffer_rsrc_t rsrc_t;
#define GAS __attribute__((address_space(1)))
#define CAS __attribute__((address_space(4)))
#define LAS __attribute__((address_space(3)))

constexpr int SK = 32768, WIN = 4096, FF = 1792;
constexpr int NTHR = 576;
constexpr unsigned WS_QG = 4096;
constexpr unsigned WS_HG = WS_QG + 65536;
constexpr unsigned WS_ML = WS_HG + 57344;
constexpr unsigned WS_OB = WS_ML + 16384;
constexpr unsigned WS_SB = WS_OB + 524288;
constexpr unsigned WS_FB = WS_SB + 262144;
constexpr unsigned WS_END = WS_FB + 262144;
constexpr int VSTR = 288, L_V = 0, L_O = 128 * VSTR, L_Q = L_O + 4096, QSTR = 320, L_S = L_Q + 4 * QSTR, L_XN = L_S + 2048, L_MX = L_XN + 1024, L_H = L_MX + 512,
              L_WG = L_H + 64, L_W = L_WG + 128, WBLK = 1088, L_END = L_W + 24 * WBLK;
static_assert(L_O % 128 == 0, "the o image is addressed with ^ 32 and ^ 64");
constexpr unsigned SPIN_CAP = 1u << 18;
constexpr float LOG2E = 1.4426950408889634f;
constexpr int OOB = 0x40000000;

__device__ __forceinline__ rsrc_t mk(const void* p, unsigned bytes) { return __builtin_amdgcn_make_buffer_rsrc((void*)p, 0, bytes, 0x00020000); }
__device__ __forceinline__ v4u ldw(rsrc_t r, int voff, int soff) { return __builtin_amdgcn_raw_buffer_load_b128(r, voff, soff, 2); }
__device__ __forceinline__ float uf(unsigned u) { return __uint_as_float(u); }
__device__ __forceinline__ float bflo(unsigned u) { return __uint_as_float(u << 16); }
__device__ __forceinline__ float bfhi(unsigned u) { return __uint_as_float(u & 0xFFFF0000u); }
__device__ __forceinline__ unsigned pk(float lo, float hi) { bf16x2 r = {(__bf16)lo, (__bf16)hi}; return __builtin_bit_cast(unsigned, r); }
__device__ __forceinline__ float rb(float f) { return __uint_as_float(pk(0.f, f)); }
__device__ __forceinline__ v4f mfma(v4u a, v4u b, v4f c) { return __builtin_amdgcn_mfma_f32_16x16x32_bf16(__builtin_bit_cast(bf16x8, a), __builtin_bit_cast(bf16x8, b), c, 0, 0, 0); }
template <int CTRL> __device__ __forceinline__ unsigned dppu(unsigned x) { return (unsigned)__builtin_amdgcn_mov_dpp((int)x, CTRL, 0xf, 0xf, true); }
template <int CTRL> __device__ __forceinline__ float dpp(float x) { return __uint_as_float(dppu<CTRL>(__float_as_uint(x))); }
template <int CTRL> __device__ __forceinline__ v4u dpp4(v4u x) { return v4u{dppu<CTRL>(x[0]), dppu<CTRL>(x[1]), dppu<CTRL>(x[2]), dppu<CTRL>(x[3])}; }
constexpr int X1 = 0xB1, X2 = 0x4E, HM = 0x141, R4 = 0x124, R8 = 0x128, R12 = 0x12C;
__device__ __forceinline__ float sw16(float a, float b, float& o) { auto r = __builtin_amdgcn_permlane16_swap(__float_as_uint(a), __float_as_uint(b), false, false); o = uf(r[1]); return uf(r[0]); }
__device__ __forceinline__ float sw32(float a, float b, float& o) { auto r = __builtin_amdgcn_permlane32_swap(__float_as_uint(a), __float_as_uint(b), false, false); o = uf(r[1]); return uf(r[0]); }
__device__ __forceinline__ float fold16(float a, float b) { float o; const float p = sw16(a, b, o); return p + o; }
__device__ __forceinline__ float fold32(float a, float b) { float o; const float p = sw32(a, b, o); return p + o; }
__device__ __forceinline__ float fmx(float a, float b) { return __builtin_elementwise_maximum(a, b); }
__device__ __forceinline__ float max16(float a) { float o; const float p = sw16(a, a, o); return fmx(p, o); }
__device__ __forceinline__ float max32(float a) { float o; const float p = sw32(a, a, o); return fmx(p, o); }
#define MAXDPP(name, ctl) __device__ __forceinline__ float name(float m) { float r; asm("s_nop 1\n\tv_max_f32_dpp %0, %1, %1 " ctl " row_mask:0xf bank_mask:0xf" : "=v"(r) : "v"(m)); return r; }
MAXDPP(mx_r8, "row_ror:8") MAXDPP(mx_r4, "row_ror:4") MAXDPP(mx_x1, "quad_perm:[1,0,3,2]") MAXDPP(mx_x2, "quad_perm:[2,3,0,1]")
__device__ __forceinline__ float sum64(float v) { v += dpp<X1>(v); v += dpp<X2>(v); v += dpp<HM>(v); v += dpp<R8>(v); v = fold16(v, v); return fold32(v, v); }
__device__ __forceinline__ v2u trd(LAS unsigned char* p) { return __builtin_bit_cast(v2u, __builtin_amdgcn_ds_read_tr16_b64_v4i16((LAS s16x4*)p)); }
__device__ __forceinline__ v4u cat(v2u a, v2u b) { return v4u{a[0], a[1], b[0], b[1]}; }
__device__ __forceinline__ float ex2(float x) { return __builtin_amdgcn_exp2f(x); }
__device__ __forceinline__ float rcp(float x) { return __builtin_amdgcn_rcpf(x); }
__device__ __forceinline__ float gval(unsigned d) { return __uint_as_float(d & 0xFFFFFF00u); }
__device__ __forceinline__ unsigned d4(float f, unsigned tagbyte) { return ((__float_as_uint(f) + 0x80u) & 0xFFFFFF00u) | tagbyte; }
#define SB() __builtin_amdgcn_sched_barrier(0)
#define LANES(mask) __builtin_amdgcn_inverse_ballot_w64(mask)
#define BAR() do { asm volatile("s_waitcnt lgkmcnt(0)" ::: "memory"); __builtin_amdgcn_s_barrier(); asm volatile("" ::: "memory"); } while (0)
#define FENCE() asm volatile("" ::: "memory")
#define WAITV(n) __builtin_amdgcn_s_waitcnt(0x0F70 | (n))
#define LD16(off, soff) __builtin_amdgcn_raw_buffer_load_b128(rs, (off), (soff), 16)
#define ST16(v, off, soff) __builtin_amdgcn_raw_buffer_store_b128((v), rs, (off), (soff), 16)
#define ST8(v, off, soff) __builtin_amdgcn_raw_buffer_store_b64((v), rs, (off), (soff), 16)
#define TBAD(v) (((v)[1] ^ tag) | ((v)[3] ^ tag))
#define DBAD(v) ((__builtin_amdgcn_perm((v)[1], (v)[0], 0x0c0c0400u) | __builtin_amdgcn_perm((v)[3], (v)[2], 0x04000c0cu)) ^ tag)
#define EPOCH() asm volatile("" : "+s"(ep0)); const unsigned tag = ((ep0 & 0x7F7F7F7Fu) + 0x01010101u) ^ (ep0 & 0x80808080u)
#define UNTIL(BADEXPR, RELOAD) do { unsigned spins_ = 0; _Pragma("nounroll") while (!__all((BADEXPR) == 0) && ++spins_ < SPIN_CAP) { FENCE(); RELOAD; WAITV(0); } } while (0)

__device__ __forceinline__ void mksel(unsigned (&sel)[8], int l) {
#pragma unroll
  for (int d = 0; d < 4; ++d) { const bool on = ((l & 7) >> 1) == d;
    sel[d] = on ? ((l & 1) ? 0x01000c0cu : 0x0c0c0100u) : 0x0c0c0c0cu; sel[4 + d] = on ? ((l & 1) ? 0x03020c0cu : 0x0c0c0302u) : 0x0c0c0c0cu; }
}
__device__ __forceinline__ v4u diag(unsigned pair, const unsigned (&sel)[8], int hi) {
  return v4u{__builtin_amdgcn_perm(0u, pair, sel[4 * hi]), __builtin_amdgcn_perm(0u, pair, sel[4 * hi + 1]), __builtin_amdgcn_perm(0u, pair, sel[4 * hi + 2]), __builtin_amdgcn_perm(0u, pair, sel[4 * hi + 3])};
}
__device__ __forceinline__ float upr8(float v) { return __builtin_bit_cast(float, __builtin_amdgcn_update_dpp(__builtin_bit_cast(int, v), __builtin_bit_cast(int, v), R8, 0xC, 0xF, false)); }
__device__ __forceinline__ v2u diag_out(v4f d, unsigned tb0, unsigned tb1) {
  const float t0 = upr8(d[0]), t1 = upr8(d[1]), t2 = upr8(d[2]), t3 = upr8(d[3]);
  return v2u{d4(fold32(t0, t2), tb0), d4(fold32(t1, t3), tb1)};
}

__global__ void __launch_bounds__(NTHR, 3) fused_decode(const u16* __restrict__ x, const u16* __restrict__ wq, const u16* __restrict__ w1, const u16* __restrict__ vc, unsigned char* ws,
                                                        const u16* __restrict__ kc, const u16* __restrict__ wo, const u16* __restrict__ w2, const u16* __restrict__ b1,
                                                        const u16* __restrict__ b2, u16* __restrict__ out) {
  __shared__ __attribute__((aligned(128))) unsigned char lds_[L_END];
  LAS unsigned char* L = (LAS unsigned char*)lds_;
  const int b = blockIdx.x, g = b & 7, j = b >> 3, l = threadIdx.x & 63;
  int w = __builtin_amdgcn_readfirstlane(threadIdx.x >> 6);
  { u64 pc; unsigned keep, t_;
    asm volatile("s_cmp_lg_u32 %3, 4\n\ts_cbranch_scc1 1f\n\ts_getpc_b64 %0\n\ts_mov_b32 %1, m0\n\ts_mov_b32 m0, %5\n\tv_lshlrev_b32 %2, 7, %4\n\t"
                 "global_load_lds_dword %2, %0\n\ts_mov_b32 m0, %1\n1:"
                 : "=&s"(pc), "=&s"(keep), "=&v"(t_) : "s"(w), "v"((j * 4 + (l & 3)) & 63), "i"(L_O) : "scc", "memory"); }
  const int kq = j & 3, rb_ = j >> 2, gn = (g + 1) & 7, gp = (g - 1) & 7;
  const int gq = l >> 4, n = l & 15;
  const bool hasw1 = rb_ != 3; const int jw = rb_ == 7 ? j - 16 : j;
  int kvo = (g * SK + (SK - WIN) + j * 128 + ((w - 1 + j) & 7) * 16) * 256, l16 = l * 16;
  int vo = (l >> 3) * (16 * 8192) + (l & 7) * 16, so = kq * (128 * 8192) + gn * 1024 + rb_ * 128;
  int vo1 = hasw1 ? (l >> 3) * (8 * FF * 2) + (l & 7) * 16 : OOB, so1 = g * (64 * FF * 2) + jw * 128;
  asm volatile("" : "+s"(kvo), "+s"(so), "+s"(so1));
  asm volatile("s_setprio 3" : "+v"(l16), "+v"(vo), "+v"(vo1));
  asm volatile("" :: "s"(x), "s"(kc), "s"(vc), "s"(wq), "s"(wo), "s"(w1), "s"(b1), "s"(w2), "s"(b2), "s"(out), "s"(ws));
  unsigned ep0 = *(const CAS unsigned*)ws;
  const rsrc_t rs = mk(ws, WS_END);
  v4u vv[4];
  if (w != 0) {
    const rsrc_t rv = mk(vc, 8u * SK * 256);
    __builtin_amdgcn_s_setprio(2);
    __builtin_amdgcn_s_barrier();
    SB();
#pragma unroll
    for (int i = 0; i < 4; ++i) vv[i] = ldw(rv, l16 + i * 1024, kvo);
    SB();
  }
  asm volatile("" : "+s"(w));
  if (__builtin_expect(w == 0, 1)) {
    v4u r16[16], f8[8];
    const v4u xv = *(const GAS v4u*)((const GAS unsigned char*)x + (unsigned)l16);
    { const rsrc_t r = mk(wq, 512 * 4096 * 2), r1 = mk(w1, 512 * FF * 2);
#pragma unroll
      for (int i = 0; i < 16; ++i) r16[i] = ldw(r, vo, so + i * 8192);
#pragma unroll
      for (int i = 0; i < 8; ++i) f8[i] = ldw(r1, vo1, so1 + i * (FF * 2)); }
    SB();
    __builtin_amdgcn_s_barrier();
    SB();
    unsigned sel[8]; mksel(sel, l);
    int gvo = (l & 8) ? OOB : (l & 7) * 32 + (l & 16) + (l >> 5) * 8, xra = L_XN + kq * 256 + (l >> 3) * 32, xrc = L_XN + g * 128 + (l >> 3) * 16;
    asm volatile("" : "+v"(sel[0]), "+v"(sel[1]), "+v"(sel[2]), "+v"(sel[3]), "+v"(sel[4]), "+v"(sel[5]), "+v"(sel[6]), "+v"(sel[7]), "+v"(gvo), "+v"(xra), "+v"(xrc));
    SB();
    float xf[8];
#pragma unroll
    for (int i = 0; i < 4; ++i) { xf[2 * i] = bflo(xv[i]); xf[2 * i + 1] = bfhi(xv[i]); }
    const float mean = sum64(((xf[0] + xf[1]) + (xf[2] + xf[3])) + ((xf[4] + xf[5]) + (xf[6] + xf[7]))) * (1.f / 512);
    float vs = 0.f;
#pragma unroll
    for (int i = 0; i < 8; ++i) { const float d = xf[i] - mean; vs = fmaf(d, d, vs); }
    const float var = sum64(vs) * (1.f / 512);
    const float mb = rb(mean), rsd = rcp(rb(__builtin_amdgcn_sqrtf(rb(rb(var) + 1e-5f))));
    v4u xn;
#pragma unroll
    for (int i = 0; i < 4; ++i) { const unsigned d = pk(xf[2 * i] - mb, xf[2 * i + 1] - mb); xn[i] = pk(bflo(d) * rsd, bfhi(d) * rsd); }
    *(LAS v4u*)(L + L_XN + l * 16) = xn;
    const v4u xa = *(LAS v4u*)(L + xra), xb = *(LAS v4u*)(L + xra + 16), xc = *(LAS v4u*)(L + xrc);
    EPOCH();
    const unsigned tb0 = (tag >> ((l >> 5) * 16)) & 0xFFu, tb1 = (tag >> ((l >> 5) * 16 + 8)) & 0xFFu;
    { v4u bx[8];
#pragma unroll
      for (int i = 0; i < 8; ++i) bx[i] = diag(xa[i >> 1], sel, i & 1);
      SB();
      v4f d = {0.f, 0.f, 0.f, 0.f};
#pragma unroll
      for (int i = 0; i < 8; ++i) { d = mfma(bx[i], r16[i], d); bx[i] = diag(xb[i >> 1], sel, i & 1); SB(); }
#pragma unroll
      for (int i = 0; i < 8; ++i) d = mfma(bx[i], r16[8 + i], d);
      ST8(diag_out(d, tb0, tb1), gvo, WS_QG + gn * 8192 + kq * 2048 + rb_ * 256);
    }
    { v4f d = {0.f, 0.f, 0.f, 0.f};
#pragma unroll
      for (int i = 0; i < 8; ++i) d = mfma(diag(xc[i >> 1], sel, i & 1), f8[i], d);
      ST8(diag_out(d, tb0, tb1), hasw1 ? gvo : OOB, WS_HG + g * (FF * 4) + jw * 256); }
    const int hr0 = gp * 224 + rb_ * 28, hu = min(l & 7, 6), ocol = (b & 63) * 8 + (l & 3) * 2 + (l >> 5);
    int mlo = (l & 51) == 0 ? (n >> 2) * 512 : OOB, mls = WS_ML + g * 2048 + j * 16, hvo = (l >> 3) * (FF * 4) + hu * 16, hso = WS_HG + hr0 * 4;
    int pvo = (l & 31) * 16, pso = WS_ML + gp * 2048 + (rb_ >> 1) * 512;
    asm volatile("" : "+v"(mlo), "+v"(hvo), "+v"(pvo));
    asm volatile("" : "+s"(mls), "+s"(hso), "+s"(pso));
    const float b1v = bflo(b1[hr0 + hu * 4 + gq]), b2v = bflo(b2[ocol]), xo = bflo(x[ocol]);
    BAR();
    v4u hg = LD16(hvo, hso);
    SB();
    BAR();
    BAR();
    { const v2u mm = *(LAS v2u*)(L + L_MX + l * 8);
      const v4f s0 = *(LAS v4f*)(L + L_S + l * 16), s1 = *(LAS v4f*)(L + L_S + 1024 + l * 16);
      const float m = max32(max16(mx_x2(mx_x1(fmx(uf(mm[0]), uf(mm[1]))))));
      float ls = fmaf((s0[0] + s0[1]) + (s0[2] + s0[3]), ex2(uf(mm[0]) - m), ((s1[0] + s1[1]) + (s1[2] + s1[3])) * ex2(uf(mm[1]) - m));
      ls += dpp<X1>(ls); ls += dpp<X2>(ls); ls = fold16(ls, ls); ls = fold32(ls, ls);
      ST16((v4u{__float_as_uint(m), tag, __float_as_uint(ls), tag}), mlo, mls);
    }
    v4u pv = LD16(pvo, pso);
    SB();
    {
      if (__builtin_expect(!__all(DBAD(hg) == 0), 0)) UNTIL(DBAD(hg), hg = LD16(hvo, hso));
      float hs = fold16(fold32(gval(hg[0]), gval(hg[2])), fold32(gval(hg[1]), gval(hg[3]))); hs += dpp<R8>(hs);
      const float hp = rb(rb(hs) + b1v);
      const float u2 = 1.5957691216057308f * fmaf(0.044715f * hp * hp, hp, hp);
      const float ge = hp * rcp(1.f + ex2(-u2 * LOG2E));
      *(LAS u16*)(L + L_H + (l & 7) * 8 + gq * 2) = ((l & 7) < 7) ? (u16)pk(ge, ge) : (u16)0;
    }
    BAR();
    {
      UNTIL(TBAD(pv), pv = LD16(pvo, pso));
      const float m = uf(pv[0]);
      const float M = max16(mx_r4(mx_r8(mx_x2(mx_x1(m)))));
      const float e = ex2(m - M);
      float ls = e * uf(pv[2]);
      ls += dpp<X1>(ls); ls += dpp<X2>(ls); ls += dpp<HM>(ls); ls += dpp<R8>(ls); ls = fold16(ls, ls);
      *(LAS float*)(L + L_WG + (l & 31) * 4) = e * rcp(ls);
    }
    BAR();
    if (b < 64) {
      const int fo = WS_FB + b * 4096;
      v4u f0 = LD16(l16, fo), f1 = LD16(l16 + 1024, fo), f2 = LD16(l16 + 2048, fo), f3 = LD16(l16 + 3072, fo);
      UNTIL(TBAD(f0) | TBAD(f1) | TBAD(f2) | TBAD(f3), (f0 = LD16(l16, fo), f1 = LD16(l16 + 1024, fo), f2 = LD16(l16 + 2048, fo), f3 = LD16(l16 + 3072, fo)));
      float t = fold32((uf(f0[0]) + uf(f1[0])) + (uf(f2[0]) + uf(f3[0])), (uf(f0[2]) + uf(f1[2])) + (uf(f2[2]) + uf(f3[2])));
      t = fold16(t, t); t += dpp<R8>(t); t += dpp<R4>(t);
      const float f = rb(rb(t) + b2v);
      float gt = rb(f * rcp(1.f + ex2(-f * LOG2E)));
      asm volatile("" : "+v"(gt));
      BAR();
      float v = *(LAS float*)(L + L_S + l * 4);
      v = fold16(v, v); v += dpp<R8>(v); v += dpp<R4>(v);
      const float o = xo + rb(rb(v) * gt);
      if (!(l & 28)) __hip_atomic_store((GAS u16*)out + ocol, (u16)pk(o, o), __ATOMIC_RELAXED, __HIP_MEMORY_SCOPE_AGENT);
      if (b == 0 && l == 0) __hip_atomic_store((GAS unsigned*)ws, tag, __ATOMIC_RELAXED, __HIP_MEMORY_SCOPE_AGENT);
    }
  } else {
    const int a = w - 1, a3 = a & 3;
    int obo = (n & 3) == 0 ? (n >> 2) * 16384 + gq * 16 : OOB;
    int sbo = l < 16 ? (n >> 3) * 4096 + (n & 7) * 8 : OOB;
    int obs = WS_OB + g * 65536 + (a3 >> 1) * 8192 + j * 256 + (a & 1) * 128, sbs = WS_SB + (kq * 16 + a * 2) * 4096 + (g * 8 + rb_) * 64;
    int sadr = L_S + (a & 1) * 1024 + gq * 256 + (n & 3) * 64 + (a >> 1) * 16 + (n >> 2) * 4, qadr = L_Q + (n & 3) * QSTR + gq * 64 + (n >> 2) * 16;
    int pso = WS_OB + gp * 65536 + rb_ * 8192 + a3 * 2048, pvo = a >= 4 ? l * 16 : OOB;
    int oadr = L_O + n * 128 + ((gq ^ (n >> 1)) & 7) * 16, owr = L_O + (a3 * 8 + gq) * 128 + ((((n >> 1) ^ ((a3 * 8 + gq) >> 1)) & 7) * 16) + (n & 1) * 8;
    int fso = WS_SB + b * 4096 + a * 512, fvo = (l & 31) * 16, fadr = L_S + (l & 16) * 8 + a * 16 + (l & 3) * 4;
    int vwr = L_V + (a * 16 + gq) * VSTR + n * 16;
    int qso = WS_QG + g * 8192 + a3 * 512, qvo = a >= 4 ? (l >> 5) * 4096 + (l & 31) * 16 : OOB;
    int mxw = L_MX + gq * 128 + (n & 3) * 32 + a * 4, qwr = L_Q + a3 * QSTR + (l & 31) * 8 + (l >> 5) * 4, kof = (n >> 2) * 1024 + gq * 64 + (n & 3) * 16;
    int wos = (gp * 512 + rb_ * 64 + a3 * 16) * 1024 + kq * 256, w2s = (gp * 224 + rb_ * 28 + a3 * 8) * 1024 + kq * 256, wvo = gq * 1024 + (n ^ (gq * 2)) * 16;
    int w2t = w2s + (a3 == 3 ? 0 : 4096);
    int wl0 = L_W + a3 * 4 * WBLK, wl1 = L_W + (16 + a3 * 2) * WBLK;
    v4u so_ = {(unsigned)(u64)wo, (unsigned)((u64)wo >> 32) & 0xFFFFu, 4096u * 512 * 2, 0x20000u}, s2_ = {(unsigned)(u64)w2, (unsigned)((u64)w2 >> 32) & 0xFFFFu, FF * 512u * 2, 0x20000u};
    asm volatile("" : "+v"(obo), "+v"(sbo), "+v"(sadr), "+v"(qadr), "+v"(oadr), "+v"(owr), "+v"(vwr), "+v"(fvo), "+v"(fadr), "+v"(qvo), "+v"(mxw), "+v"(qwr), "+v"(kof), "+v"(pvo), "+v"(wvo));
    asm volatile("" : "+s"(obs), "+s"(sbs), "+s"(pso), "+s"(qso), "+s"(fso), "+s"(wos), "+s"(w2s), "+s"(so_), "+s"(s2_), "+s"(wl0), "+s"(wl1), "+s"(w2t));
    const rsrc_t rk = mk(kc, 8u * SK * 256);
    SB();
    v4u kv[4];
#pragma unroll
    for (int i = 0; i < 4; ++i) {
      asm volatile("" : "+v"(vv[i]));
      if (i == 0) {
#pragma unroll
        for (int k = 0; k < 4; ++k) kv[k] = ldw(rk, kof + k * 256, kvo);
      }
      *(LAS v4u*)(L + vwr + i * 4 * VSTR) = vv[i];
      SB();
    }
    EPOCH();
    if (a < 4) WAITV(0);
    BAR();
    v4u q0 = LD16(qvo, qso), q1 = LD16(qvo, qso + 2048);
    v2u vt[16];
    if (a < 4) {
#pragma unroll
      for (int i = 0; i < 16; ++i) vt[i] = trd(L + L_V + (gq * 4 + (n >> 2)) * VSTR + a * 64 + (n & 3) * 8 + (i & 7) * 16 * VSTR + (i >> 3) * 32);
    }
    SB();
    asm volatile("" : "+v"(kv[2]) :: "memory");
    const v4u r0 = LD16(qvo, qso), r1 = LD16(qvo, qso + 2048);
    SB();
    WAITV(2);
    if (a < 4) {
      unsigned keep;
#define DMA1(SRD, SO) "buffer_load_dwordx4 %3, " SRD ", " SO " offen nt lds\n\ts_add_u32 m0, m0, %8\n\ts_add_u32 " SO ", " SO ", 0x1000\n\t"
      asm volatile("s_mov_b32 %0, m0\n\ts_mov_b32 m0, %6\n\ts_nop 0\n\t" DMA1("%4", "%1") DMA1("%4", "%1") DMA1("%4", "%1") DMA1("%4", "%1")
                   "s_mov_b32 m0, %7\n\ts_nop 0\n\t" DMA1("%5", "%2") "buffer_load_dwordx4 %3, %5, %9 offen nt lds\n\ts_mov_b32 m0, %0"
                   : "=&s"(keep), "+s"(wos), "+s"(w2s) : "v"(wvo), "s"(so_), "s"(s2_), "s"(wl0), "s"(wl1), "i"(WBLK), "s"(w2t) : "memory", "scc");
    } else {
#define QSUM() *(LAS unsigned*)(L + qwr) = pk(fold32(gval(q0[0]) + gval(q1[0]), gval(q0[2]) + gval(q1[2])), fold32(gval(q0[1]) + gval(q1[1]), gval(q0[3]) + gval(q1[3])))
      QSUM();
      SB();
      if (__builtin_expect(!__all((DBAD(q0) | DBAD(q1)) == 0), 0)) {
        q0 = r0; q1 = r1;
        UNTIL(DBAD(q0) | DBAD(q1), (q0 = LD16(qvo, qso), q1 = LD16(qvo, qso + 2048)));
        QSUM();
      }
    }
    BAR();
    {
      const v4u qf = *(LAS v4u*)(L + qadr);
      v4f sc[4];
#pragma unroll
      for (int i = 0; i < 4; ++i) sc[i] = mfma(kv[i], qf, v4f{0.f, 0.f, 0.f, 0.f});
      SB();
      float t0 = sc[0][0] + dpp<R12>(sc[0][1]);
      asm volatile("" : "+v"(t0)); t0 += dpp<R8>(sc[0][2]);
      float t1 = sc[1][1] + dpp<R4>(sc[1][0]);
      asm volatile("" : "+v"(t0), "+v"(t1)); t0 += dpp<R4>(sc[0][3]); t1 += dpp<R12>(sc[1][2]);
      float t2 = sc[2][2] + dpp<R8>(sc[2][0]);
      asm volatile("" : "+v"(t0), "+v"(t1), "+v"(t2)); t1 += dpp<R8>(sc[1][3]); t2 += dpp<R4>(sc[2][1]);
      float t3 = sc[3][3] + dpp<R12>(sc[3][0]);
      asm volatile("" : "+v"(t1), "+v"(t2), "+v"(t3)); t2 += dpp<R12>(sc[2][3]); t3 += dpp<R8>(sc[3][1]);
      t0 = LANES(0x00F000F000F000F0ull) ? t1 : t0;
      asm volatile("" : "+v"(t2), "+v"(t3)); t3 += dpp<R4>(sc[3][2]);
      t0 = LANES(0x0F000F000F000F00ull) ? t2 : t0;
      const float s1 = LANES(0xF000F000F000F000ull) ? t3 : t0;
      float s2 = rb(rb(s1) * 0.08838834764831845f) * LOG2E;
      asm volatile("" : "+v"(s2));
      const float m = mx_r4(mx_r8(s2));
      *(LAS float*)(L + mxw) = m;
      *(LAS float*)(L + sadr) = ex2(s2 - m);
    }
    BAR();
    if (a < 4) {
      const v2u mm = *(LAS v2u*)(L + L_MX + l * 8);
      const v4f s0 = *(LAS v4f*)(L + L_S + l * 16), s1 = *(LAS v4f*)(L + L_S + 1024 + l * 16);
      const float m = max32(max16(mx_x2(mx_x1(fmx(uf(mm[0]), uf(mm[1]))))));
      const v4f p0 = s0 * ex2(uf(mm[0]) - m), p1 = s1 * ex2(uf(mm[1]) - m);
      const v4u pb = {pk(p0[0], p0[1]), pk(p0[2], p0[3]), pk(p1[0], p1[1]), pk(p1[2], p1[3])};
      const v4f z = {0.f, 0.f, 0.f, 0.f};
      v4f d0 = mfma(cat(vt[0], vt[1]), pb, z), d1 = mfma(cat(vt[8], vt[9]), pb, z);
      const v4u pb1 = dpp4<0x55>(pb), pb2 = dpp4<0xAA>(pb);
      SB();
      d0 = mfma(cat(vt[2], vt[3]), pb1, d0); d1 = mfma(cat(vt[10], vt[11]), pb1, d1);
      v4u pb3 = dpp4<0xFF>(pb), o0 = {0u, tag, 0u, tag}, o1 = o0;
      asm volatile("" : "+v"(pb3), "+v"(o0), "+v"(o1) : "v"(pb1), "v"(vt[0]), "v"(vt[1]), "v"(vt[8]), "v"(vt[9]));
      d0 = mfma(cat(vt[4], vt[5]), pb2, d0); d1 = mfma(cat(vt[12], vt[13]), pb2, d1);
      d0 = mfma(cat(vt[6], vt[7]), pb3, d0); d1 = mfma(cat(vt[14], vt[15]), pb3, d1);
      o0[0] = pk(d0[0], d0[1]); o0[2] = pk(d0[2], d0[3]); o1[0] = pk(d1[0], d1[1]); o1[2] = pk(d1[2], d1[3]);
      ST16(o0, obo, obs);
      ST16(o1, obo + 64, obs);
      asm volatile("s_waitcnt vmcnt(2)" ::: "memory");
    }
    SB();
    v4u pv = LD16(pvo, pso), pu = LD16(pvo, pso + 1024);
    BAR();
    if (a >= 4 && !__all((TBAD(pv) | TBAD(pu)) == 0)) { pv = LD16(pvo, pso); pu = LD16(pvo, pso + 1024); }
    SB();
    v2u wt[4];
    const int wrd = L_W + gq * 2 * WBLK + (n >> 2) * 256 + (a ^ (n >> 2)) * 32 + (n & 3) * 8;
#pragma unroll
    for (int i = 0; i < 4; ++i) wt[i] = trd(L + wrd + ((i >> 1) * 8 + (i & 1)) * WBLK);
    SB();
    WAITV(0);
    asm volatile("" :: "v"(r0), "v"(r1));
    if (a < 4) {
      const v4u hf = *(LAS v4u*)(L + L_H + gq * 16);
      LAS unsigned char* p = L + wrd + 16 * WBLK;
      const v4f z = {0.f, 0.f, 0.f, 0.f};
      const v4f f0 = mfma(hf, cat(trd(p), trd(p + WBLK)), z), f1 = mfma(hf, cat(trd(p + 128), trd(p + 128 + WBLK)), z);
      ST8((v2u{__float_as_uint(gq ? f1[0] : f0[0]), tag}), l < 32 ? gq * 32768 + (n >> 3) * 4096 + (n & 7) * 8 : OOB, sbs + (WS_FB - WS_SB));
    }
    SB();
    if (a >= 4) {
      UNTIL(TBAD(pv) | TBAD(pu), (pv = LD16(pvo, pso), pu = LD16(pvo, pso + 1024)));
      *(LAS v2u*)(L + owr) = v2u{pv[0], pv[2]}; *(LAS v2u*)(L + (owr ^ 32) + 512) = v2u{pu[0], pu[2]};
    }
    BAR();
    {
      const v4u of0 = *(LAS v4u*)(L + oadr), of2 = *(LAS v4u*)(L + oadr + 2048), of1 = *(LAS v4u*)(L + (oadr ^ 64)), of3 = *(LAS v4u*)(L + (oadr ^ 64) + 2048);
      const v4f w0 = *(LAS v4f*)(L + L_WG + gq * 16), w1_ = *(LAS v4f*)(L + L_WG + 64 + gq * 16);
      SB();
      const v4f z = {0.f, 0.f, 0.f, 0.f};
      v4f d0 = mfma(of0, cat(wt[0], wt[1]), z), d1 = mfma(of2, cat(wt[0], wt[1]), z);
      d0 = mfma(of1, cat(wt[2], wt[3]), d0); d1 = mfma(of3, cat(wt[2], wt[3]), d1);
      const v4f t = d0 * w0 + d1 * w1_;
      float e = (t[0] + t[2]) + (t[1] + t[3]);
      e = fold32(e, e); e = fold16(e, e);
      ST8((v2u{__float_as_uint(e), tag}), sbo, sbs);
    }
    if (b < 64) {
      if (a == g) __builtin_amdgcn_s_sleep(20); else __builtin_amdgcn_s_sleep(10);
      v4u sv = LD16(fvo, fso);
      UNTIL(TBAD(sv), sv = LD16(fvo, fso));
      float v = fold16(uf(sv[0]), uf(sv[2]));
      v += dpp<R8>(v); v += dpp<R4>(v);
      *(LAS float*)(L + fadr) = v;
      BAR();
    }
  }
}

extern "C" void kernel_launch(void* const* d_in, const int* in_sizes, int n_in, void* d_out, int out_size, void* d_ws, size_t ws_size, hipStream_t stream) {
  fused_decode<<<256, NTHR, 0, stream>>>((const u16*)d_in[0], (const u16*)d_in[3], (const u16*)d_in[6], (const u16*)d_in[2], (unsigned char*)d_ws, (const u16*)d_in[1],
                                         (const u16*)d_in[4], (const u16*)d_in[8], (const u16*)d_in[7], (const u16*)d_in[9], (u16*)d_out);
}
```

```cpp
#define DA 3
#define DB 4
#include <hip/hip_runtime.h>
#include <hip/hip_bf16.h>

typedef unsigned short u16;
typedef unsigned long long u64;
typedef unsigned v4u __attribute__((ext_vector_type(4)));
typedef unsigned v2u __attribute__((ext_vector_type(2)));
typedef float v4f __attribute__((ext_vector_type(4)));
typedef short s16x4 __attribute__((ext_vector_type(4)));
typedef __bf16 bf16x2 __attribute__((ext_vector_type(2)));
typedef __bf16 bf16x8 __attribute__((ext_vector_type(8)));
typedef __amdgpu_buffer_rsrc_t rsrc_t;
#define GAS __attribute__((address_space(1)))
#define CAS __attribute__((address_space(4)))
#define LAS __attribute__((address_space(3)))

#define CK(i) do { if ((i) == DA) { SB(); ca = (unsigned)__builtin_amdgcn_s_memtime(); SB(); } if ((i) == DB) { SB(); cb = (unsigned)__builtin_amdgcn_s_memtime(); SB(); } } while (0)
constexpr int SK = 32768, WIN = 4096, FF = 1792;
constexpr int NTHR = 576;
constexpr unsigned WS_QG = 4096;
constexpr unsigned WS_HG = WS_QG + 65536;
constexpr unsigned WS_ML = WS_HG + 57344;
constexpr unsigned WS_OB = WS_ML + 16384;
constexpr unsigned WS_SB = WS_OB + 524288;
constexpr unsigned WS_FB = WS_SB + 262144;
constexpr unsigned WS_END = WS_FB + 262144;
constexpr int VSTR = 288, L_V = 0, L_O = 128 * VSTR, L_Q = L_O + 4096, QSTR = 320, L_S = L_Q + 4 * QSTR, L_XN = L_S + 2048, L_MX = L_XN + 1024, L_M = L_MX + 512, L_H = L_M + 32,
              L_WG = L_H + 64, L_W = L_WG + 128, WBLK = 1088, L_END = L_W + 24 * WBLK;
static_assert(L_O % 128 == 0, "the o image is addressed with ^ 32 and ^ 64");
constexpr unsigned SPIN_CAP = 1u << 18;
constexpr float LOG2E = 1.4426950408889634f;
constexpr int OOB = 0x40000000;

__device__ __forceinline__ rsrc_t mk(const void* p, unsigned bytes) { return __builtin_amdgcn_make_buffer_rsrc((void*)p, 0, bytes, 0x00020000); }
__device__ __forceinline__ v4u ldw(rsrc_t r, int voff, int soff) { return __builtin_amdgcn_raw_buffer_load_b128(r, voff, soff, 2); }
__device__ __forceinline__ float uf(unsigned u) { return __uint_as_float(u); }
__device__ __forceinline__ float bflo(unsigned u) { return __uint_as_float(u << 16); }
__device__ __forceinline__ float bfhi(unsigned u) { return __uint_as_float(u & 0xFFFF0000u); }
__device__ __forceinline__ unsigned pk(float lo, float hi) { bf16x2 r = {(__bf16)lo, (__bf16)hi}; return __builtin_bit_cast(unsigned, r); }
typedef float v2f __attribute__((ext_vector_type(2)));
__device__ __forceinline__ float rb(float f) { return __builtin_bit_cast(float, __builtin_convertvector((v2f{0.f, f}), bf16x2)); }
__device__ __forceinline__ v4f mfma(v4u a, v4u b, v4f c) { return __builtin_amdgcn_mfma_f32_16x16x32_bf16(__builtin_bit_cast(bf16x8, a), __builtin_bit_cast(bf16x8, b), c, 0, 0, 0); }
template <int CTRL> __device__ __forceinline__ unsigned dppu(unsigned x) { return (unsigned)__builtin_amdgcn_mov_dpp((int)x, CTRL, 0xf, 0xf, true); }
template <int CTRL> __device__ __forceinline__ float dpp(float x) { return __uint_as_float(dppu<CTRL>(__float_as_uint(x))); }
template <int CTRL> __device__ __forceinline__ v4u dpp4(v4u x) { return v4u{dppu<CTRL>(x[0]), dppu<CTRL>(x[1]), dppu<CTRL>(x[2]), dppu<CTRL>(x[3])}; }
constexpr int X1 = 0xB1, X2 = 0x4E, HM = 0x141, R4 = 0x124, R8 = 0x128;
__device__ __forceinline__ float sw16(float a, float b, float& o) { auto r = __builtin_amdgcn_permlane16_swap(__float_as_uint(a), __float_as_uint(b), false, false); o = uf(r[1]); return uf(r[0]); }
__device__ __forceinline__ float sw32(float a, float b, float& o) { auto r = __builtin_amdgcn_permlane32_swap(__float_as_uint(a), __float_as_uint(b), false, false); o = uf(r[1]); return uf(r[0]); }
__device__ __forceinline__ float fold16(float a, float b) { float o; const float p = sw16(a, b, o); return p + o; }
__device__ __forceinline__ float fold32(float a, float b) { float o; const float p = sw32(a, b, o); return p + o; }
__device__ __forceinline__ float fmx(float a, float b) { return __builtin_elementwise_maximum(a, b); }
__device__ __forceinline__ float max16(float a) { float o; const float p = sw16(a, a, o); return fmx(p, o); }
#define MAXDPP(name, ctl) __device__ __forceinline__ float name(float m) { float r; asm("s_nop 1\n\tv_max_f32_dpp %0, %1, %1 " ctl " row_mask:0xf bank_mask:0xf" : "=v"(r) : "v"(m)); return r; }
MAXDPP(mx_r8, "row_ror:8") MAXDPP(mx_r4, "row_ror:4") MAXDPP(mx_x1, "quad_perm:[1,0,3,2]") MAXDPP(mx_x2, "quad_perm:[2,3,0,1]")
__device__ __forceinline__ float sum64(float v) { v += dpp<X1>(v); v += dpp<X2>(v); v += dpp<HM>(v); v += dpp<R8>(v); v = fold16(v, v); return fold32(v, v); }
__device__ __forceinline__ v2u trd(LAS unsigned char* p) { return __builtin_bit_cast(v2u, __builtin_amdgcn_ds_read_tr16_b64_v4i16((LAS s16x4*)p)); }
__device__ __forceinline__ v4u cat(v2u a, v2u b) { return v4u{a[0], a[1], b[0], b[1]}; }
__device__ __forceinline__ float ex2(float x) { return __builtin_amdgcn_exp2f(x); }
__device__ __forceinline__ float rcp(float x) { return __builtin_amdgcn_rcpf(x); }
__device__ __forceinline__ float gval(unsigned d) { return __uint_as_float(d & 0xFFFFFF00u); }
__device__ __forceinline__ unsigned d4(float f, unsigned tagbyte) { return ((__float_as_uint(f) + 0x80u) & 0xFFFFFF00u) | tagbyte; }
#define SB() __builtin_amdgcn_sched_barrier(0)
#define BAR() do { asm volatile("s_waitcnt lgkmcnt(0)" ::: "memory"); __builtin_amdgcn_s_barrier(); asm volatile("" ::: "memory"); } while (0)
#define FENCE() asm volatile("" ::: "memory")
#define WAITV(n) __builtin_amdgcn_s_waitcnt(0x0F70 | (n))
#define LD16(off, soff) __builtin_amdgcn_raw_buffer_load_b128(rs, (off), (soff), 16)
#define ST16(v, off, soff) __builtin_amdgcn_raw_buffer_store_b128((v), rs, (off), (soff), 16)
#define ST8(v, off, soff) __builtin_amdgcn_raw_buffer_store_b64((v), rs, (off), (soff), 16)
#define TBAD(v) __builtin_amdgcn_bitop3_b32((v)[1], (v)[3], tag, 0x7E)
#define ANY(t) (__builtin_amdgcn_ballot_w64((t) != 0) != 0)
#define DBAD(v) __builtin_amdgcn_bitop3_b32(__builtin_amdgcn_perm((v)[1], (v)[0], 0x0c0c0400u), __builtin_amdgcn_perm((v)[3], (v)[2], 0x04000c0cu), tag, 0x56)
#define EPOCH() asm volatile("" : "+s"(ep0)); const unsigned tag = ((ep0 & 0x7F7F7F7Fu) + 0x01010101u) ^ (ep0 & 0x80808080u)
#define UNTIL(BADEXPR, RELOAD) do { if (ANY(BADEXPR)) { int spins_ = SPIN_CAP; _Pragma("nounroll") do { FENCE(); RELOAD; if (!ANY(BADEXPR)) break; asm volatile("" : "+s"(spins_)); } while (--spins_); } } while (0)

__device__ __forceinline__ void mksel(unsigned (&sel)[8], int l) {
#pragma unroll
  for (int d = 0; d < 4; ++d) { const bool on = ((l & 7) >> 1) == d;
    sel[d] = on ? ((l & 1) ? 0x01000c0cu : 0x0c0c0100u) : 0x0c0c0c0cu; sel[4 + d] = on ? ((l & 1) ? 0x03020c0cu : 0x0c0c0302u) : 0x0c0c0c0cu; }
}
__device__ __forceinline__ v4u diag(unsigned pair, const unsigned (&sel)[8], int hi) {
  return v4u{__builtin_amdgcn_perm(0u, pair, sel[4 * hi]), __builtin_amdgcn_perm(0u, pair, sel[4 * hi + 1]), __builtin_amdgcn_perm(0u, pair, sel[4 * hi + 2]), __builtin_amdgcn_perm(0u, pair, sel[4 * hi + 3])};
}
__device__ __forceinline__ float upr8(float v) { return __builtin_bit_cast(float, __builtin_amdgcn_update_dpp(__builtin_bit_cast(int, v), __builtin_bit_cast(int, v), R8, 0xC, 0xF, false)); }
__device__ __forceinline__ v2u diag_out(v4f d, unsigned tb0, unsigned tb1) {
  const float t0 = upr8(d[0]), t1 = upr8(d[1]), t2 = upr8(d[2]), t3 = upr8(d[3]);
  return v2u{d4(fold32(t0, t2), tb0), d4(fold32(t1, t3), tb1)};
}

__global__ void __launch_bounds__(NTHR, 3) fused_decode(const u16* __restrict__ x, const u16* __restrict__ wq, const u16* __restrict__ w1, const u16* __restrict__ vc, unsigned char* ws,
                                                        const u16* __restrict__ kc, const u16* __restrict__ wo, const u16* __restrict__ w2, const u16* __restrict__ b1,
                                                        const u16* __restrict__ b2, u16* __restrict__ out) {
  __shared__ __attribute__((aligned(128))) unsigned char lds_[L_END];
  LAS unsigned char* L = (LAS unsigned char*)lds_;
  const int b = blockIdx.x, g = b & 7, j = b >> 3, l = threadIdx.x & 63;
  int w = __builtin_amdgcn_readfirstlane(threadIdx.x >> 6);
  { u64 pc; unsigned keep, t_;
    asm volatile("s_cmp_lg_u32 %3, 4\n\ts_cbranch_scc1 1f\n\ts_getpc_b64 %0\n\ts_mov_b32 %1, m0\n\ts_mov_b32 m0, %5\n\tv_lshlrev_b32 %2, 7, %4\n\t"
                 "global_load_lds_dword %2, %0\n\ts_mov_b32 m0, %1\n1:"
                 : "=&s"(pc), "=&s"(keep), "=&v"(t_) : "s"(w), "v"((j * 4 + (l & 3)) & 63), "i"(L_O) : "scc", "memory"); }
  const int kq = j & 3, rb_ = j >> 2, gn = (g + 1) & 7, gp = (g - 1) & 7;
  const int gq = l >> 4, n = l & 15;
  const bool hasw1 = rb_ != 3; const int jw = rb_ == 7 ? j - 16 : j;
  int kvo = (g * SK + (SK - WIN) + j * 128 + (((w - 1) * 2 + ((w - 1) >> 2) + j) & 7) * 16) * 256, l16 = l * 16;
  int vo = (l >> 3) * (16 * 8192) + (l & 7) * 16, so = kq * (128 * 8192) + gn * 1024 + rb_ * 128;
  int vo1 = hasw1 ? (l >> 3) * (8 * FF * 2) + (l & 7) * 16 : OOB, so1 = g * (64 * FF * 2) + jw * 128;
  asm volatile("" : "+s"(kvo), "+s"(so), "+s"(so1));
  asm volatile("s_setprio 3" : "+v"(l16), "+v"(vo), "+v"(vo1));
  asm volatile("" :: "s"(x), "s"(kc), "s"(vc), "s"(wq), "s"(wo), "s"(w1), "s"(b1), "s"(w2), "s"(b2), "s"(out), "s"(ws));
  unsigned ep0 = *(const CAS unsigned*)ws;
  const rsrc_t rs = mk(ws, WS_END);
  v4u vv[4];
  if (w != 0) {
    const rsrc_t rv = mk(vc, 8u * SK * 256);
    __builtin_amdgcn_s_setprio(2);
    __builtin_amdgcn_s_barrier();
    SB();
#pragma unroll
    for (int i = 0; i < 4; ++i) vv[i] = ldw(rv, l16 + i * 1024, kvo);
    SB();
  }
  asm volatile("" : "+s"(w));
  if (__builtin_expect(w == 0, 1)) {
    v4u r16[16], f8[8];
    const v4u xv = *(const GAS v4u*)((const GAS unsigned char*)x + (unsigned)l16);
    { const rsrc_t r = mk(wq, 512 * 4096 * 2), r1 = mk(w1, 512 * FF * 2);
#pragma unroll
      for (int i = 0; i < 16; ++i) r16[i] = ldw(r, vo, so + i * 8192);
#pragma unroll
      for (int i = 0; i < 8; ++i) f8[i] = ldw(r1, vo1, so1 + i * (FF * 2)); }
    SB();
    __builtin_amdgcn_s_barrier();
    SB();
    unsigned sel[8]; mksel(sel, l);
    int gvo = (l & 8) ? OOB : (l & 7) * 32 + (l & 16) + (l >> 5) * 8, xra = L_XN + kq * 256 + (l >> 3) * 32, xrc = L_XN + g * 128 + (l >> 3) * 16;
    asm volatile("" : "+v"(sel[0]), "+v"(sel[1]), "+v"(sel[2]), "+v"(sel[3]), "+v"(sel[4]), "+v"(sel[5]), "+v"(sel[6]), "+v"(sel[7]), "+v"(gvo), "+v"(xra), "+v"(xrc));
    SB();
    float xf[8];
#pragma unroll
    for (int i = 0; i < 4; ++i) { xf[2 * i] = bflo(xv[i]); xf[2 * i + 1] = bfhi(xv[i]); }
    const float mean = sum64(((xf[0] + xf[1]) + (xf[2] + xf[3])) + ((xf[4] + xf[5]) + (xf[6] + xf[7]))) * (1.f / 512);
    float vs = 0.f;
#pragma unroll
    for (int i = 0; i < 8; ++i) { const float d = xf[i] - mean; vs = fmaf(d, d, vs); }
    const float var = sum64(vs) * (1.f / 512);
    const float mb = rb(mean), rsd = rcp(rb(__builtin_amdgcn_sqrtf(rb(rb(var) + 1e-5f))));
    v4u xn;
#pragma unroll
    for (int i = 0; i < 4; ++i) { const unsigned d = pk(xf[2 * i] - mb, xf[2 * i + 1] - mb); xn[i] = pk(bflo(d) * rsd, bfhi(d) * rsd); }
    *(LAS v4u*)(L + L_XN + l * 16) = xn;
    *(LAS float*)(L + L_M + (l & 3) * 4) = -__builtin_inff();
    const v4u xa = *(LAS v4u*)(L + xra), xb = *(LAS v4u*)(L + xra + 16), xc = *(LAS v4u*)(L + xrc);
    EPOCH();
    const unsigned tb0 = (tag >> ((l >> 5) * 16)) & 0xFFu, tb1 = (tag >> ((l >> 5) * 16 + 8)) & 0xFFu;
    { v4u bx[8];
#pragma unroll
      for (int i = 0; i < 8; ++i) bx[i] = diag(xa[i >> 1], sel, i & 1);
      SB();
      v4f d = {0.f, 0.f, 0.f, 0.f};
#pragma unroll
      for (int i = 0; i < 8; ++i) { d = mfma(bx[i], r16[i], d); bx[i] = diag(xb[i >> 1], sel, i & 1); SB(); }
#pragma unroll
      for (int i = 0; i < 8; ++i) d = mfma(bx[i], r16[8 + i], d);
      ST8(diag_out(d, tb0, tb1), gvo, WS_QG + gn * 8192 + kq * 2048 + rb_ * 256);
    }
    { v4f d = {0.f, 0.f, 0.f, 0.f};
#pragma unroll
      for (int i = 0; i < 8; ++i) d = mfma(diag(xc[i >> 1], sel, i & 1), f8[i], d);
      ST8(diag_out(d, tb0, tb1), hasw1 ? gvo : OOB, WS_HG + g * (FF * 4) + jw * 256); }
    const int hr0 = gp * 224 + rb_ * 28, hu = min(l & 7, 6), ocol = (b & 63) * 8 + (l & 3) * 2 + (l >> 5);
    int mlo = (l & 51) == 0 ? (n >> 2) * 512 : OOB, mls = WS_ML + g * 2048 + j * 16, hvo = (l >> 3) * (FF * 4) + hu * 16, hso = WS_HG + hr0 * 4;
    int pvo = (l & 31) * 16, pso = WS_ML + gp * 2048 + (rb_ >> 1) * 512;
    asm volatile("" : "+v"(mlo), "+v"(hvo), "+v"(pvo));
    asm volatile("" : "+s"(mls), "+s"(hso), "+s"(pso));
    const float b1v = bflo(b1[hr0 + hu * 4 + gq]), b2v = bflo(b2[ocol]), xo = bflo(x[ocol]);
    BAR();
    v4u hg = LD16(hvo, hso);
    SB();
    BAR();
    BAR();
    { const v2u mm = *(LAS v2u*)(L + L_MX + l * 8);
      const v4f s0 = *(LAS v4f*)(L + L_S + l * 16), s1 = *(LAS v4f*)(L + L_S + 1024 + l * 16);
      const float m = *(LAS float*)(L + L_M + (n >> 2) * 4);
      float ls = fmaf((s0[0] + s0[1]) + (s0[2] + s0[3]), ex2(uf(mm[0]) - m), ((s1[0] + s1[1]) + (s1[2] + s1[3])) * ex2(uf(mm[1]) - m));
      ls += dpp<X1>(ls); ls += dpp<X2>(ls); ls = fold16(ls, ls); ls = fold32(ls, ls);
      ST16((v4u{__float_as_uint(m), tag, __float_as_uint(ls), tag}), mlo, mls);
    }
    SB();
    {
      if (__builtin_expect(ANY(DBAD(hg)), 0)) UNTIL(DBAD(hg), hg = LD16(hvo, hso));
      float hs = fold16(fold32(gval(hg[0]), gval(hg[2])), fold32(gval(hg[1]), gval(hg[3]))); hs += dpp<R8>(hs);
      const float hp = rb(rb(hs) + b1v);
      const float u2 = 1.5957691216057308f * fmaf(0.044715f * hp * hp, hp, hp);
      const float ge = hp * rcp(1.f + ex2(-u2 * LOG2E));
      *(LAS u16*)(L + L_H + (l & 7) * 8 + gq * 2) = ((l & 7) < 7) ? (u16)pk(ge, ge) : (u16)0;
    }
    __builtin_amdgcn_s_sleep(4);
    v4u pv = LD16(pvo, pso);
    SB();
    BAR();
    {
      UNTIL(TBAD(pv), pv = LD16(pvo, pso));
      const float m = uf(pv[0]);
      const float M = max16(mx_r4(mx_r8(mx_x2(mx_x1(m)))));
      const float e = ex2(m - M);
      float ls = e * uf(pv[2]);
      ls += dpp<X1>(ls); ls += dpp<X2>(ls); ls += dpp<HM>(ls); ls += dpp<R8>(ls); ls = fold16(ls, ls);
      *(LAS float*)(L + L_WG + (l & 31) * 4) = e * rcp(ls);
    }
    BAR();
    if (b < 64) {
      const int fo = WS_FB + b * 4096;
      v4u f0 = LD16(l16, fo), f1 = LD16(l16 + 1024, fo), f2 = LD16(l16 + 2048, fo), f3 = LD16(l16 + 3072, fo);
      UNTIL(TBAD(f0) | TBAD(f1) | TBAD(f2) | TBAD(f3), (f0 = LD16(l16, fo), f1 = LD16(l16 + 1024, fo), f2 = LD16(l16 + 2048, fo), f3 = LD16(l16 + 3072, fo)));
      float t = fold32((uf(f0[0]) + uf(f1[0])) + (uf(f2[0]) + uf(f3[0])), (uf(f0[2]) + uf(f1[2])) + (uf(f2[2]) + uf(f3[2])));
      t = fold16(t, t); t += dpp<R8>(t); t += dpp<R4>(t);
      const float f = rb(rb(t) + b2v);
      float gt = rb(f * rcp(1.f + ex2(-f * LOG2E)));
      int frd = L_S + ((l >> 5) * 320 + (l & 3) * 72 + ((l >> 2) & 7) * 8) * 4;
      int ovo = (l & 28) ? OOB : ocol * 2; float xo_ = xo;
      asm volatile("" : "+v"(gt), "+v"(frd), "+v"(ovo), "+v"(xo_));
      BAR();
      const v4f u0 = *(LAS v4f*)(L + frd), u1 = *(LAS v4f*)(L + frd + 16);
      const v4f u = u0 + u1;
      float v = (u[0] + u[2]) + (u[1] + u[3]);
      v = fold16(v, v); v += dpp<R8>(v); v += dpp<R4>(v);
      const float o = xo_ + rb(rb(v) * gt);
      __builtin_amdgcn_raw_buffer_store_b16((u16)pk(o, o), mk(out, 1024), ovo, 0, 16);
      if (b == 0 && l == 0) __hip_atomic_store((GAS unsigned*)ws, tag, __ATOMIC_RELAXED, __HIP_MEMORY_SCOPE_AGENT);
    }
  } else {
    const int a = w - 1, a3 = a & 3; unsigned ca = 0, cb = 0;
    int obo = (n & 3) == 0 ? (n >> 2) * 16384 + gq * 16 : OOB;
    int sbo = l < 16 ? (n >> 3) * 4096 + (n & 7) * 8 : OOB;
    int obs = WS_OB + g * 65536 + (a3 >> 1) * 8192 + j * 256 + (a & 1) * 128, sbs = WS_SB + (kq * 16 + a * 2) * 4096 + (g * 8 + rb_) * 64;
    int sadr = L_S + (a & 1) * 1024 + gq * 256 + (n & 3) * 64 + (a >> 1) * 16 + (n >> 2) * 4, qadr = L_Q + (n & 3) * QSTR + gq * 64 + (n >> 2) * 16;
    int pso = WS_OB + gp * 65536 + rb_ * 8192 + a3 * 2048, pvo = a >= 4 ? l * 16 : OOB;
    int oadr = L_O + n * 128 + ((gq ^ (n >> 1)) & 7) * 16, owr = L_O + (a3 * 8 + gq) * 128 + ((((n >> 1) ^ ((a3 * 8 + gq) >> 1)) & 7) * 16) + (n & 1) * 8;
    int mad = L_M + (n & 3) * 4, l8 = l * 8, mrd = L_M + (n >> 2) * 4;
    asm volatile("" : "+v"(mad), "+v"(l8), "+v"(mrd));
    int owr2 = (owr ^ 32) + 512, oadr2 = oadr ^ 64;
    int fso = WS_SB + b * 4096 + a * 512, fvo = (l & 31) * 16, fadr = L_S + ((l & 3) * 72 + a * 8 + ((l >> 2) & 7)) * 4;
    int vwr = L_V + (a * 16 + gq) * VSTR + n * 16;
    int qso = WS_QG + g * 8192 + a3 * 512, qvo = a >= 4 ? (l >> 5) * 4096 + (l & 31) * 16 : OOB;
    int mxw = L_MX + gq * 128 + (n & 3) * 32 + a * 4, qwr = L_Q + a3 * QSTR + (l & 31) * 8 + (l >> 5) * 4, kof = (n >> 2) * 1024 + gq * 64 + (n & 3) * 16;
    int wos = (gp * 512 + rb_ * 64 + a3 * 16) * 1024 + kq * 256, w2s = (gp * 224 + rb_ * 28 + a3 * 8) * 1024 + kq * 256, wvo = gq * 1024 + (n ^ (gq * 2)) * 16;
    int w2t = w2s + (a3 == 3 ? 0 : 4096);
    int wl0 = L_W + a3 * 4 * WBLK, wl1 = L_W + (16 + a3 * 2) * WBLK;
    v4u so_ = {(unsigned)(u64)wo, (unsigned)((u64)wo >> 32) & 0xFFFFu, 4096u * 512 * 2, 0x20000u}, s2_ = {(unsigned)(u64)w2, (unsigned)((u64)w2 >> 32) & 0xFFFFu, FF * 512u * 2, 0x20000u};
    asm volatile("" : "+v"(obo), "+v"(sbo), "+v"(sadr), "+v"(qadr), "+v"(oadr), "+v"(owr), "+v"(owr2), "+v"(oadr2), "+v"(vwr), "+v"(fvo), "+v"(fadr), "+v"(qvo), "+v"(mxw), "+v"(qwr), "+v"(kof), "+v"(pvo), "+v"(wvo));
    asm volatile("" : "+s"(obs), "+s"(sbs), "+s"(pso), "+s"(qso), "+s"(fso), "+s"(wos), "+s"(w2s), "+s"(so_), "+s"(s2_), "+s"(wl0), "+s"(wl1), "+s"(w2t));
    const rsrc_t rk = mk(kc, 8u * SK * 256);
    SB();
    v4u kv[4];
#pragma unroll
    for (int i = 0; i < 4; ++i) {
      asm volatile("" : "+v"(vv[i]));
      if (i == 0) {
#pragma unroll
        for (int k = 0; k < 4; ++k) kv[k] = ldw(rk, kof + k * 256, kvo);
      }
      *(LAS v4u*)(L + vwr + i * 4 * VSTR) = vv[i];
      SB();
    }
    EPOCH();
    if (a < 4) WAITV(0);
    BAR();
    v4u q0 = LD16(qvo, qso), q1 = LD16(qvo, qso + 2048);
    v2u vt[16];
    if (a < 4) {
#pragma unroll
      for (int i = 0; i < 16; ++i) vt[i] = trd(L + L_V + (gq * 4 + (n >> 2)) * VSTR + a * 64 + (n & 3) * 8 + (i & 7) * 16 * VSTR + (i >> 3) * 32);
    }
    SB();
    v4u o0 = {0u, tag, 0u, tag}, o1 = o0; u64 lead = 0x000F000F000F000Full;
    asm volatile("" : "+v"(o0), "+v"(o1)); asm volatile("" : "+s"(lead));
    asm volatile("" : "+v"(kv[2]) :: "memory");
    const v4u r0 = LD16(qvo, qso), r1 = LD16(qvo, qso + 2048);
    SB();
    WAITV(2);
    if (a < 4) {
      unsigned keep;
#define DMA1(SRD, SO) "buffer_load_dwordx4 %3, " SRD ", " SO " offen nt lds\n\ts_add_u32 m0, m0, %8\n\ts_add_u32 " SO ", " SO ", 0x1000\n\t"
      asm volatile("s_mov_b32 %0, m0\n\ts_mov_b32 m0, %6\n\ts_nop 0\n\t" DMA1("%4", "%1") DMA1("%4", "%1") DMA1("%4", "%1") DMA1("%4", "%1")
                   "s_mov_b32 m0, %7\n\ts_nop 0\n\t" DMA1("%5", "%2") "buffer_load_dwordx4 %3, %5, %9 offen nt lds\n\t"
                   "s_mov_b32 m0, %0"
                   : "=&s"(keep), "+s"(wos), "+s"(w2s) : "v"(wvo), "s"(so_), "s"(s2_), "s"(wl0), "s"(wl1), "i"(WBLK), "s"(w2t) : "memory", "scc");
    } else {
#define QSUM() do { const v4f u_ = __builtin_bit_cast(v4f, q0 & 0xFFFFFF00u) + __builtin_bit_cast(v4f, q1 & 0xFFFFFF00u); float c_, d_; const float a_ = sw32(u_[0], u_[2], c_), b_ = sw32(u_[1], u_[3], d_); \
        *(LAS unsigned*)(L + qwr) = pk(a_ + c_, b_ + d_); } while (0)
      QSUM();
      SB();
      if (__builtin_expect(ANY(DBAD(q0) | DBAD(q1)), 0)) {
        q0 = r0; q1 = r1;
        UNTIL(DBAD(q0) | DBAD(q1), (q0 = LD16(qvo, qso), q1 = LD16(qvo, qso + 2048)));
        QSUM();
      }
    }
    BAR();
    CK(1);
    {
      const v4u qf = *(LAS v4u*)(L + qadr);
      v4f sc[4];
#pragma unroll
      for (int i = 0; i < 4; ++i) { sc[i] = mfma(kv[i], qf, v4f{0.f, 0.f, 0.f, 0.f}); SB(); }
      float t, m;
#define DADD(src, acc, rot, bank) "v_add_f32_dpp %0, " src ", " acc " row_ror:" rot " row_mask:0xf bank_mask:" bank "\n\t"
      asm volatile("s_nop 4\n\t"
                   DADD("%3", "%2", "12", "0x1") DADD("%6", "%7", "4", "0x2") DADD("%10", "%12", "8", "0x4") DADD("%14", "%17", "12", "0x8")
                   DADD("%4", "%0", "8", "0x1") DADD("%8", "%0", "12", "0x2") DADD("%11", "%0", "4", "0x4") DADD("%15", "%0", "8", "0x8")
                   DADD("%5", "%0", "4", "0x1") DADD("%9", "%0", "8", "0x2") DADD("%13", "%0", "12", "0x4") DADD("%16", "%0", "4", "0x8")
                   "v_cvt_pk_bf16_f32 %0, 0, %0\n\tv_mul_f32 %0, 0x3db504f3, %0\n\tv_cvt_pk_bf16_f32 %0, 0, %0\n\tv_mul_f32 %0, 0x3fb8aa3b, %0\n\ts_nop 1\n\t"
                   "v_max_f32_dpp %1, %0, %0 row_ror:8 row_mask:0xf bank_mask:0xf\n\ts_nop 1\n\t"
                   "v_max_f32_dpp %1, %1, %1 row_ror:4 row_mask:0xf bank_mask:0xf\n\t"
                   "v_sub_f32 %0, %0, %1\n\tv_exp_f32 %0, %0\n\t"
                   "s_mov_b64 exec, %21\n\tds_max_f32 %18, %1\n\tds_write_b32 %19, %1\n\ts_mov_b64 exec, -1\n\tds_write_b32 %20, %0"
                   : "=&v"(t), "=&v"(m)
                   : "v"(sc[0][0]), "v"(sc[0][1]), "v"(sc[0][2]), "v"(sc[0][3]), "v"(sc[1][0]), "v"(sc[1][1]), "v"(sc[1][2]), "v"(sc[1][3]),
                     "v"(sc[2][0]), "v"(sc[2][1]), "v"(sc[2][2]), "v"(sc[2][3]), "v"(sc[3][0]), "v"(sc[3][1]), "v"(sc[3][2]), "v"(sc[3][3]),
                     "v"(mad), "v"(mxw), "v"(sadr), "s"(lead)
                   : "memory");
#pragma unroll
      for (int i = 0; i < 5; ++i) __builtin_amdgcn_s_setprio(2);
    }
    BAR();
    if (a < 4) {
      const v2u mm = *(LAS v2u*)(L + L_MX + l8);
      const float m = *(LAS float*)(L + mrd);
      const v4f s0 = *(LAS v4f*)(L + L_S + l16), s1 = *(LAS v4f*)(L + L_S + 1024 + l16);
      const v4f p0 = s0 * ex2(uf(mm[0]) - m), p1 = s1 * ex2(uf(mm[1]) - m);
      const v4u pb = {pk(p0[0], p0[1]), pk(p0[2], p0[3]), pk(p1[0], p1[1]), pk(p1[2], p1[3])};
      const v4f z = {0.f, 0.f, 0.f, 0.f};
      v4f d0 = mfma(cat(vt[0], vt[1]), pb, z), d1 = mfma(cat(vt[8], vt[9]), pb, z);
      const v4u pb1 = dpp4<0x55>(pb), pb2 = dpp4<0xAA>(pb);
      SB();
      d0 = mfma(cat(vt[2], vt[3]), pb1, d0); d1 = mfma(cat(vt[10], vt[11]), pb1, d1);
      v4u pb3 = dpp4<0xFF>(pb);
      asm volatile("" : "+v"(pb3) : "v"(pb1), "v"(vt[0]), "v"(vt[1]), "v"(vt[8]), "v"(vt[9]));
      SB();
      d0 = mfma(cat(vt[4], vt[5]), pb2, d0); d1 = mfma(cat(vt[12], vt[13]), pb2, d1);
      d0 = mfma(cat(vt[6], vt[7]), pb3, d0); d1 = mfma(cat(vt[14], vt[15]), pb3, d1);
      o0[0] = pk(d0[0], d0[1]); o0[2] = pk(d0[2], d0[3]); o1[0] = pk(d1[0], d1[1]); o1[2] = pk(d1[2], d1[3]);
      ST16(o0, obo, obs);
      ST16(o1, obo + 64, obs);
      CK(2);
      asm volatile("s_waitcnt vmcnt(2)" ::: "memory");
    }
    else __builtin_amdgcn_s_sleep(16);
    SB();
    v4u pv = LD16(pvo, pso), pu = LD16(pvo, pso + 1024);
    BAR();
    if (a >= 4 && ANY(TBAD(pv) | TBAD(pu))) { pv = LD16(pvo, pso); pu = LD16(pvo, pso + 1024); }
    SB();
    v2u wt[4];
    const int wrd = L_W + gq * 2 * WBLK + (n >> 2) * 256 + (a ^ (n >> 2)) * 32 + (n & 3) * 8;
#pragma unroll
    for (int i = 0; i < 4; ++i) wt[i] = trd(L + wrd + ((i >> 1) * 8 + (i & 1)) * WBLK);
    SB();
    WAITV(0);
    asm volatile("" :: "v"(r0), "v"(r1));
    if (a < 4) {
      const v4u hf = *(LAS v4u*)(L + L_H + gq * 16);
      LAS unsigned char* p = L + wrd + 16 * WBLK;
      const v4f z = {0.f, 0.f, 0.f, 0.f};
      const v4f f0 = mfma(hf, cat(trd(p), trd(p + WBLK)), z), f1 = mfma(hf, cat(trd(p + 128), trd(p + 128 + WBLK)), z);
      ST8((v2u{__float_as_uint(gq ? f1[0] : f0[0]), tag}), l < 32 ? gq * 32768 + (n >> 3) * 4096 + (n & 7) * 8 : OOB, sbs + (WS_FB - WS_SB));
    }
    SB();
    if (a >= 4) {
#define OWR() (((LAS unsigned*)(L + owr))[0] = pv[0], ((LAS unsigned*)(L + owr))[1] = pv[2], ((LAS unsigned*)(L + owr2))[0] = pu[0], ((LAS unsigned*)(L + owr2))[1] = pu[2])
      OWR();
      UNTIL(TBAD(pv) | TBAD(pu), (pv = LD16(pvo, pso), pu = LD16(pvo, pso + 1024), OWR()));
    }
    BAR();
    CK(3);
    {
      const v4u of0 = *(LAS v4u*)(L + oadr), of2 = *(LAS v4u*)(L + oadr + 2048), of1 = *(LAS v4u*)(L + oadr2), of3 = *(LAS v4u*)(L + oadr2 + 2048);
      const v4f w0 = *(LAS v4f*)(L + L_WG + gq * 16), w1_ = *(LAS v4f*)(L + L_WG + 64 + gq * 16);
      SB();
      const v4f z = {0.f, 0.f, 0.f, 0.f};
      v4f d0 = mfma(of0, cat(wt[0], wt[1]), z), d1 = mfma(of2, cat(wt[0], wt[1]), z);
      d0 = mfma(of1, cat(wt[2], wt[3]), d0); d1 = mfma(of3, cat(wt[2], wt[3]), d1);
      SB();
      v4f t = d0 * w0;
      asm volatile("" : "+v"(t));
      t += d1 * w1_;
      float e = (t[0] + t[2]) + (t[1] + t[3]);
      e = fold32(e, e); e = fold16(e, e);
      ST8((v2u{__float_as_uint(e), tag}), sbo, sbs);
    }
    CK(4);
    if (b < 64) {
      __builtin_amdgcn_s_sleep(10); if (a == g) __builtin_amdgcn_s_sleep(10);
      v4u sv = LD16(fvo, fso);
#define SWR() (((LAS unsigned*)(L + fadr))[0] = sv[0], ((LAS unsigned*)(L + fadr))[320] = sv[2])
      SWR();
      UNTIL(TBAD(sv), (sv = LD16(fvo, fso), SWR()));
      BAR();
      CK(5);
      if (a == 0) { const unsigned d_ = 10 * (cb - ca), e_ = (unsigned)__builtin_amdgcn_s_memtime(); while ((unsigned)__builtin_amdgcn_s_memtime() - e_ < d_ && d_ < 1000000u) {} }
    }
  }
}

extern "C" void kernel_launch(void* const* d_in, const int* in_sizes, int n_in, void* d_out, int out_size, void* d_ws, size_t ws_size, hipStream_t stream) {
  fused_decode<<<256, NTHR, 0, stream>>>((const u16*)d_in[0], (const u16*)d_in[3], (const u16*)d_in[6], (const u16*)d_in[2], (unsigned char*)d_ws, (const u16*)d_in[1],
                                         (const u16*)d_in[4], (const u16*)d_in[8], (const u16*)d_in[7], (const u16*)d_in[9], (u16*)d_out);
}
```

```cpp
#include <hip/hip_runtime.h>
#include <hip/hip_bf16.h>

typedef unsigned short u16;
typedef unsigned long long u64;
typedef unsigned v4u __attribute__((ext_vector_type(4)));
typedef unsigned v2u __attribute__((ext_vector_type(2)));
typedef float v4f __attribute__((ext_vector_type(4)));
typedef short s16x4 __attribute__((ext_vector_type(4)));
typedef __bf16 bf16x2 __attribute__((ext_vector_type(2)));
typedef __bf16 bf16x8 __attribute__((ext_vector_type(8)));
typedef __amdgpu_buffer_rsrc_t rsrc_t;
#define GAS __attribute__((address_space(1)))
#define CAS __attribute__((address_space(4)))
#define LAS __attribute__((address_space(3)))

constexpr int SK = 32768, WIN = 4096, FF = 1792;
constexpr int NTHR = 576;
constexpr unsigned WS_QG = 4096;
constexpr unsigned WS_HG = WS_QG + 65536;
constexpr unsigned WS_ML = WS_HG + 57344;
constexpr unsigned WS_OB = WS_ML + 16384;
constexpr unsigned WS_SB = WS_OB + 524288;
constexpr unsigned WS_FB = WS_SB + 262144;
constexpr unsigned WS_END = WS_FB + 262144;
constexpr int VSTR = 288, L_V = 0, L_O = 128 * VSTR, L_Q = L_O + 4096, QSTR = 320, L_S = L_Q + 4 * QSTR, L_XN = L_S + 2048, L_MX = L_XN + 1024, L_M = L_MX + 512, L_H = L_M + 32,
              L_WG = L_H + 64, L_W = L_WG + 128, WBLK = 1088, L_END = L_W + 24 * WBLK;
static_assert(L_O % 128 == 0, "the o image is addressed with ^ 32 and ^ 64");
constexpr unsigned SPIN_CAP = 1u << 18;
constexpr float LOG2E = 1.4426950408889634f;
constexpr int OOB = 0x40000000;

__device__ __forceinline__ rsrc_t mk(const void* p, unsigned bytes) { return __builtin_amdgcn_make_buffer_rsrc((void*)p, 0, bytes, 0x00020000); }
__device__ __forceinline__ v4u ldw(rsrc_t r, int voff, int soff) { return __builtin_amdgcn_raw_buffer_load_b128(r, voff, soff, 2); }
__device__ __forceinline__ float uf(unsigned u) { return __uint_as_float(u); }
__device__ __forceinline__ float bflo(unsigned u) { return __uint_as_float(u << 16); }
__device__ __forceinline__ float bfhi(unsigned u) { return __uint_as_float(u & 0xFFFF0000u); }
__device__ __forceinline__ unsigned pk(float lo, float hi) { bf16x2 r = {(__bf16)lo, (__bf16)hi}; return __builtin_bit_cast(unsigned, r); }
typedef float v2f __attribute__((ext_vector_type(2)));
__device__ __forceinline__ float rb(float f) { return __builtin_bit_cast(float, __builtin_convertvector((v2f{0.f, f}), bf16x2)); }
__device__ __forceinline__ v4f mfma(v4u a, v4u b, v4f c) { return __builtin_amdgcn_mfma_f32_16x16x32_bf16(__builtin_bit_cast(bf16x8, a), __builtin_bit_cast(bf16x8, b), c, 0, 0, 0); }
template <int CTRL> __device__ __forceinline__ unsigned dppu(unsigned x) { return (unsigned)__builtin_amdgcn_mov_dpp((int)x, CTRL, 0xf, 0xf, true); }
template <int CTRL> __device__ __forceinline__ float dpp(float x) { return __uint_as_float(dppu<CTRL>(__float_as_uint(x))); }
template <int CTRL> __device__ __forceinline__ v4u dpp4(v4u x) { return v4u{dppu<CTRL>(x[0]), dppu<CTRL>(x[1]), dppu<CTRL>(x[2]), dppu<CTRL>(x[3])}; }
constexpr int X1 = 0xB1, X2 = 0x4E, HM = 0x141, R4 = 0x124, R8 = 0x128;
__device__ __forceinline__ float sw16(float a, float b, float& o) { auto r = __builtin_amdgcn_permlane16_swap(__float_as_uint(a), __float_as_uint(b), false, false); o = uf(r[1]); return uf(r[0]); }
__device__ __forceinline__ float sw32(float a, float b, float& o) { auto r = __builtin_amdgcn_permlane32_swap(__float_as_uint(a), __float_as_uint(b), false, false); o = uf(r[1]); return uf(r[0]); }
__device__ __forceinline__ float fold16(float a, float b) { float o; const float p = sw16(a, b, o); return p + o; }
__device__ __forceinline__ float fold32(float a, float b) { float o; const float p = sw32(a, b, o); return p + o; }
__device__ __forceinline__ float fmx(float a, float b) { return __builtin_elementwise_maximum(a, b); }
__device__ __forceinline__ float max16(float a) { float o; const float p = sw16(a, a, o); return fmx(p, o); }
#define MAXDPP(name, ctl) __device__ __forceinline__ float name(float m) { float r; asm("s_nop 1\n\tv_max_f32_dpp %0, %1, %1 " ctl " row_mask:0xf bank_mask:0xf" : "=v"(r) : "v"(m)); return r; }
MAXDPP(mx_r8, "row_ror:8") MAXDPP(mx_r4, "row_ror:4") MAXDPP(mx_x1, "quad_perm:[1,0,3,2]") MAXDPP(mx_x2, "quad_perm:[2,3,0,1]")
__device__ __forceinline__ float sum64(float v) { v += dpp<X1>(v); v += dpp<X2>(v); v += dpp<HM>(v); v += dpp<R8>(v); v = fold16(v, v); return fold32(v, v); }
__device__ __forceinline__ v2u trd(LAS unsigned char* p) { return __builtin_bit_cast(v2u, __builtin_amdgcn_ds_read_tr16_b64_v4i16((LAS s16x4*)p)); }
__device__ __forceinline__ v4u cat(v2u a, v2u b) { return v4u{a[0], a[1], b[0], b[1]}; }
__device__ __forceinline__ float ex2(float x) { return __builtin_amdgcn_exp2f(x); }
__device__ __forceinline__ float rcp(float x) { return __builtin_amdgcn_rcpf(x); }
__device__ __forceinline__ float gval(unsigned d) { return __uint_as_float(d & 0xFFFFFF00u); }
__device__ __forceinline__ unsigned d4(float f, unsigned tagbyte) { return ((__float_as_uint(f) + 0x80u) & 0xFFFFFF00u) | tagbyte; }
#define SB() __builtin_amdgcn_sched_barrier(0)
#define BAR() do { asm volatile("s_waitcnt lgkmcnt(0)" ::: "memory"); __builtin_amdgcn_s_barrier(); asm volatile("" ::: "memory"); } while (0)
#define FENCE() asm volatile("" ::: "memory")
#define WAITV(n) __builtin_amdgcn_s_waitcnt(0x0F70 | (n))
#define LD16(off, soff) __builtin_amdgcn_raw_buffer_load_b128(rs, (off), (soff), 16)
#define ST16(v, off, soff) __builtin_amdgcn_raw_buffer_store_b128((v), rs, (off), (soff), 16)
#define ST8(v, off, soff) __builtin_amdgcn_raw_buffer_store_b64((v), rs, (off), (soff), 16)
#define TBAD(v) __builtin_amdgcn_bitop3_b32((v)[1], (v)[3], tag, 0x7E)
#define ANY(t) (__builtin_amdgcn_ballot_w64((t) != 0) != 0)
#define DBAD(v) __builtin_amdgcn_bitop3_b32(__builtin_amdgcn_perm((v)[1], (v)[0], 0x0c0c0400u), __builtin_amdgcn_perm((v)[3], (v)[2], 0x04000c0cu), tag, 0x56)
#define EPOCH() asm volatile("" : "+s"(ep0)); const unsigned tag = ((ep0 & 0x7F7F7F7Fu) + 0x01010101u) ^ (ep0 & 0x80808080u)
#define UNTIL(BADEXPR, RELOAD) do { if (ANY(BADEXPR)) { int spins_ = SPIN_CAP; _Pragma("nounroll") do { FENCE(); RELOAD; if (!ANY(BADEXPR)) break; asm volatile("" : "+s"(spins_)); } while (--spins_); } } while (0)

__device__ __forceinline__ void mksel(unsigned (&sel)[8], int l) {
#pragma unroll
  for (int d = 0; d < 4; ++d) { const bool on = ((l & 7) >> 1) == d;
    sel[d] = on ? ((l & 1) ? 0x01000c0cu : 0x0c0c0100u) : 0x0c0c0c0cu; sel[4 + d] = on ? ((l & 1) ? 0x03020c0cu : 0x0c0c0302u) : 0x0c0c0c0cu; }
}
__device__ __forceinline__ v4u diag(unsigned pair, const unsigned (&sel)[8], int hi) {
  return v4u{__builtin_amdgcn_perm(0u, pair, sel[4 * hi]), __builtin_amdgcn_perm(0u, pair, sel[4 * hi + 1]), __builtin_amdgcn_perm(0u, pair, sel[4 * hi + 2]), __builtin_amdgcn_perm(0u, pair, sel[4 * hi + 3])};
}
__device__ __forceinline__ float upr8(float v) { return __builtin_bit_cast(float, __builtin_amdgcn_update_dpp(__builtin_bit_cast(int, v), __builtin_bit_cast(int, v), R8, 0xC, 0xF, false)); }
__device__ __forceinline__ v2u diag_out(v4f d, unsigned tb0, unsigned tb1) {
  const float t0 = upr8(d[0]), t1 = upr8(d[1]), t2 = upr8(d[2]), t3 = upr8(d[3]);
  return v2u{d4(fold32(t0, t2), tb0), d4(fold32(t1, t3), tb1)};
}

__global__ void __launch_bounds__(NTHR, 3) fused_decode(const u16* __restrict__ x, const u16* __restrict__ wq, const u16* __restrict__ w1, const u16* __restrict__ vc, unsigned char* ws,
                                                        const u16* __restrict__ kc, const u16* __restrict__ wo, const u16* __restrict__ w2, const u16* __restrict__ b1,
                                                        const u16* __restrict__ b2, u16* __restrict__ out) {
  __shared__ __attribute__((aligned(128))) unsigned char lds_[L_END];
  LAS unsigned char* L = (LAS unsigned char*)lds_;
  const int b = blockIdx.x, g = b & 7, j = b >> 3, l = threadIdx.x & 63;
  int w = __builtin_amdgcn_readfirstlane(threadIdx.x >> 6);
  { u64 pc; unsigned keep, t_;
    asm volatile("s_cmp_lg_u32 %3, 4\n\ts_cbranch_scc1 1f\n\ts_getpc_b64 %0\n\ts_mov_b32 %1, m0\n\ts_mov_b32 m0, %5\n\tv_lshlrev_b32 %2, 7, %4\n\t"
                 "global_load_lds_dword %2, %0\n\ts_mov_b32 m0, %1\n1:"
                 : "=&s"(pc), "=&s"(keep), "=&v"(t_) : "s"(w), "v"((j * 4 + (l & 3)) & 63), "i"(L_O) : "scc", "memory"); }
  const int kq = j & 3, rb_ = j >> 2, gn = (g + 1) & 7, gp = (g - 1) & 7;
  const int gq = l >> 4, n = l & 15;
  const bool hasw1 = rb_ != 3; const int jw = rb_ == 7 ? j - 16 : j;
  int kvo = (g * SK + (SK - WIN) + j * 128 + (((w - 1) * 2 + ((w - 1) >> 2) + j) & 7) * 16) * 256, l16 = l * 16;
  int vo = (l >> 3) * (16 * 8192) + (l & 7) * 16, so = kq * (128 * 8192) + gn * 1024 + rb_ * 128;
  int vo1 = hasw1 ? (l >> 3) * (8 * FF * 2) + (l & 7) * 16 : OOB, so1 = g * (64 * FF * 2) + jw * 128;
  asm volatile("" : "+s"(kvo), "+s"(so), "+s"(so1));
  asm volatile("s_setprio 3" : "+v"(l16), "+v"(vo), "+v"(vo1));
  asm volatile("" :: "s"(x), "s"(kc), "s"(vc), "s"(wq), "s"(wo), "s"(w1), "s"(b1), "s"(w2), "s"(b2), "s"(out), "s"(ws));
  unsigned ep0 = *(const CAS unsigned*)ws;
  const rsrc_t rs = mk(ws, WS_END);
  v4u vv[4];
  if (w != 0) {
    const rsrc_t rv = mk(vc, 8u * SK * 256);
    __builtin_amdgcn_s_setprio(2);
    __builtin_amdgcn_s_barrier();
    SB();
#pragma unroll
    for (int i = 0; i < 4; ++i) vv[i] = ldw(rv, l16 + i * 1024, kvo);
    SB();
  }
  asm volatile("" : "+s"(w));
  if (__builtin_expect(w == 0, 1)) {
    v4u r16[16], f8[8];
    const v4u xv = *(const GAS v4u*)((const GAS unsigned char*)x + (unsigned)l16);
    { const rsrc_t r = mk(wq, 512 * 4096 * 2), r1 = mk(w1, 512 * FF * 2);
#pragma unroll
      for (int i = 0; i < 16; ++i) r16[i] = ldw(r, vo, so + i * 8192);
#pragma unroll
      for (int i = 0; i < 8; ++i) f8[i] = ldw(r1, vo1, so1 + i * (FF * 2)); }
    SB();
    __builtin_amdgcn_s_barrier();
    SB();
    unsigned sel[8]; mksel(sel, l);
    int gvo = (l & 8) ? OOB : (l & 7) * 32 + (l & 16) + (l >> 5) * 8, xra = L_XN + kq * 256 + (l >> 3) * 32, xrc = L_XN + g * 128 + (l >> 3) * 16;
    asm volatile("" : "+v"(sel[0]), "+v"(sel[1]), "+v"(sel[2]), "+v"(sel[3]), "+v"(sel[4]), "+v"(sel[5]), "+v"(sel[6]), "+v"(sel[7]), "+v"(gvo), "+v"(xra), "+v"(xrc));
    SB();
    float xf[8];
#pragma unroll
    for (int i = 0; i < 4; ++i) { xf[2 * i] = bflo(xv[i]); xf[2 * i + 1] = bfhi(xv[i]); }
    const float mean = sum64(((xf[0] + xf[1]) + (xf[2] + xf[3])) + ((xf[4] + xf[5]) + (xf[6] + xf[7]))) * (1.f / 512);
    float vs = 0.f;
#pragma unroll
    for (int i = 0; i < 8; ++i) { const float d = xf[i] - mean; vs = fmaf(d, d, vs); }
    const float var = sum64(vs) * (1.f / 512);
    const float mb = rb(mean), rsd = rcp(rb(__builtin_amdgcn_sqrtf(rb(rb(var) + 1e-5f))));
    v4u xn;
#pragma unroll
    for (int i = 0; i < 4; ++i) { const unsigned d = pk(xf[2 * i] - mb, xf[2 * i + 1] - mb); xn[i] = pk(bflo(d) * rsd, bfhi(d) * rsd); }
    *(LAS v4u*)(L + L_XN + l * 16) = xn;
    *(LAS float*)(L + L_M + (l & 3) * 4) = -__builtin_inff();
    const v4u xa = *(LAS v4u*)(L + xra), xb = *(LAS v4u*)(L + xra + 16), xc = *(LAS v4u*)(L + xrc);
    EPOCH();
    const unsigned tb0 = (tag >> ((l >> 5) * 16)) & 0xFFu, tb1 = (tag >> ((l >> 5) * 16 + 8)) & 0xFFu;
    { v4u bx[8];
#pragma unroll
      for (int i = 0; i < 8; ++i) bx[i] = diag(xa[i >> 1], sel, i & 1);
      SB();
      v4f d = {0.f, 0.f, 0.f, 0.f};
#pragma unroll
      for (int i = 0; i < 8; ++i) { d = mfma(bx[i], r16[i], d); bx[i] = diag(xb[i >> 1], sel, i & 1); SB(); }
#pragma unroll
      for (int i = 0; i < 8; ++i) d = mfma(bx[i], r16[8 + i], d);
      ST8(diag_out(d, tb0, tb1), gvo, WS_QG + gn * 8192 + kq * 2048 + rb_ * 256);
    }
    { v4f d = {0.f, 0.f, 0.f, 0.f};
#pragma unroll
      for (int i = 0; i < 8; ++i) d = mfma(diag(xc[i >> 1], sel, i & 1), f8[i], d);
      ST8(diag_out(d, tb0, tb1), hasw1 ? gvo : OOB, WS_HG + g * (FF * 4) + jw * 256); }
    const int hr0 = gp * 224 + rb_ * 28, hu = min(l & 7, 6), ocol = (b & 63) * 8 + (l & 3) * 2 + (l >> 5);
    int mlo = (l & 51) == 0 ? (n >> 2) * 512 : OOB, mls = WS_ML + g * 2048 + j * 16, hvo = (l >> 3) * (FF * 4) + hu * 16, hso = WS_HG + hr0 * 4;
    int pvo = (l & 31) * 16, pso = WS_ML + gp * 2048 + (rb_ >> 1) * 512;
    asm volatile("" : "+v"(mlo), "+v"(hvo), "+v"(pvo));
    asm volatile("" : "+s"(mls), "+s"(hso), "+s"(pso));
    const float b1v = bflo(b1[hr0 + hu * 4 + gq]), b2v = bflo(b2[ocol]), xo = bflo(x[ocol]);
    BAR();
    v4u hg = LD16(hvo, hso);
    SB();
    BAR();
    BAR();
    { const v2u mm = *(LAS v2u*)(L + L_MX + l * 8);
      const v4f s0 = *(LAS v4f*)(L + L_S + l * 16), s1 = *(LAS v4f*)(L + L_S + 1024 + l * 16);
      const float m = *(LAS float*)(L + L_M + (n >> 2) * 4);
      float ls = fmaf((s0[0] + s0[1]) + (s0[2] + s0[3]), ex2(uf(mm[0]) - m), ((s1[0] + s1[1]) + (s1[2] + s1[3])) * ex2(uf(mm[1]) - m));
      ls += dpp<X1>(ls); ls += dpp<X2>(ls); ls = fold16(ls, ls); ls = fold32(ls, ls);
      ST16((v4u{__float_as_uint(m), tag, __float_as_uint(ls), tag}), mlo, mls);
    }
    SB();
    {
      if (__builtin_expect(ANY(DBAD(hg)), 0)) UNTIL(DBAD(hg), hg = LD16(hvo, hso));
      float hs = fold16(fold32(gval(hg[0]), gval(hg[2])), fold32(gval(hg[1]), gval(hg[3]))); hs += dpp<R8>(hs);
      const float hp = rb(rb(hs) + b1v);
      const float u2 = 1.5957691216057308f * fmaf(0.044715f * hp * hp, hp, hp);
      const float ge = hp * rcp(1.f + ex2(-u2 * LOG2E));
      *(LAS u16*)(L + L_H + (l & 7) * 8 + gq * 2) = ((l & 7) < 7) ? (u16)pk(ge, ge) : (u16)0;
    }
    __builtin_amdgcn_s_sleep(4);
    v4u pv = LD16(pvo, pso);
    SB();
    BAR();
    {
      UNTIL(TBAD(pv), pv = LD16(pvo, pso));
      const float m = uf(pv[0]);
      const float M = max16(mx_r4(mx_r8(mx_x2(mx_x1(m)))));
      const float e = ex2(m - M);
      float ls = e * uf(pv[2]);
      ls += dpp<X1>(ls); ls += dpp<X2>(ls); ls += dpp<HM>(ls); ls += dpp<R8>(ls); ls = fold16(ls, ls);
      *(LAS float*)(L + L_WG + (l & 31) * 4) = e * rcp(ls);
    }
    BAR();
    if (b < 64) {
      const int fo = WS_FB + b * 4096;
      v4u f0 = LD16(l16, fo), f1 = LD16(l16 + 1024, fo), f2 = LD16(l16 + 2048, fo), f3 = LD16(l16 + 3072, fo);
      UNTIL(TBAD(f0) | TBAD(f1) | TBAD(f2) | TBAD(f3), (f0 = LD16(l16, fo), f1 = LD16(l16 + 1024, fo), f2 = LD16(l16 + 2048, fo), f3 = LD16(l16 + 3072, fo)));
      float t = fold32((uf(f0[0]) + uf(f1[0])) + (uf(f2[0]) + uf(f3[0])), (uf(f0[2]) + uf(f1[2])) + (uf(f2[2]) + uf(f3[2])));
      t = fold16(t, t); t += dpp<R8>(t); t += dpp<R4>(t);
      const float f = rb(rb(t) + b2v);
      float gt = rb(f * rcp(1.f + ex2(-f * LOG2E)));
      int frd = L_S + ((l >> 5) * 320 + (l & 3) * 72 + ((l >> 2) & 7) * 8) * 4;
      int ovo = (l & 28) ? OOB : ocol * 2; float xo_ = xo;
      asm volatile("" : "+v"(gt), "+v"(frd), "+v"(ovo), "+v"(xo_));
      BAR();
      const v4f u0 = *(LAS v4f*)(L + frd), u1 = *(LAS v4f*)(L + frd + 16);
      const v4f u = u0 + u1;
      float v = (u[0] + u[2]) + (u[1] + u[3]);
      v = fold16(v, v); v += dpp<R8>(v); v += dpp<R4>(v);
      const float o = xo_ + rb(rb(v) * gt);
      __builtin_amdgcn_raw_buffer_store_b16((u16)pk(o, o), mk(out, 1024), ovo, 0, 16);
      if (b == 0 && l == 0) __hip_atomic_store((GAS unsigned*)ws, tag, __ATOMIC_RELAXED, __HIP_MEMORY_SCOPE_AGENT);
    }
  } else {
    const int a = w - 1, a3 = a & 3;
    int obo = (n & 3) == 0 ? (n >> 2) * 16384 + gq * 16 : OOB;
    int sbo = l < 16 ? (n >> 3) * 4096 + (n & 7) * 8 : OOB;
    int obs = WS_OB + g * 65536 + (a3 >> 1) * 8192 + j * 256 + (a & 1) * 128, sbs = WS_SB + (kq * 16 + a * 2) * 4096 + (g * 8 + rb_) * 64;
    int sadr = L_S + (a & 1) * 1024 + gq * 256 + (n & 3) * 64 + (a >> 1) * 16 + (n >> 2) * 4, qadr = L_Q + (n & 3) * QSTR + gq * 64 + (n >> 2) * 16;
    int pso = WS_OB + gp * 65536 + rb_ * 8192 + a3 * 2048, pvo = a >= 4 ? l * 16 : OOB;
    int oadr = L_O + n * 128 + ((gq ^ (n >> 1)) & 7) * 16, owr = L_O + (a3 * 8 + gq) * 128 + ((((n >> 1) ^ ((a3 * 8 + gq) >> 1)) & 7) * 16) + (n & 1) * 8;
    int mad = L_M + (n & 3) * 4, l8 = l * 8, mrd = L_M + (n >> 2) * 4;
    asm volatile("" : "+v"(mad), "+v"(l8), "+v"(mrd));
    int owr2 = (owr ^ 32) + 512, oadr2 = oadr ^ 64;
    int fso = WS_SB + b * 4096 + a * 512, fvo = (l & 31) * 16, fadr = L_S + ((l & 3) * 72 + a * 8 + ((l >> 2) & 7)) * 4;
    int vwr = L_V + (a * 16 + gq) * VSTR + n * 16;
    int qso = WS_QG + g * 8192 + a3 * 512, qvo = a >= 4 ? (l >> 5) * 4096 + (l & 31) * 16 : OOB;
    int mxw = L_MX + gq * 128 + (n & 3) * 32 + a * 4, qwr = L_Q + a3 * QSTR + (l & 31) * 8 + (l >> 5) * 4, kof = (n >> 2) * 1024 + gq * 64 + (n & 3) * 16;
    int wos = (gp * 512 + rb_ * 64 + a3 * 16) * 1024 + kq * 256, w2s = (gp * 224 + rb_ * 28 + a3 * 8) * 1024 + kq * 256, wvo = gq * 1024 + (n ^ (gq * 2)) * 16;
    int w2t = w2s + (a3 == 3 ? 0 : 4096);
    int wl0 = L_W + a3 * 4 * WBLK, wl1 = L_W + (16 + a3 * 2) * WBLK;
    v4u so_ = {(unsigned)(u64)wo, (unsigned)((u64)wo >> 32) & 0xFFFFu, 4096u * 512 * 2, 0x20000u}, s2_ = {(unsigned)(u64)w2, (unsigned)((u64)w2 >> 32) & 0xFFFFu, FF * 512u * 2, 0x20000u};
    asm volatile("" : "+v"(obo), "+v"(sbo), "+v"(sadr), "+v"(qadr), "+v"(oadr), "+v"(owr), "+v"(owr2), "+v"(oadr2), "+v"(vwr), "+v"(fvo), "+v"(fadr), "+v"(qvo), "+v"(mxw), "+v"(qwr), "+v"(kof), "+v"(pvo), "+v"(wvo));
    asm volatile("" : "+s"(obs), "+s"(sbs), "+s"(pso), "+s"(qso), "+s"(fso), "+s"(wos), "+s"(w2s), "+s"(so_), "+s"(s2_), "+s"(wl0), "+s"(wl1), "+s"(w2t));
    const rsrc_t rk = mk(kc, 8u * SK * 256);
    SB();
    v4u kv[4];
#pragma unroll
    for (int i = 0; i < 4; ++i) {
      asm volatile("" : "+v"(vv[i]));
      if (i == 0) {
#pragma unroll
        for (int k = 0; k < 4; ++k) kv[k] = ldw(rk, kof + k * 256, kvo);
      }
      *(LAS v4u*)(L + vwr + i * 4 * VSTR) = vv[i];
      SB();
    }
    EPOCH();
    if (a < 4) WAITV(0);
    BAR();
    v4u q0 = LD16(qvo, qso), q1 = LD16(qvo, qso + 2048);
    v2u vt[16];
    if (a < 4) {
#pragma unroll
      for (int i = 0; i < 16; ++i) vt[i] = trd(L + L_V + (gq * 4 + (n >> 2)) * VSTR + a * 64 + (n & 3) * 8 + (i & 7) * 16 * VSTR + (i >> 3) * 32);
    }
    SB();
    v4u o0 = {0u, tag, 0u, tag}, o1 = o0; u64 lead = 0x000F000F000F000Full;
    asm volatile("" : "+v"(o0), "+v"(o1)); asm volatile("" : "+s"(lead));
    asm volatile("" : "+v"(kv[2]) :: "memory");
    const v4u r0 = LD16(qvo, qso), r1 = LD16(qvo, qso + 2048);
    SB();
    WAITV(2);
    if (a < 4) {
      unsigned keep;
#define DMA1(SRD, SO) "buffer_load_dwordx4 %3, " SRD ", " SO " offen nt lds\n\ts_add_u32 m0, m0, %8\n\ts_add_u32 " SO ", " SO ", 0x1000\n\t"
      asm volatile("s_mov_b32 %0, m0\n\ts_mov_b32 m0, %6\n\ts_nop 0\n\t" DMA1("%4", "%1") DMA1("%4", "%1") DMA1("%4", "%1") DMA1("%4", "%1")
                   "s_mov_b32 m0, %7\n\ts_nop 0\n\t" DMA1("%5", "%2") "buffer_load_dwordx4 %3, %5, %9 offen nt lds\n\t"
                   "s_mov_b32 m0, %0"
                   : "=&s"(keep), "+s"(wos), "+s"(w2s) : "v"(wvo), "s"(so_), "s"(s2_), "s"(wl0), "s"(wl1), "i"(WBLK), "s"(w2t) : "memory", "scc");
    } else {
#define QSUM() do { const v4f u_ = __builtin_bit_cast(v4f, q0 & 0xFFFFFF00u) + __builtin_bit_cast(v4f, q1 & 0xFFFFFF00u); float c_, d_; const float a_ = sw32(u_[0], u_[2], c_), b_ = sw32(u_[1], u_[3], d_); \
        *(LAS unsigned*)(L + qwr) = pk(a_ + c_, b_ + d_); } while (0)
      QSUM();
      SB();
      if (__builtin_expect(ANY(DBAD(q0) | DBAD(q1)), 0)) {
        q0 = r0; q1 = r1;
        UNTIL(DBAD(q0) | DBAD(q1), (q0 = LD16(qvo, qso), q1 = LD16(qvo, qso + 2048)));
        QSUM();
      }
    }
    BAR();
    {
      const v4u qf = *(LAS v4u*)(L + qadr);
      v4f sc[4];
#pragma unroll
      for (int i = 0; i < 4; ++i) { sc[i] = mfma(kv[i], qf, v4f{0.f, 0.f, 0.f, 0.f}); SB(); }
      float t, m;
#define DADD(src, acc, rot, bank) "v_add_f32_dpp %0, " src ", " acc " row_ror:" rot " row_mask:0xf bank_mask:" bank "\n\t"
      asm volatile("s_nop 4\n\t"
                   DADD("%3", "%2", "12", "0x1") DADD("%6", "%7", "4", "0x2") DADD("%10", "%12", "8", "0x4") DADD("%14", "%17", "12", "0x8")
                   DADD("%4", "%0", "8", "0x1") DADD("%8", "%0", "12", "0x2") DADD("%11", "%0", "4", "0x4") DADD("%15", "%0", "8", "0x8")
                   DADD("%5", "%0", "4", "0x1") DADD("%9", "%0", "8", "0x2") DADD("%13", "%0", "12", "0x4") DADD("%16", "%0", "4", "0x8")
                   "v_cvt_pk_bf16_f32 %0, 0, %0\n\tv_mul_f32 %0, 0x3db504f3, %0\n\tv_cvt_pk_bf16_f32 %0, 0, %0\n\tv_mul_f32 %0, 0x3fb8aa3b, %0\n\ts_nop 1\n\t"
                   "v_max_f32_dpp %1, %0, %0 row_ror:8 row_mask:0xf bank_mask:0xf\n\ts_nop 1\n\t"
                   "v_max_f32_dpp %1, %1, %1 row_ror:4 row_mask:0xf bank_mask:0xf\n\t"
                   "v_sub_f32 %0, %0, %1\n\tv_exp_f32 %0, %0\n\t"
                   "s_mov_b64 exec, %21\n\tds_max_f32 %18, %1\n\tds_write_b32 %19, %1\n\ts_mov_b64 exec, -1\n\tds_write_b32 %20, %0"
                   : "=&v"(t), "=&v"(m)
                   : "v"(sc[0][0]), "v"(sc[0][1]), "v"(sc[0][2]), "v"(sc[0][3]), "v"(sc[1][0]), "v"(sc[1][1]), "v"(sc[1][2]), "v"(sc[1][3]),
                     "v"(sc[2][0]), "v"(sc[2][1]), "v"(sc[2][2]), "v"(sc[2][3]), "v"(sc[3][0]), "v"(sc[3][1]), "v"(sc[3][2]), "v"(sc[3][3]),
                     "v"(mad), "v"(mxw), "v"(sadr), "s"(lead)
                   : "memory");
#pragma unroll
      for (int i = 0; i < 5; ++i) __builtin_amdgcn_s_setprio(2);
    }
    BAR();
    if (a < 4) {
      const v2u mm = *(LAS v2u*)(L + L_MX + l8);
      const float m = *(LAS float*)(L + mrd);
      const v4f s0 = *(LAS v4f*)(L + L_S + l16), s1 = *(LAS v4f*)(L + L_S + 1024 + l16);
      const v4f p0 = s0 * ex2(uf(mm[0]) - m), p1 = s1 * ex2(uf(mm[1]) - m);
      const v4u pb = {pk(p0[0], p0[1]), pk(p0[2], p0[3]), pk(p1[0], p1[1]), pk(p1[2], p1[3])};
      const v4f z = {0.f, 0.f, 0.f, 0.f};
      v4f d0 = mfma(cat(vt[0], vt[1]), pb, z), d1 = mfma(cat(vt[8], vt[9]), pb, z);
      const v4u pb1 = dpp4<0x55>(pb), pb2 = dpp4<0xAA>(pb);
      SB();
      d0 = mfma(cat(vt[2], vt[3]), pb1, d0); d1 = mfma(cat(vt[10], vt[11]), pb1, d1);
      v4u pb3 = dpp4<0xFF>(pb);
      asm volatile("" : "+v"(pb3) : "v"(pb1), "v"(vt[0]), "v"(vt[1]), "v"(vt[8]), "v"(vt[9]));
      SB();
      d0 = mfma(cat(vt[4], vt[5]), pb2, d0); d1 = mfma(cat(vt[12], vt[13]), pb2, d1);
      d0 = mfma(cat(vt[6], vt[7]), pb3, d0); d1 = mfma(cat(vt[14], vt[15]), pb3, d1);
      o0[0] = pk(d0[0], d0[1]); o0[2] = pk(d0[2], d0[3]); o1[0] = pk(d1[0], d1[1]); o1[2] = pk(d1[2], d1[3]);
      ST16(o0, obo, obs);
      ST16(o1, obo + 64, obs);
      asm volatile("s_waitcnt vmcnt(2)" ::: "memory");
    }
    else __builtin_amdgcn_s_sleep(16);
    SB();
    v4u pv = LD16(pvo, pso), pu = LD16(pvo, pso + 1024);
    BAR();
    if (a >= 4 && ANY(TBAD(pv) | TBAD(pu))) { pv = LD16(pvo, pso); pu = LD16(pvo, pso + 1024); }
    SB();
    v2u wt[4];
    const int wrd = L_W + gq * 2 * WBLK + (n >> 2) * 256 + (a ^ (n >> 2)) * 32 + (n & 3) * 8;
#pragma unroll
    for (int i = 0; i < 4; ++i) wt[i] = trd(L + wrd + ((i >> 1) * 8 + (i & 1)) * WBLK);
    SB();
    WAITV(0);
    asm volatile("" :: "v"(r0), "v"(r1));
    if (a < 4) {
      const v4u hf = *(LAS v4u*)(L + L_H + gq * 16);
      LAS unsigned char* p = L + wrd + 16 * WBLK;
      const v4f z = {0.f, 0.f, 0.f, 0.f};
      const v4f f0 = mfma(hf, cat(trd(p), trd(p + WBLK)), z), f1 = mfma(hf, cat(trd(p + 128), trd(p + 128 + WBLK)), z);
      ST8((v2u{__float_as_uint(gq ? f1[0] : f0[0]), tag}), l < 32 ? gq * 32768 + (n >> 3) * 4096 + (n & 7) * 8 : OOB, sbs + (WS_FB - WS_SB));
    }
    SB();
    if (a >= 4) {
#define OWR() (((LAS unsigned*)(L + owr))[0] = pv[0], ((LAS unsigned*)(L + owr))[1] = pv[2], ((LAS unsigned*)(L + owr2))[0] = pu[0], ((LAS unsigned*)(L + owr2))[1] = pu[2])
      OWR();
      UNTIL(TBAD(pv) | TBAD(pu), (pv = LD16(pvo, pso), pu = LD16(pvo, pso + 1024), OWR()));
    }
    BAR();
    {
      const v4u of0 = *(LAS v4u*)(L + oadr), of2 = *(LAS v4u*)(L + oadr + 2048), of1 = *(LAS v4u*)(L + oadr2), of3 = *(LAS v4u*)(L + oadr2 + 2048);
      const v4f w0 = *(LAS v4f*)(L + L_WG + gq * 16), w1_ = *(LAS v4f*)(L + L_WG + 64 + gq * 16);
      SB();
      const v4f z = {0.f, 0.f, 0.f, 0.f};
      v4f d0 = mfma(of0, cat(wt[0], wt[1]), z), d1 = mfma(of2, cat(wt[0], wt[1]), z);
      d0 = mfma(of1, cat(wt[2], wt[3]), d0); d1 = mfma(of3, cat(wt[2], wt[3]), d1);
      SB();
      v4f t = d0 * w0;
      asm volatile("" : "+v"(t));
      t += d1 * w1_;
      float e = (t[0] + t[2]) + (t[1] + t[3]);
      e = fold32(e, e); e = fold16(e, e);
      ST8((v2u{__float_as_uint(e), tag}), sbo, sbs);
    }
    if (b < 64) {
      __builtin_amdgcn_s_sleep(10); if (a == g) __builtin_amdgcn_s_sleep(10);
      v4u sv = LD16(fvo, fso);
#define SWR() (((LAS unsigned*)(L + fadr))[0] = sv[0], ((LAS unsigned*)(L + fadr))[320] = sv[2])
      SWR();
      UNTIL(TBAD(sv), (sv = LD16(fvo, fso), SWR()));
      BAR();
    }
  }
}

extern "C" void kernel_launch(void* const* d_in, const int* in_sizes, int n_in, void* d_out, int out_size, void* d_ws, size_t ws_size, hipStream_t stream) {
  fused_decode<<<256, NTHR, 0, stream>>>((const u16*)d_in[0], (const u16*)d_in[3], (const u16*)d_in[6], (const u16*)d_in[2], (unsigned char*)d_ws, (const u16*)d_in[1],
                                         (const u16*)d_in[4], (const u16*)d_in[8], (const u16*)d_in[7], (const u16*)d_in[9], (u16*)d_out);
}
```

```cpp
#include <hip/hip_runtime.h>
#include <hip/hip_bf16.h>

typedef unsigned short u16;
typedef unsigned long long u64;
typedef unsigned v4u __attribute__((ext_vector_type(4)));
typedef unsigned v2u __attribute__((ext_vector_type(2)));
typedef float v4f __attribute__((ext_vector_type(4)));
typedef short s16x4 __attribute__((ext_vector_type(4)));
typedef __bf16 bf16x2 __attribute__((ext_vector_type(2)));
typedef __bf16 bf16x8 __attribute__((ext_vector_type(8)));
typedef __amdgpu_buffer_rsrc_t rsrc_t;
#define GAS __attribute__((address_space(1)))
#define CAS __attribute__((address_space(4)))
#define LAS __attribute__((address_space(3)))

constexpr int SK = 32768, WIN = 4096, FF = 1792;
constexpr int NTHR = 576;
constexpr unsigned WS_QG = 4096;
constexpr unsigned WS_HG = WS_QG + 65536;
constexpr unsigned WS_ML = WS_HG + 57344;
constexpr unsigned WS_OB = WS_ML + 16384;
constexpr unsigned WS_SB = WS_OB + 524288;
constexpr unsigned WS_FB = WS_SB + 524288;
constexpr unsigned WS_END = WS_FB + 262144;
constexpr int VSTR = 288, L_V = 0, L_O = 128 * VSTR, L_Q = L_O + 4096, QSTR = 320, L_S = L_Q + 4 * QSTR, L_XN = L_S + 2048, L_MX = L_XN + 1024, L_M = L_MX + 512, L_H = L_M + 32,
              L_WG = L_H + 64, L_W = L_WG + 128, WBLK = 1088, L_END = L_W + 24 * WBLK;
constexpr unsigned SPIN_CAP = 1u << 18;
constexpr float LOG2E = 1.4426950408889634f;
constexpr int OOB = 0x40000000;

__device__ __forceinline__ rsrc_t mk(const void* p, unsigned bytes) { return __builtin_amdgcn_make_buffer_rsrc((void*)p, 0, bytes, 0x00020000); }
__device__ __forceinline__ v4u ldw(rsrc_t r, int voff, int soff) { return __builtin_amdgcn_raw_buffer_load_b128(r, voff, soff, 2); }
__device__ __forceinline__ float uf(unsigned u) { return __uint_as_float(u); }
__device__ __forceinline__ float bflo(unsigned u) { return __uint_as_float(u << 16); }
__device__ __forceinline__ float bfhi(unsigned u) { return __uint_as_float(u & 0xFFFF0000u); }
__device__ __forceinline__ unsigned pk(float lo, float hi) { bf16x2 r = {(__bf16)lo, (__bf16)hi}; return __builtin_bit_cast(unsigned, r); }
typedef float v2f __attribute__((ext_vector_type(2)));
__device__ __forceinline__ float rb(float f) { return __builtin_bit_cast(float, __builtin_convertvector((v2f{0.f, f}), bf16x2)); }
__device__ __forceinline__ v4f mfma(v4u a, v4u b, v4f c) { return __builtin_amdgcn_mfma_f32_16x16x32_bf16(__builtin_bit_cast(bf16x8, a), __builtin_bit_cast(bf16x8, b), c, 0, 0, 0); }
template <int CTRL> __device__ __forceinline__ unsigned dppu(unsigned x) { return (unsigned)__builtin_amdgcn_mov_dpp((int)x, CTRL, 0xf, 0xf, true); }
template <int CTRL> __device__ __forceinline__ float dpp(float x) { return __uint_as_float(dppu<CTRL>(__float_as_uint(x))); }
template <int CTRL> __device__ __forceinline__ v4u dpp4(v4u x) { return v4u{dppu<CTRL>(x[0]), dppu<CTRL>(x[1]), dppu<CTRL>(x[2]), dppu<CTRL>(x[3])}; }
constexpr int X1 = 0xB1, X2 = 0x4E, HM = 0x141, R4 = 0x124, R8 = 0x128;
__device__ __forceinline__ float sw16(float a, float b, float& o) { auto r = __builtin_amdgcn_permlane16_swap(__float_as_uint(a), __float_as_uint(b), false, false); o = uf(r[1]); return uf(r[0]); }
__device__ __forceinline__ float sw32(float a, float b, float& o) { auto r = __builtin_amdgcn_permlane32_swap(__float_as_uint(a), __float_as_uint(b), false, false); o = uf(r[1]); return uf(r[0]); }
__device__ __forceinline__ float fold16(float a, float b) { float o; const float p = sw16(a, b, o); return p + o; }
__device__ __forceinline__ float fold32(float a, float b) { float o; const float p = sw32(a, b, o); return p + o; }
__device__ __forceinline__ float fmx(float a, float b) { return __builtin_elementwise_maximum(a, b); }
__device__ __forceinline__ float max16(float a) { float o; const float p = sw16(a, a, o); return fmx(p, o); }
#define MAXDPP(name, ctl) __device__ __forceinline__ float name(float m) { float r; asm("s_nop 1\n\tv_max_f32_dpp %0, %1, %1 " ctl " row_mask:0xf bank_mask:0xf" : "=v"(r) : "v"(m)); return r; }
MAXDPP(mx_r8, "row_ror:8") MAXDPP(mx_r4, "row_ror:4") MAXDPP(mx_x1, "quad_perm:[1,0,3,2]") MAXDPP(mx_x2, "quad_perm:[2,3,0,1]")
__device__ __forceinline__ float sum64(float v) { v += dpp<X1>(v); v += dpp<X2>(v); v += dpp<HM>(v); v += dpp<R8>(v); v = fold16(v, v); return fold32(v, v); }
__device__ __forceinline__ v2u trd(LAS unsigned char* p) { return __builtin_bit_cast(v2u, __builtin_amdgcn_ds_read_tr16_b64_v4i16((LAS s16x4*)p)); }
__device__ __forceinline__ v4u cat(v2u a, v2u b) { return v4u{a[0], a[1], b[0], b[1]}; }
__device__ __forceinline__ float ex2(float x) { return __builtin_amdgcn_exp2f(x); }
__device__ __forceinline__ float rcp(float x) { return __builtin_amdgcn_rcpf(x); }
__device__ __forceinline__ float gval(unsigned d) { return __uint_as_float(d & 0xFFFFFF00u); }
__device__ __forceinline__ unsigned d4(float f, unsigned tagbyte) { return ((__float_as_uint(f) + 0x80u) & 0xFFFFFF00u) | tagbyte; }
#define SB() __builtin_amdgcn_sched_barrier(0)
#define BAR() do { asm volatile("s_waitcnt lgkmcnt(0)" ::: "memory"); __builtin_amdgcn_s_barrier(); asm volatile("" ::: "memory"); } while (0)
#define FENCE() asm volatile("" ::: "memory")
#define WAITV(n) __builtin_amdgcn_s_waitcnt(0x0F70 | (n))
#define LD16(off, soff) __builtin_amdgcn_raw_buffer_load_b128(rs, (off), (soff), 16)
#define ST16(v, off, soff) __builtin_amdgcn_raw_buffer_store_b128((v), rs, (off), (soff), 16)
#define ST8(v, off, soff) __builtin_amdgcn_raw_buffer_store_b64((v), rs, (off), (soff), 16)
#define TBAD(v) __builtin_amdgcn_bitop3_b32((v)[1], (v)[3], tag, 0x7E)
#define ANY(t) (__builtin_amdgcn_ballot_w64((t) != 0) != 0)
#define DBAD(v) __builtin_amdgcn_bitop3_b32(__builtin_amdgcn_perm((v)[1], (v)[0], 0x0c0c0400u), __builtin_amdgcn_perm((v)[3], (v)[2], 0x04000c0cu), tag, 0x56)
#define EPOCH() asm volatile("" : "+s"(ep0)); const unsigned tag = ((ep0 & 0x7F7F7F7Fu) + 0x01010101u) ^ (ep0 & 0x80808080u)
#define UNTIL(BADEXPR, RELOAD) do { if (ANY(BADEXPR)) { int spins_ = SPIN_CAP; _Pragma("nounroll") do { FENCE(); RELOAD; if (!ANY(BADEXPR)) break; asm volatile("" : "+s"(spins_)); } while (--spins_); } } while (0)

__device__ __forceinline__ void mksel(unsigned (&sel)[8], int l) {
#pragma unroll
  for (int d = 0; d < 4; ++d) { const bool on = ((l & 7) >> 1) == d;
    sel[d] = on ? ((l & 1) ? 0x01000c0cu : 0x0c0c0100u) : 0x0c0c0c0cu; sel[4 + d] = on ? ((l & 1) ? 0x03020c0cu : 0x0c0c0302u) : 0x0c0c0c0cu; }
}
__device__ __forceinline__ v4u diag(unsigned pair, const unsigned (&sel)[8], int hi) {
  return v4u{__builtin_amdgcn_perm(0u, pair, sel[4 * hi]), __builtin_amdgcn_perm(0u, pair, sel[4 * hi + 1]), __builtin_amdgcn_perm(0u, pair, sel[4 * hi + 2]), __builtin_amdgcn_perm(0u, pair, sel[4 * hi + 3])};
}
__device__ __forceinline__ float upr8(float v) { return __builtin_bit_cast(float, __builtin_amdgcn_update_dpp(__builtin_bit_cast(int, v), __builtin_bit_cast(int, v), R8, 0xC, 0xF, false)); }
__device__ __forceinline__ v2u diag_out(v4f d, unsigned tb0, unsigned tb1) {
  const float t0 = upr8(d[0]), t1 = upr8(d[1]), t2 = upr8(d[2]), t3 = upr8(d[3]);
  return v2u{d4(fold32(t0, t2), tb0), d4(fold32(t1, t3), tb1)};
}

__global__ void __launch_bounds__(NTHR, 3) fused_decode(const u16* __restrict__ x, const u16* __restrict__ wq, const u16* __restrict__ w1, const u16* __restrict__ vc, unsigned char* ws,
                                                        const u16* __restrict__ kc, const u16* __restrict__ wo, const u16* __restrict__ w2, const u16* __restrict__ b1,
                                                        const u16* __restrict__ b2, u16* __restrict__ out) {
  __shared__ __attribute__((aligned(128))) unsigned char lds_[L_END];
  LAS unsigned char* L = (LAS unsigned char*)lds_;
  const int b = blockIdx.x, g = b & 7, j = b >> 3, l = threadIdx.x & 63;
  int w = __builtin_amdgcn_readfirstlane(threadIdx.x >> 6);
  { u64 pc; unsigned keep, t_;
    asm volatile("s_cmp_lg_u32 %3, 4\n\ts_cbranch_scc1 1f\n\ts_getpc_b64 %0\n\ts_mov_b32 %1, m0\n\ts_mov_b32 m0, %5\n\tv_lshlrev_b32 %2, 7, %4\n\t"
                 "global_load_lds_dword %2, %0\n\ts_mov_b32 m0, %1\n1:"
                 : "=&s"(pc), "=&s"(keep), "=&v"(t_) : "s"(w), "v"((j * 4 + (l & 3)) & 63), "i"(L_O) : "scc", "memory"); }
  const int kq = j & 3, rb_ = j >> 2, gn = (g + 1) & 7, gp = (g - 1) & 7;
  const int gq = l >> 4, n = l & 15;
  const bool hasw1 = rb_ != 3; const int jw = rb_ == 7 ? j - 16 : j;
  int kvo = (g * SK + (SK - WIN) + j * 128 + (((w - 1) * 2 + ((w - 1) >> 2) + j) & 7) * 16) * 256, l16 = l * 16, kof = (n >> 2) * 1024 + gq * 64 + (n & 3) * 16;
  int vo = (l >> 3) * (16 * 8192) + (l & 7) * 16, so = kq * (128 * 8192) + gn * 1024 + rb_ * 128;
  int vo1 = hasw1 ? (l >> 3) * (8 * FF * 2) + (l & 7) * 16 : OOB, so1 = g * (64 * FF * 2) + jw * 128;
  asm volatile("" : "+s"(kvo), "+s"(so), "+s"(so1));
  asm volatile("s_setprio 3" : "+v"(l16), "+v"(vo), "+v"(vo1), "+v"(kof));
  asm volatile("" :: "s"(x), "s"(kc), "s"(vc), "s"(wq), "s"(wo), "s"(w1), "s"(b1), "s"(w2), "s"(b2), "s"(out), "s"(ws));
  unsigned ep0 = 0;
  const rsrc_t rs = mk(ws, WS_END);
  v4u kv[4];
  if (w != 0) {
    const rsrc_t rk = mk(kc, 8u * SK * 256);
    if (w == 3) {
      unsigned keep;
      const unsigned po = (j * 16 + 8 + (l & 7)) * 8192 + gn * 1024 + 384 + ((l >> 3) & 1) * 512;
      asm volatile("s_mov_b32 %0, m0\n\ts_mov_b32 m0, %3\n\ts_nop 0\n\tglobal_load_lds_dword %1, %2 nt\n\ts_mov_b32 m0, %0" : "=&s"(keep) : "v"(po), "s"(wq), "i"(L_O + 256) : "memory");
    }
    ep0 = *(const CAS unsigned*)ws;
    __builtin_amdgcn_s_setprio(2);
    __builtin_amdgcn_s_barrier();
    SB();
    kv[0] = ldw(rk, kof, kvo); kv[1] = ldw(rk, kof + 256, kvo);
    SB();
  }
  asm volatile("" : "+s"(w));
  if (__builtin_expect(w == 0, 1)) {
    v4u r16[16], f8[8];
    const v4u xv = *(const GAS v4u*)((const GAS unsigned char*)x + (unsigned)l16);
    const unsigned epv = __builtin_amdgcn_raw_buffer_load_b32(rs, 0, 0, 0);
    { const rsrc_t r = mk(wq, 512 * 4096 * 2), r1 = mk(w1, 512 * FF * 2);
#pragma unroll
      for (int i = 0; i < 16; ++i) r16[i] = ldw(r, vo, so + i * 8192);
#pragma unroll
      for (int i = 0; i < 8; ++i) f8[i] = ldw(r1, vo1, so1 + i * (FF * 2)); }
    SB();
    __builtin_amdgcn_s_barrier();
    SB();
    unsigned sel[8]; mksel(sel, l);
    int gvo = (l & 8) ? OOB : (l & 7) * 32 + (l & 16) + (l >> 5) * 8, xra = L_XN + kq * 256 + (l >> 3) * 32, xrc = L_XN + g * 128 + (l >> 3) * 16;
    asm volatile("" : "+v"(sel[0]), "+v"(sel[1]), "+v"(sel[2]), "+v"(sel[3]), "+v"(sel[4]), "+v"(sel[5]), "+v"(sel[6]), "+v"(sel[7]), "+v"(gvo), "+v"(xra), "+v"(xrc));
    SB();
    float xf[8];
#pragma unroll
    for (int i = 0; i < 4; ++i) { xf[2 * i] = bflo(xv[i]); xf[2 * i + 1] = bfhi(xv[i]); }
    const float mean = sum64(((xf[0] + xf[1]) + (xf[2] + xf[3])) + ((xf[4] + xf[5]) + (xf[6] + xf[7]))) * (1.f / 512);
    float vs = 0.f;
#pragma unroll
    for (int i = 0; i < 8; ++i) { const float d = xf[i] - mean; vs = fmaf(d, d, vs); }
    const float var = sum64(vs) * (1.f / 512);
    const float mb = rb(mean), rsd = rcp(rb(__builtin_amdgcn_sqrtf(rb(rb(var) + 1e-5f))));
    v4u xn;
#pragma unroll
    for (int i = 0; i < 4; ++i) { const unsigned d = pk(xf[2 * i] - mb, xf[2 * i + 1] - mb); xn[i] = pk(bflo(d) * rsd, bfhi(d) * rsd); }
    *(LAS v4u*)(L + L_XN + l * 16) = xn;
    *(LAS float*)(L + L_M + (l & 3) * 4) = -__builtin_inff();
    const v4u xa = *(LAS v4u*)(L + xra), xb = *(LAS v4u*)(L + xra + 16), xc = *(LAS v4u*)(L + xrc);
    ep0 = __builtin_amdgcn_readfirstlane(epv);
    EPOCH();
    const unsigned tb0 = (tag >> ((l >> 5) * 16)) & 0xFFu, tb1 = (tag >> ((l >> 5) * 16 + 8)) & 0xFFu;
    { v4u bx[8];
#pragma unroll
      for (int i = 0; i < 8; ++i) bx[i] = diag(xa[i >> 1], sel, i & 1);
      SB();
      v4f d = {0.f, 0.f, 0.f, 0.f};
#pragma unroll
      for (int i = 0; i < 8; ++i) { d = mfma(bx[i], r16[i], d); bx[i] = diag(xb[i >> 1], sel, i & 1); SB(); }
#pragma unroll
      for (int i = 0; i < 8; ++i) d = mfma(bx[i], r16[8 + i], d);
      ST8(diag_out(d, tb0, tb1), gvo, WS_QG + gn * 8192 + kq * 2048 + rb_ * 256);
      SB();
      __builtin_amdgcn_s_barrier();
      SB();
    }
    { v4f d = {0.f, 0.f, 0.f, 0.f};
#pragma unroll
      for (int i = 0; i < 8; ++i) d = mfma(diag(xc[i >> 1], sel, i & 1), f8[i], d);
      ST8(diag_out(d, tb0, tb1), hasw1 ? gvo : OOB, WS_HG + g * (FF * 4) + jw * 256); }
    const int hr0 = gp * 224 + rb_ * 28, hu = min(l & 7, 6), ocol = (b & 63) * 8 + (l & 3) * 2 + (l >> 5);
    int mlo = (l & 51) == 0 ? (n >> 2) * 512 : OOB, mls = WS_ML + g * 2048 + j * 16, hvo = (l >> 3) * (FF * 4) + hu * 16, hso = WS_HG + hr0 * 4;
    int pvo = (l & 31) * 16, pso = WS_ML + gp * 2048 + (j >> 3) * 512;
    asm volatile("" : "+v"(mlo), "+v"(hvo), "+v"(pvo));
    asm volatile("" : "+s"(mls), "+s"(hso), "+s"(pso));
    const float b1v = bflo(b1[hr0 + hu * 4 + gq]), b2v = bflo(b2[ocol]), xo = bflo(x[ocol]);
    BAR();
    BAR();
    v4u hg = LD16(hvo, hso);
    SB();
    BAR();
    { const v2u mm = *(LAS v2u*)(L + L_MX + l * 8);
      const v4f s0 = *(LAS v4f*)(L + L_S + l * 16), s1 = *(LAS v4f*)(L + L_S + 1024 + l * 16);
      const float m = *(LAS float*)(L + L_M + (n >> 2) * 4);
      float ls = fmaf((s0[0] + s0[1]) + (s0[2] + s0[3]), ex2(uf(mm[0]) - m), ((s1[0] + s1[1]) + (s1[2] + s1[3])) * ex2(uf(mm[1]) - m));
      ls += dpp<X1>(ls); ls += dpp<X2>(ls); ls = fold16(ls, ls); ls = fold32(ls, ls);
      ST16((v4u{__float_as_uint(m), tag, __float_as_uint(ls), tag}), mlo, mls);
    }
    BAR();
    {
      if (__builtin_expect(ANY(DBAD(hg)), 0)) UNTIL(DBAD(hg), hg = LD16(hvo, hso));
      float hs = fold16(fold32(gval(hg[0]), gval(hg[2])), fold32(gval(hg[1]), gval(hg[3]))); hs += dpp<R8>(hs);
      const float hp = rb(rb(hs) + b1v);
      const float u2 = 1.5957691216057308f * fmaf(0.044715f * hp * hp, hp, hp);
      const float ge = hp * rcp(1.f + ex2(-u2 * LOG2E));
      *(LAS u16*)(L + L_H + (l & 7) * 8 + gq * 2) = ((l & 7) < 7) ? (u16)pk(ge, ge) : (u16)0;
    }
    __builtin_amdgcn_s_sleep(8);
    v4u pv = LD16(pvo, pso);
    SB();
    BAR();
    {
      UNTIL(TBAD(pv), pv = LD16(pvo, pso));
      const float m = uf(pv[0]);
      const float M = max16(mx_r4(mx_r8(mx_x2(mx_x1(m)))));
      const float e = ex2(m - M);
      float ls = e * uf(pv[2]);
      ls += dpp<X1>(ls); ls += dpp<X2>(ls); ls += dpp<HM>(ls); ls += dpp<R8>(ls); ls = fold16(ls, ls);
      *(LAS float*)(L + L_WG + (l & 31) * 4) = e * rcp(ls);
    }
    BAR();
    if (b < 64) {
      const int fo = WS_FB + b * 4096;
      v4u f0 = LD16(l16, fo), f1 = LD16(l16 + 1024, fo), f2 = LD16(l16 + 2048, fo), f3 = LD16(l16 + 3072, fo);
      UNTIL(TBAD(f0) | TBAD(f1) | TBAD(f2) | TBAD(f3), (f0 = LD16(l16, fo), f1 = LD16(l16 + 1024, fo), f2 = LD16(l16 + 2048, fo), f3 = LD16(l16 + 3072, fo)));
      float t = fold32((uf(f0[0]) + uf(f1[0])) + (uf(f2[0]) + uf(f3[0])), (uf(f0[2]) + uf(f1[2])) + (uf(f2[2]) + uf(f3[2])));
      t = fold16(t, t); t += dpp<R8>(t); t += dpp<R4>(t);
      const float f = rb(rb(t) + b2v);
      float gt = rb(f * rcp(1.f + ex2(-f * LOG2E)));
      int frd = L_V + ((l >> 5) * 576 + (l & 3) * 136 + ((l >> 2) & 7) * 16) * 4;
      int ovo = (l & 28) ? OOB : ocol * 2; float xo_ = xo;
      asm volatile("" : "+v"(gt), "+v"(frd), "+v"(ovo), "+v"(xo_));
      BAR();
      const v4f u0 = *(LAS v4f*)(L + frd), u1 = *(LAS v4f*)(L + frd + 16);
      const v4f u = (u0 + u1) + (*(LAS v4f*)(L + frd + 32) + *(LAS v4f*)(L + frd + 48));
      float v = (u[0] + u[2]) + (u[1] + u[3]);
      v = fold16(v, v); v += dpp<R8>(v); v += dpp<R4>(v);
      const float o = xo_ + rb(rb(v) * gt);
      __builtin_amdgcn_raw_buffer_store_b16((u16)pk(o, o), mk(out, 1024), ovo, 0, 16);
      if (b == 0 && l == 0) __hip_atomic_store((GAS unsigned*)ws, tag, __ATOMIC_RELAXED, __HIP_MEMORY_SCOPE_AGENT);
    }
  } else {
    const int a = w - 1, a3 = a & 3;
    int obo = (n & 3) == 0 ? (n >> 2) * 16384 + gq * 16 : OOB;
    int sbo = (l & 16) ? OOB : (l >> 5) * 16384 + (n >> 3) * 8192 + (n & 7) * 8;
    int obs = WS_OB + g * 65536 + a3 * 4096 + j * 128, sbs = WS_SB + ((j & 1) * 32 + a * 4) * 8192 + (g * 16 + (j >> 1)) * 64;
    int fbs = WS_FB + (kq * 16 + a * 2) * 4096 + (g * 8 + rb_) * 64;
    int sadr = L_S + (a & 1) * 1024 + gq * 256 + (n & 3) * 64 + (a >> 1) * 16 + (n >> 2) * 4, qadr = L_Q + (n & 3) * QSTR + gq * 64 + (n >> 2) * 16;
    int pso = WS_OB + gp * 65536 + (j >> 1) * 4096 + a3 * 1024, pvo = a >= 4 ? l * 16 : OOB;
    int oadr = L_O + n * 80 + gq * 16, owr = L_O + (a3 * 8 + (l >> 3)) * 80 + (l & 7) * 8;
    int mad = L_M + (n & 3) * 4, l8 = l * 8, mrd = L_M + (n >> 2) * 4;
    asm volatile("" : "+v"(mad), "+v"(l8), "+v"(mrd));
    int fso = WS_SB + b * 8192 + a * 1024, fadr = L_V + ((l & 3) * 136 + a * 16 + (l >> 2)) * 4;
    int vwr = L_V + (a * 16 + gq) * VSTR + n * 16;
    int qso = WS_QG + g * 8192 + a3 * 512, qvo = a >= 4 ? (l >> 5) * 4096 + (l & 31) * 16 : OOB;
    int mxw = L_MX + gq * 128 + (n & 3) * 32 + a * 4, qwr = L_Q + a3 * QSTR + (l & 31) * 8 + (l >> 5) * 4;
    int wov = (l >> 5) * 1024 + ((l & 31) ^ ((l >> 5) * 2)) * 16;
    asm volatile("" : "+v"(wov));
    int wos = (gp * 512 + (j >> 1) * 32 + a3 * 8) * 1024 + (j & 1) * 512, w2s = (gp * 224 + rb_ * 28 + a3 * 8) * 1024 + kq * 256, wvo = gq * 1024 + (n ^ (gq * 2)) * 16;
    int w2t = w2s + (a3 == 3 ? 0 : 4096);
    int wl0 = L_W + a3 * 4 * WBLK, wl1 = L_W + (16 + a3 * 2) * WBLK;
    v4u so_ = {(unsigned)(u64)wo, (unsigned)((u64)wo >> 32) & 0xFFFFu, 4096u * 512 * 2, 0x20000u}, s2_ = {(unsigned)(u64)w2, (unsigned)((u64)w2 >> 32) & 0xFFFFu, FF * 512u * 2, 0x20000u};
    asm volatile("" : "+v"(obo), "+v"(sbo), "+v"(sadr), "+v"(qadr), "+v"(oadr), "+v"(owr), "+v"(vwr), "+v"(fadr), "+v"(qvo), "+v"(mxw), "+v"(qwr), "+v"(pvo), "+v"(wvo));
    asm volatile("" : "+s"(obs), "+s"(sbs), "+s"(pso), "+s"(qso), "+s"(fso), "+s"(wos), "+s"(w2s), "+s"(so_), "+s"(s2_), "+s"(wl0), "+s"(wl1), "+s"(w2t));
    const rsrc_t rv = mk(vc, 8u * SK * 256);
    int tadr = L_V + (gq * 4 + (n >> 2)) * VSTR + a3 * 64 + (n & 3) * 8;
    asm volatile("" : "+v"(tadr));
    EPOCH();
    v4u o0 = {0u, tag, 0u, tag}, o1 = o0; u64 lead = 0x000F000F000F000Full;
    asm volatile("" : "+v"(o0), "+v"(o1)); asm volatile("" : "+s"(lead));
    SB();
#define WDO() do { unsigned keep; asm volatile("s_mov_b32 %0, m0\n\ts_mov_b32 m0, %4\n\ts_nop 0\n\t" \
      "buffer_load_dwordx4 %2, %3, %1 offen nt lds\n\ts_add_u32 m0, m0, %5\n\ts_add_u32 %1, %1, 0x800\n\tbuffer_load_dwordx4 %2, %3, %1 offen nt lds\n\ts_add_u32 m0, m0, %5\n\ts_add_u32 %1, %1, 0x800\n\t" \
      "buffer_load_dwordx4 %2, %3, %1 offen nt lds\n\ts_add_u32 m0, m0, %5\n\ts_add_u32 %1, %1, 0x800\n\tbuffer_load_dwordx4 %2, %3, %1 offen nt lds\n\ts_mov_b32 m0, %0" \
      : "=&s"(keep), "+s"(wos) : "v"(wov), "s"(so_), "s"(wl0), "i"(WBLK) : "memory", "scc"); } while (0)
#define WD2() do { unsigned keep; asm volatile("s_mov_b32 %0, m0\n\ts_mov_b32 m0, %4\n\ts_nop 0\n\t" \
      "buffer_load_dwordx4 %2, %3, %1 offen nt lds\n\ts_add_u32 m0, m0, %5\n\ts_nop 0\n\tbuffer_load_dwordx4 %2, %3, %6 offen nt lds\n\ts_mov_b32 m0, %0" \
      : "=&s"(keep) : "s"(w2s), "v"(wvo), "s"(s2_), "s"(wl1), "i"(WBLK), "s"(w2t) : "memory", "scc"); } while (0)
    { const rsrc_t rk = mk(kc, 8u * SK * 256);
      if (a >= 4) __builtin_amdgcn_s_barrier();
      SB();
      kv[2] = ldw(rk, kof + 512, kvo); kv[3] = ldw(rk, kof + 768, kvo);
      SB();
      if (a < 4) __builtin_amdgcn_s_barrier(); }
    WAITV(3);
    SB();
    if (a >= 4) { WAITV(1); __builtin_amdgcn_s_barrier(); }
    SB();
    v4u q0 = LD16(qvo, qso), q1 = LD16(qvo, qso + 2048);
    SB();
    if (a >= 4) __builtin_amdgcn_s_sleep(1);
    v4u vv[4];
#pragma unroll
    for (int i = 0; i < 4; ++i) vv[i] = ldw(rv, l16 + i * 1024, kvo);
    SB();
    if (a < 4) __builtin_amdgcn_s_barrier();
    SB();
    WAITV(4);
#define QSUM() do { const v4f u_ = __builtin_bit_cast(v4f, q0 & 0xFFFFFF00u) + __builtin_bit_cast(v4f, q1 & 0xFFFFFF00u); float c_, d_; const float a_ = sw32(u_[0], u_[2], c_), b_ = sw32(u_[1], u_[3], d_); \
        *(LAS unsigned*)(L + qwr) = pk(a_ + c_, b_ + d_); } while (0)
    if (a >= 4) {
      QSUM();
      SB();
      if (__builtin_expect(ANY(DBAD(q0) | DBAD(q1)), 0)) {
        UNTIL(DBAD(q0) | DBAD(q1), (q0 = LD16(qvo, qso), q1 = LD16(qvo, qso + 2048)));
        QSUM();
      }
    }
    SB();
    if (a < 4) {
#pragma unroll
      for (int i = 0; i < 4; ++i) *(LAS v4u*)(L + vwr + i * 4 * VSTR) = vv[i];
    }
    BAR();
    {
      const v4u qf = *(LAS v4u*)(L + qadr);
      if (a < 4) WD2();
      v4f sc[4];
#pragma unroll
      for (int i = 0; i < 4; ++i) { sc[i] = mfma(kv[i], qf, v4f{0.f, 0.f, 0.f, 0.f}); SB(); }
      float t, m;
#define DADD(src, acc, rot, bank) "v_add_f32_dpp %0, " src ", " acc " row_ror:" rot " row_mask:0xf bank_mask:" bank "\n\t"
      asm volatile("s_nop 4\n\t"
                   DADD("%3", "%2", "12", "0x1") DADD("%6", "%7", "4", "0x2") DADD("%10", "%12", "8", "0x4") DADD("%14", "%17", "12", "0x8")
                   DADD("%4", "%0", "8", "0x1") DADD("%8", "%0", "12", "0x2") DADD("%11", "%0", "4", "0x4") DADD("%15", "%0", "8", "0x8")
                   DADD("%5", "%0", "4", "0x1") DADD("%9", "%0", "8", "0x2") DADD("%13", "%0", "12", "0x4") DADD("%16", "%0", "4", "0x8")
                   "v_cvt_pk_bf16_f32 %0, 0, %0\n\tv_mul_f32 %0, 0x3db504f3, %0\n\tv_cvt_pk_bf16_f32 %0, 0, %0\n\tv_mul_f32 %0, 0x3fb8aa3b, %0\n\ts_nop 1\n\t"
                   "v_max_f32_dpp %1, %0, %0 row_ror:8 row_mask:0xf bank_mask:0xf\n\ts_nop 1\n\t"
                   "v_max_f32_dpp %1, %1, %1 row_ror:4 row_mask:0xf bank_mask:0xf\n\t"
                   "v_sub_f32 %0, %0, %1\n\tv_exp_f32 %0, %0\n\t"
                   "s_mov_b64 exec, %21\n\tds_max_f32 %18, %1\n\tds_write_b32 %19, %1\n\ts_mov_b64 exec, -1\n\tds_write_b32 %20, %0"
                   : "=&v"(t), "=&v"(m)
                   : "v"(sc[0][0]), "v"(sc[0][1]), "v"(sc[0][2]), "v"(sc[0][3]), "v"(sc[1][0]), "v"(sc[1][1]), "v"(sc[1][2]), "v"(sc[1][3]),
                     "v"(sc[2][0]), "v"(sc[2][1]), "v"(sc[2][2]), "v"(sc[2][3]), "v"(sc[3][0]), "v"(sc[3][1]), "v"(sc[3][2]), "v"(sc[3][3]),
                     "v"(mad), "v"(mxw), "v"(sadr), "s"(lead)
                   : "memory");
#pragma unroll
      for (int i = 0; i < 5; ++i) __builtin_amdgcn_s_setprio(2);
    }
    v2u vt[16];
    if (a < 4) {
#pragma unroll
      for (int i = 0; i < 8; ++i) vt[(i & 3) + (i >> 2) * 8] = trd(L + tadr + (i & 3) * 16 * VSTR + (i >> 2) * 32);
    }
    BAR();
    if (a < 4) {
      const v2u mm = *(LAS v2u*)(L + L_MX + l8);
      const float m = *(LAS float*)(L + mrd);
      const v4f s0 = *(LAS v4f*)(L + L_S + l16), s1 = *(LAS v4f*)(L + L_S + 1024 + l16);
      const v4f p0 = s0 * ex2(uf(mm[0]) - m), p1 = s1 * ex2(uf(mm[1]) - m);
      const v4u pb = {pk(p0[0], p0[1]), pk(p0[2], p0[3]), pk(p1[0], p1[1]), pk(p1[2], p1[3])};
      const v4f z = {0.f, 0.f, 0.f, 0.f};
      v4f d0 = mfma(cat(vt[0], vt[1]), pb, z), d1 = mfma(cat(vt[8], vt[9]), pb, z);
      const v4u pb1 = dpp4<0x55>(pb), pb2 = dpp4<0xAA>(pb);
      SB();
      d0 = mfma(cat(vt[2], vt[3]), pb1, d0); d1 = mfma(cat(vt[10], vt[11]), pb1, d1);
      v4u pb3 = dpp4<0xFF>(pb);
      asm volatile("" : "+v"(pb3) : "v"(pb1), "v"(vt[0]), "v"(vt[1]), "v"(vt[8]), "v"(vt[9]));
      SB();
      WDO();
      BAR();
#pragma unroll
      for (int i = 0; i < 8; ++i) vt[4 + (i & 1) + ((i >> 2) & 1) * 2 + ((i >> 1) & 1) * 8] = trd(L + tadr + (4 + (i & 1) + ((i >> 2) & 1) * 2) * 16 * VSTR + ((i >> 1) & 1) * 32);
      d0 = mfma(cat(vt[4], vt[5]), pb2, d0); d1 = mfma(cat(vt[12], vt[13]), pb2, d1);
      d0 = mfma(cat(vt[6], vt[7]), pb3, d0); d1 = mfma(cat(vt[14], vt[15]), pb3, d1);
      o0[0] = pk(d0[0], d0[1]); o0[2] = pk(d0[2], d0[3]); o1[0] = pk(d1[0], d1[1]); o1[2] = pk(d1[2], d1[3]);
      ST16(o0, obo, obs);
      ST16(o1, obo + 64, obs);
      asm volatile("s_waitcnt vmcnt(2)" ::: "memory");
    }
    SB();
    if (a >= 4) {
#pragma unroll
      for (int i = 0; i < 4; ++i) *(LAS v4u*)(L + vwr + i * 4 * VSTR) = vv[i];
      BAR();
      __builtin_amdgcn_s_sleep(20);
    }
    SB();
    v4u pv = LD16(pvo, pso);
    BAR();
    if (a >= 4 && ANY(TBAD(pv))) pv = LD16(pvo, pso);
    SB();
    v2u wt[4];
    const int wrd = L_W + gq * 2 * WBLK + (n >> 2) * 256 + (a ^ (n >> 2)) * 32 + (n & 3) * 8;
#pragma unroll
    for (int i = 0; i < 4; ++i) wt[i] = trd(L + L_W + (gq * 4 + (i & 1) * 2 + (n >> 3)) * WBLK + ((n >> 2) & 1) * 512 + ((a * 2 + (i >> 1)) ^ ((n >> 2) & 1)) * 32 + (n & 3) * 8);
    SB();
    WAITV(0);
    if (a < 4) {
      const v4u hf = *(LAS v4u*)(L + L_H + gq * 16);
      LAS unsigned char* p = L + wrd + 16 * WBLK;
      const v4f z = {0.f, 0.f, 0.f, 0.f};
      const v4f f0 = mfma(hf, cat(trd(p), trd(p + WBLK)), z), f1 = mfma(hf, cat(trd(p + 128), trd(p + 128 + WBLK)), z);
      ST8((v2u{__float_as_uint(gq ? f1[0] : f0[0]), tag}), l < 32 ? gq * 32768 + (n >> 3) * 4096 + (n & 7) * 8 : OOB, fbs);
    }
    SB();
    if (a >= 4) {
#define OWR() (((LAS unsigned*)(L + owr))[0] = pv[0], ((LAS unsigned*)(L + owr))[1] = pv[2])
      OWR();
      UNTIL(TBAD(pv), (pv = LD16(pvo, pso), OWR()));
    }
    BAR();
    {
      const v4u of0 = *(LAS v4u*)(L + oadr), of1 = *(LAS v4u*)(L + oadr + 16 * 80);
      const v4f w0 = *(LAS v4f*)(L + L_WG + gq * 16), w1_ = *(LAS v4f*)(L + L_WG + 64 + gq * 16);
      SB();
      const v4f z = {0.f, 0.f, 0.f, 0.f};
      const v4f d00 = mfma(of0, cat(wt[0], wt[1]), z), d01 = mfma(of0, cat(wt[2], wt[3]), z), d10 = mfma(of1, cat(wt[0], wt[1]), z), d11 = mfma(of1, cat(wt[2], wt[3]), z);
      SB();
      v2f c0 = d00.hi * w0.hi + d00.lo * w0.lo, c1 = d01.hi * w0.hi + d01.lo * w0.lo;
      c0 = d10.hi * w1_.hi + (d10.lo * w1_.lo + c0); c1 = d11.hi * w1_.hi + (d11.lo * w1_.lo + c1);
      float e = fold32(c0[0] + c0[1], c1[0] + c1[1]);
      e = fold16(e, e);
      ST8((v2u{__float_as_uint(e), tag}), sbo, sbs);
    }
    if (b < 64) {
      __builtin_amdgcn_s_sleep(10); if (a == g) __builtin_amdgcn_s_sleep(10);
      v4u sv = LD16(l16, fso);
#define SWR() (((LAS unsigned*)(L + fadr))[0] = sv[0], ((LAS unsigned*)(L + fadr))[576] = sv[2])
      SWR();
      UNTIL(TBAD(sv), (sv = LD16(l16, fso), SWR()));
      BAR();
    }
  }
}

extern "C" void kernel_launch(void* const* d_in, const int* in_sizes, int n_in, void* d_out, int out_size, void* d_ws, size_t ws_size, hipStream_t stream) {
  fused_decode<<<256, NTHR, 0, stream>>>((const u16*)d_in[0], (const u16*)d_in[3], (const u16*)d_in[6], (const u16*)d_in[2], (unsigned char*)d_ws, (const u16*)d_in[1],
                                         (const u16*)d_in[4], (const u16*)d_in[8], (const u16*)d_in[7], (const u16*)d_in[9], (u16*)d_out);
}
```

```cpp
#include <hip/hip_runtime.h>
#include <hip/hip_bf16.h>

typedef unsigned short u16;
typedef unsigned long long u64;
typedef unsigned v4u __attribute__((ext_vector_type(4)));
typedef unsigned v2u __attribute__((ext_vector_type(2)));
typedef float v4f __attribute__((ext_vector_type(4)));
typedef short s16x4 __attribute__((ext_vector_type(4)));
typedef __bf16 bf16x2 __attribute__((ext_vector_type(2)));
typedef __bf16 bf16x8 __attribute__((ext_vector_type(8)));
typedef __amdgpu_buffer_rsrc_t rsrc_t;
#define GAS __attribute__((address_space(1)))
#define CAS __attribute__((address_space(4)))
#define LAS __attribute__((address_space(3)))

constexpr int SK = 32768, WIN = 4096, FF = 1792;
constexpr int NTHR = 576;
constexpr unsigned WS_QG = 4096;
constexpr unsigned WS_HG = WS_QG + 65536;
constexpr unsigned WS_ML = WS_HG + 57344;
constexpr unsigned WS_OB = WS_ML + 16384;
constexpr unsigned WS_SB = WS_OB + 524288;
constexpr unsigned WS_FB = WS_SB + 524288;
constexpr unsigned WS_END = WS_FB + 262144;
constexpr int VSTR = 288, L_V = 0, L_O = 128 * VSTR, L_Q = L_O + 4096, QSTR = 320, L_S = L_Q + 4 * QSTR, L_XN = L_S + 2048, L_MX = L_XN + 1024, L_M = L_MX + 512, L_H = L_M + 32,
              L_WG = L_H + 64, L_W = L_WG + 128, WBLK = 1088, L_END = L_W + 24 * WBLK;
constexpr unsigned SPIN_CAP = 1u << 18;
constexpr float LOG2E = 1.4426950408889634f;
constexpr int OOB = 0x40000000;

__device__ __forceinline__ rsrc_t mk(const void* p, unsigned bytes) { return __builtin_amdgcn_make_buffer_rsrc((void*)p, 0, bytes, 0x00020000); }
__device__ __forceinline__ v4u ldw(rsrc_t r, int voff, int soff) { return __builtin_amdgcn_raw_buffer_load_b128(r, voff, soff, 2); }
__device__ __forceinline__ float uf(unsigned u) { return __uint_as_float(u); }
__device__ __forceinline__ float bflo(unsigned u) { return __uint_as_float(u << 16); }
__device__ __forceinline__ float bfhi(unsigned u) { return __uint_as_float(u & 0xFFFF0000u); }
__device__ __forceinline__ unsigned pk(float lo, float hi) { bf16x2 r = {(__bf16)lo, (__bf16)hi}; return __builtin_bit_cast(unsigned, r); }
typedef float v2f __attribute__((ext_vector_type(2)));
__device__ __forceinline__ float rb(float f) { return __builtin_bit_cast(float, __builtin_convertvector((v2f{0.f, f}), bf16x2)); }
__device__ __forceinline__ v4f mfma(v4u a, v4u b, v4f c) { return __builtin_amdgcn_mfma_f32_16x16x32_bf16(__builtin_bit_cast(bf16x8, a), __builtin_bit_cast(bf16x8, b), c, 0, 0, 0); }
template <int CTRL> __device__ __forceinline__ unsigned dppu(unsigned x) { return (unsigned)__builtin_amdgcn_mov_dpp((int)x, CTRL, 0xf, 0xf, true); }
template <int CTRL> __device__ __forceinline__ float dpp(float x) { return __uint_as_float(dppu<CTRL>(__float_as_uint(x))); }
template <int CTRL> __device__ __forceinline__ v4u dpp4(v4u x) { return v4u{dppu<CTRL>(x[0]), dppu<CTRL>(x[1]), dppu<CTRL>(x[2]), dppu<CTRL>(x[3])}; }
constexpr int X1 = 0xB1, X2 = 0x4E, HM = 0x141, R4 = 0x124, R8 = 0x128;
__device__ __forceinline__ float sw16(float a, float b, float& o) { auto r = __builtin_amdgcn_permlane16_swap(__float_as_uint(a), __float_as_uint(b), false, false); o = uf(r[1]); return uf(r[0]); }
__device__ __forceinline__ float sw32(float a, float b, float& o) { auto r = __builtin_amdgcn_permlane32_swap(__float_as_uint(a), __float_as_uint(b), false, false); o = uf(r[1]); return uf(r[0]); }
__device__ __forceinline__ float fold16(float a, float b) { float o; const float p = sw16(a, b, o); return p + o; }
__device__ __forceinline__ float fold32(float a, float b) { float o; const float p = sw32(a, b, o); return p + o; }
__device__ __forceinline__ float fmx(float a, float b) { return __builtin_elementwise_maximum(a, b); }
__device__ __forceinline__ float max16(float a) { float o; const float p = sw16(a, a, o); return fmx(p, o); }
#define MAXDPP(name, ctl) __device__ __forceinline__ float name(float m) { float r; asm("s_nop 1\n\tv_max_f32_dpp %0, %1, %1 " ctl " row_mask:0xf bank_mask:0xf" : "=v"(r) : "v"(m)); return r; }
MAXDPP(mx_r8, "row_ror:8") MAXDPP(mx_r4, "row_ror:4") MAXDPP(mx_x1, "quad_perm:[1,0,3,2]") MAXDPP(mx_x2, "quad_perm:[2,3,0,1]")
__device__ __forceinline__ float sum64(float v) { v += dpp<X1>(v); v += dpp<X2>(v); v += dpp<HM>(v); v += dpp<R8>(v); v = fold16(v, v); return fold32(v, v); }
__device__ __forceinline__ v2u trd(LAS unsigned char* p) { return __builtin_bit_cast(v2u, __builtin_amdgcn_ds_read_tr16_b64_v4i16((LAS s16x4*)p)); }
__device__ __forceinline__ v4u cat(v2u a, v2u b) { return v4u{a[0], a[1], b[0], b[1]}; }
__device__ __forceinline__ float ex2(float x) { return __builtin_amdgcn_exp2f(x); }
__device__ __forceinline__ float rcp(float x) { return __builtin_amdgcn_rcpf(x); }
__device__ __forceinline__ float gval(unsigned d) { return __uint_as_float(d & 0xFFFFFF00u); }
__device__ __forceinline__ unsigned d4(float f, unsigned tagbyte) { return ((__float_as_uint(f) + 0x80u) & 0xFFFFFF00u) | tagbyte; }
#define SB() __builtin_amdgcn_sched_barrier(0)
#define BAR() do { asm volatile("s_waitcnt lgkmcnt(0)" ::: "memory"); __builtin_amdgcn_s_barrier(); asm volatile("" ::: "memory"); } while (0)
#define FENCE() asm volatile("" ::: "memory")
#define WAITV(n) __builtin_amdgcn_s_waitcnt(0x0F70 | (n))
#define LD16(off, soff) __builtin_amdgcn_raw_buffer_load_b128(rs, (off), (soff), 16)
#define ST16(v, off, soff) __builtin_amdgcn_raw_buffer_store_b128((v), rs, (off), (soff), 16)
#define ST8(v, off, soff) __builtin_amdgcn_raw_buffer_store_b64((v), rs, (off), (soff), 16)
#define TBAD(v) __builtin_amdgcn_bitop3_b32((v)[1], (v)[3], tag, 0x7E)
#define ANY(t) (__builtin_amdgcn_ballot_w64((t) != 0) != 0)
#define DBAD(v) __builtin_amdgcn_bitop3_b32(__builtin_amdgcn_perm((v)[1], (v)[0], 0x0c0c0400u), __builtin_amdgcn_perm((v)[3], (v)[2], 0x04000c0cu), tag, 0x56)
#define EPOCH() asm volatile("" : "+s"(ep0)); const unsigned tag = ((ep0 & 0x7F7F7F7Fu) + 0x01010101u) ^ (ep0 & 0x80808080u)
#define UNTIL(BADEXPR, RELOAD) do { if (ANY(BADEXPR)) { int spins_ = SPIN_CAP; _Pragma("nounroll") do { FENCE(); RELOAD; if (!ANY(BADEXPR)) break; asm volatile("" : "+s"(spins_)); } while (--spins_); } } while (0)

__device__ __forceinline__ void mksel(unsigned (&sel)[8], int l) {
#pragma unroll
  for (int d = 0; d < 4; ++d) { const bool on = ((l & 7) >> 1) == d;
    sel[d] = on ? ((l & 1) ? 0x01000c0cu : 0x0c0c0100u) : 0x0c0c0c0cu; sel[4 + d] = on ? ((l & 1) ? 0x03020c0cu : 0x0c0c0302u) : 0x0c0c0c0cu; }
}
__device__ __forceinline__ v4u diag(unsigned pair, const unsigned (&sel)[8], int hi) {
  return v4u{__builtin_amdgcn_perm(0u, pair, sel[4 * hi]), __builtin_amdgcn_perm(0u, pair, sel[4 * hi + 1]), __builtin_amdgcn_perm(0u, pair, sel[4 * hi + 2]), __builtin_amdgcn_perm(0u, pair, sel[4 * hi + 3])};
}
__device__ __forceinline__ float upr8(float v) { return __builtin_bit_cast(float, __builtin_amdgcn_update_dpp(__builtin_bit_cast(int, v), __builtin_bit_cast(int, v), R8, 0xC, 0xF, false)); }
__device__ __forceinline__ v2u diag_out(v4f d, unsigned tb0, unsigned tb1) {
  const float t0 = upr8(d[0]), t1 = upr8(d[1]), t2 = upr8(d[2]), t3 = upr8(d[3]);
  return v2u{d4(fold32(t0, t2), tb0), d4(fold32(t1, t3), tb1)};
}

__global__ void __launch_bounds__(NTHR, 3) fused_decode(const u16* __restrict__ x, const u16* __restrict__ wq, const u16* __restrict__ w1, const u16* __restrict__ vc, unsigned char* ws,
                                                        const u16* __restrict__ kc, const u16* __restrict__ wo, const u16* __restrict__ w2, const u16* __restrict__ b1,
                                                        const u16* __restrict__ b2, u16* __restrict__ out) {
  __shared__ __attribute__((aligned(128))) unsigned char lds_[L_END];
  LAS unsigned char* L = (LAS unsigned char*)lds_;
  const int b = blockIdx.x, g = b & 7, j = b >> 3, l = threadIdx.x & 63;
  int w = __builtin_amdgcn_readfirstlane(threadIdx.x >> 6);
  unsigned c0_; { u64 t_; asm volatile("s_memrealtime %0\n s_waitcnt lgkmcnt(0)" : "=s"(t_) :: "memory"); c0_ = (unsigned)t_; }
  { u64 pc; unsigned keep, t_;
    asm volatile("s_cmp_lg_u32 %3, 4\n\ts_cbranch_scc1 1f\n\ts_getpc_b64 %0\n\ts_mov_b32 %1, m0\n\ts_mov_b32 m0, %5\n\tv_lshlrev_b32 %2, 7, %4\n\t"
                 "global_load_lds_dword %2, %0\n\ts_mov_b32 m0, %1\n1:"
                 : "=&s"(pc), "=&s"(keep), "=&v"(t_) : "s"(w), "v"((j * 4 + (l & 3)) & 63), "i"(L_O) : "scc", "memory"); }
  const int kq = j & 3, rb_ = j >> 2, gn = (g + 1) & 7, gp = (g - 1) & 7;
  const int gq = l >> 4, n = l & 15;
  const bool hasw1 = rb_ != 3; const int jw = rb_ == 7 ? j - 16 : j;
  int kvo = (g * SK + (SK - WIN) + j * 128 + (((w - 1) * 2 + ((w - 1) >> 2) + j) & 7) * 16) * 256, l16 = l * 16, kof = (n >> 2) * 1024 + gq * 64 + (n & 3) * 16;
  int vo = (l >> 3) * (16 * 8192) + (l & 7) * 16, so = kq * (128 * 8192) + gn * 1024 + rb_ * 128;
  int vo1 = hasw1 ? (l >> 3) * (8 * FF * 2) + (l & 7) * 16 : OOB, so1 = g * (64 * FF * 2) + jw * 128;
  asm volatile("" : "+s"(kvo), "+s"(so), "+s"(so1));
  asm volatile("s_setprio 3" : "+v"(l16), "+v"(vo), "+v"(vo1), "+v"(kof));
  asm volatile("" :: "s"(x), "s"(kc), "s"(vc), "s"(wq), "s"(wo), "s"(w1), "s"(b1), "s"(w2), "s"(b2), "s"(out), "s"(ws));
  unsigned ep0 = 0;
  const rsrc_t rs = mk(ws, WS_END);
  v4u kv[4];
  if (w != 0) {
    const rsrc_t rk = mk(kc, 8u * SK * 256);
    if (w == 3) {
      unsigned keep;
      const unsigned po = (j * 16 + 8 + (l & 7)) * 8192 + gn * 1024 + 384 + ((l >> 3) & 1) * 512;
      asm volatile("s_mov_b32 %0, m0\n\ts_mov_b32 m0, %3\n\ts_nop 0\n\tglobal_load_lds_dword %1, %2 nt\n\ts_mov_b32 m0, %0" : "=&s"(keep) : "v"(po), "s"(wq), "i"(L_O + 256) : "memory");
    }
    ep0 = *(const CAS unsigned*)ws;
    __builtin_amdgcn_s_setprio(2);
    __builtin_amdgcn_s_barrier();
    SB();
    kv[0] = ldw(rk, kof, kvo); kv[1] = ldw(rk, kof + 256, kvo);
    SB();
  }
  asm volatile("" : "+s"(w));
  if (__builtin_expect(w == 0, 1)) {
    v4u r16[16], f8[8];
    const v4u xv = *(const GAS v4u*)((const GAS unsigned char*)x + (unsigned)l16);
    const unsigned epv = __builtin_amdgcn_raw_buffer_load_b32(rs, 0, 0, 0);
    { const rsrc_t r = mk(wq, 512 * 4096 * 2), r1 = mk(w1, 512 * FF * 2);
#pragma unroll
      for (int i = 0; i < 16; ++i) r16[i] = ldw(r, vo, so + i * 8192);
#pragma unroll
      for (int i = 0; i < 8; ++i) f8[i] = ldw(r1, vo1, so1 + i * (FF * 2)); }
    SB();
    __builtin_amdgcn_s_barrier();
    SB();
    unsigned sel[8]; mksel(sel, l);
    int gvo = (l & 8) ? OOB : (l & 7) * 32 + (l & 16) + (l >> 5) * 8, xra = L_XN + kq * 256 + (l >> 3) * 32, xrc = L_XN + g * 128 + (l >> 3) * 16;
    asm volatile("" : "+v"(sel[0]), "+v"(sel[1]), "+v"(sel[2]), "+v"(sel[3]), "+v"(sel[4]), "+v"(sel[5]), "+v"(sel[6]), "+v"(sel[7]), "+v"(gvo), "+v"(xra), "+v"(xrc));
    SB();
    float xf[8];
#pragma unroll
    for (int i = 0; i < 4; ++i) { xf[2 * i] = bflo(xv[i]); xf[2 * i + 1] = bfhi(xv[i]); }
    const float mean = sum64(((xf[0] + xf[1]) + (xf[2] + xf[3])) + ((xf[4] + xf[5]) + (xf[6] + xf[7]))) * (1.f / 512);
    float vs = 0.f;
#pragma unroll
    for (int i = 0; i < 8; ++i) { const float d = xf[i] - mean; vs = fmaf(d, d, vs); }
    const float var = sum64(vs) * (1.f / 512);
    const float mb = rb(mean), rsd = rcp(rb(__builtin_amdgcn_sqrtf(rb(rb(var) + 1e-5f))));
    v4u xn;
#pragma unroll
    for (int i = 0; i < 4; ++i) { const unsigned d = pk(xf[2 * i] - mb, xf[2 * i + 1] - mb); xn[i] = pk(bflo(d) * rsd, bfhi(d) * rsd); }
    *(LAS v4u*)(L + L_XN + l * 16) = xn;
    *(LAS float*)(L + L_M + (l & 3) * 4) = -__builtin_inff();
    const v4u xa = *(LAS v4u*)(L + xra), xb = *(LAS v4u*)(L + xra + 16), xc = *(LAS v4u*)(L + xrc);
    ep0 = __builtin_amdgcn_readfirstlane(epv);
    EPOCH();
    const unsigned tb0 = (tag >> ((l >> 5) * 16)) & 0xFFu, tb1 = (tag >> ((l >> 5) * 16 + 8)) & 0xFFu;
    { v4u bx[8];
#pragma unroll
      for (int i = 0; i < 8; ++i) bx[i] = diag(xa[i >> 1], sel, i & 1);
      SB();
      v4f d = {0.f, 0.f, 0.f, 0.f};
#pragma unroll
      for (int i = 0; i < 8; ++i) { d = mfma(bx[i], r16[i], d); bx[i] = diag(xb[i >> 1], sel, i & 1); SB(); }
#pragma unroll
      for (int i = 0; i < 8; ++i) d = mfma(bx[i], r16[8 + i], d);
      ST8(diag_out(d, tb0, tb1), gvo, WS_QG + gn * 8192 + kq * 2048 + rb_ * 256);
      SB();
      __builtin_amdgcn_s_barrier();
      SB();
    }
    { v4f d = {0.f, 0.f, 0.f, 0.f};
#pragma unroll
      for (int i = 0; i < 8; ++i) d = mfma(diag(xc[i >> 1], sel, i & 1), f8[i], d);
      ST8(diag_out(d, tb0, tb1), hasw1 ? gvo : OOB, WS_HG + g * (FF * 4) + jw * 256); }
    const int hr0 = gp * 224 + rb_ * 28, hu = min(l & 7, 6), ocol = (b & 63) * 8 + (l & 3) * 2 + (l >> 5);
    int mlo = (l & 51) == 0 ? (n >> 2) * 512 : OOB, mls = WS_ML + g * 2048 + j * 16, hvo = (l >> 3) * (FF * 4) + hu * 16, hso = WS_HG + hr0 * 4;
    int pvo = (l & 31) * 16, pso = WS_ML + gp * 2048 + (j >> 3) * 512;
    asm volatile("" : "+v"(mlo), "+v"(hvo), "+v"(pvo));
    asm volatile("" : "+s"(mls), "+s"(hso), "+s"(pso));
    const float b1v = bflo(b1[hr0 + hu * 4 + gq]), b2v = bflo(b2[ocol]), xo = bflo(x[ocol]);
    BAR();
    BAR();
    v4u hg = LD16(hvo, hso);
    SB();
    BAR();
    { const v2u mm = *(LAS v2u*)(L + L_MX + l * 8);
      const v4f s0 = *(LAS v4f*)(L + L_S + l * 16), s1 = *(LAS v4f*)(L + L_S + 1024 + l * 16);
      const float m = *(LAS float*)(L + L_M + (n >> 2) * 4);
      float ls = fmaf((s0[0] + s0[1]) + (s0[2] + s0[3]), ex2(uf(mm[0]) - m), ((s1[0] + s1[1]) + (s1[2] + s1[3])) * ex2(uf(mm[1]) - m));
      ls += dpp<X1>(ls); ls += dpp<X2>(ls); ls = fold16(ls, ls); ls = fold32(ls, ls);
      ST16((v4u{__float_as_uint(m), tag, __float_as_uint(ls), tag}), mlo, mls);
    }
    BAR();
    {
      if (__builtin_expect(ANY(DBAD(hg)), 0)) UNTIL(DBAD(hg), hg = LD16(hvo, hso));
      float hs = fold16(fold32(gval(hg[0]), gval(hg[2])), fold32(gval(hg[1]), gval(hg[3]))); hs += dpp<R8>(hs);
      const float hp = rb(rb(hs) + b1v);
      const float u2 = 1.5957691216057308f * fmaf(0.044715f * hp * hp, hp, hp);
      const float ge = hp * rcp(1.f + ex2(-u2 * LOG2E));
      *(LAS u16*)(L + L_H + (l & 7) * 8 + gq * 2) = ((l & 7) < 7) ? (u16)pk(ge, ge) : (u16)0;
    }
    __builtin_amdgcn_s_sleep(8);
    v4u pv = LD16(pvo, pso);
    SB();
    BAR();
    {
      UNTIL(TBAD(pv), pv = LD16(pvo, pso));
      const float m = uf(pv[0]);
      const float M = max16(mx_r4(mx_r8(mx_x2(mx_x1(m)))));
      const float e = ex2(m - M);
      float ls = e * uf(pv[2]);
      ls += dpp<X1>(ls); ls += dpp<X2>(ls); ls += dpp<HM>(ls); ls += dpp<R8>(ls); ls = fold16(ls, ls);
      *(LAS float*)(L + L_WG + (l & 31) * 4) = e * rcp(ls);
    }
    BAR();
    if (b < 64) {
      const int fo = WS_FB + b * 4096;
      v4u f0 = LD16(l16, fo), f1 = LD16(l16 + 1024, fo), f2 = LD16(l16 + 2048, fo), f3 = LD16(l16 + 3072, fo);
      UNTIL(TBAD(f0) | TBAD(f1) | TBAD(f2) | TBAD(f3), (f0 = LD16(l16, fo), f1 = LD16(l16 + 1024, fo), f2 = LD16(l16 + 2048, fo), f3 = LD16(l16 + 3072, fo)));
      float t = fold32((uf(f0[0]) + uf(f1[0])) + (uf(f2[0]) + uf(f3[0])), (uf(f0[2]) + uf(f1[2])) + (uf(f2[2]) + uf(f3[2])));
      t = fold16(t, t); t += dpp<R8>(t); t += dpp<R4>(t);
      const float f = rb(rb(t) + b2v);
      float gt = rb(f * rcp(1.f + ex2(-f * LOG2E)));
      int frd = L_V + ((l >> 5) * 576 + (l & 3) * 136 + ((l >> 2) & 7) * 16) * 4;
      int ovo = (l & 28) ? OOB : ocol * 2; float xo_ = xo;
      asm volatile("" : "+v"(gt), "+v"(frd), "+v"(ovo), "+v"(xo_));
      BAR();
      const v4f u0 = *(LAS v4f*)(L + frd), u1 = *(LAS v4f*)(L + frd + 16);
      const v4f u = (u0 + u1) + (*(LAS v4f*)(L + frd + 32) + *(LAS v4f*)(L + frd + 48));
      float v = (u[0] + u[2]) + (u[1] + u[3]);
      v = fold16(v, v); v += dpp<R8>(v); v += dpp<R4>(v);
      const float o = xo_ + rb(rb(v) * gt);
      __builtin_amdgcn_raw_buffer_store_b16((u16)pk(o, o), mk(out, 1024), ovo, 0, 16);
      if (b == 0 && l == 0) __hip_atomic_store((GAS unsigned*)ws, tag, __ATOMIC_RELAXED, __HIP_MEMORY_SCOPE_AGENT);
    }
  } else {
    const int a = w - 1, a3 = a & 3;
    int obo = (n & 3) == 0 ? (n >> 2) * 16384 + gq * 16 : OOB;
    int sbo = (l & 16) ? OOB : (l >> 5) * 16384 + (n >> 3) * 8192 + (n & 7) * 8;
    int obs = WS_OB + g * 65536 + a3 * 4096 + j * 128, sbs = WS_SB + ((j & 1) * 32 + a * 4) * 8192 + (g * 16 + (j >> 1)) * 64;
    int fbs = WS_FB + (kq * 16 + a * 2) * 4096 + (g * 8 + rb_) * 64;
    int sadr = L_S + (a & 1) * 1024 + gq * 256 + (n & 3) * 64 + (a >> 1) * 16 + (n >> 2) * 4, qadr = L_Q + (n & 3) * QSTR + gq * 64 + (n >> 2) * 16;
    int pso = WS_OB + gp * 65536 + (j >> 1) * 4096 + a3 * 1024, pvo = a >= 4 ? l * 16 : OOB;
    int oadr = L_O + n * 80 + gq * 16, owr = L_O + (a3 * 8 + (l >> 3)) * 80 + (l & 7) * 8;
    int mad = L_M + (n & 3) * 4, l8 = l * 8, mrd = L_M + (n >> 2) * 4;
    asm volatile("" : "+v"(mad), "+v"(l8), "+v"(mrd));
    int fso = WS_SB + b * 8192 + a * 1024, fadr = L_V + ((l & 3) * 136 + a * 16 + (l >> 2)) * 4;
    int vwr = L_V + (a * 16 + gq) * VSTR + n * 16;
    int qso = WS_QG + g * 8192 + a3 * 512, qvo = a >= 4 ? (l >> 5) * 4096 + (l & 31) * 16 : OOB;
    int mxw = L_MX + gq * 128 + (n & 3) * 32 + a * 4, qwr = L_Q + a3 * QSTR + (l & 31) * 8 + (l >> 5) * 4;
    int wov = (l >> 5) * 1024 + ((l & 31) ^ ((l >> 5) * 2)) * 16;
    asm volatile("" : "+v"(wov));
    int wos = (gp * 512 + (j >> 1) * 32 + a3 * 8) * 1024 + (j & 1) * 512, w2s = (gp * 224 + rb_ * 28 + a3 * 8) * 1024 + kq * 256, wvo = gq * 1024 + (n ^ (gq * 2)) * 16;
    int w2t = w2s + (a3 == 3 ? 0 : 4096);
    int wl0 = L_W + a3 * 4 * WBLK, wl1 = L_W + (16 + a3 * 2) * WBLK;
    v4u so_ = {(unsigned)(u64)wo, (unsigned)((u64)wo >> 32) & 0xFFFFu, 4096u * 512 * 2, 0x20000u}, s2_ = {(unsigned)(u64)w2, (unsigned)((u64)w2 >> 32) & 0xFFFFu, FF * 512u * 2, 0x20000u};
    asm volatile("" : "+v"(obo), "+v"(sbo), "+v"(sadr), "+v"(qadr), "+v"(oadr), "+v"(owr), "+v"(vwr), "+v"(fadr), "+v"(qvo), "+v"(mxw), "+v"(qwr), "+v"(pvo), "+v"(wvo));
    asm volatile("" : "+s"(obs), "+s"(sbs), "+s"(pso), "+s"(qso), "+s"(fso), "+s"(wos), "+s"(w2s), "+s"(so_), "+s"(s2_), "+s"(wl0), "+s"(wl1), "+s"(w2t));
    const rsrc_t rv = mk(vc, 8u * SK * 256);
    int tadr = L_V + (gq * 4 + (n >> 2)) * VSTR + a3 * 64 + (n & 3) * 8;
    asm volatile("" : "+v"(tadr));
    EPOCH();
    v4u o0 = {0u, tag, 0u, tag}, o1 = o0; u64 lead = 0x000F000F000F000Full;
    asm volatile("" : "+v"(o0), "+v"(o1)); asm volatile("" : "+s"(lead));
    SB();
#define WDO() do { unsigned keep; asm volatile("s_mov_b32 %0, m0\n\ts_mov_b32 m0, %4\n\ts_nop 0\n\t" \
      "buffer_load_dwordx4 %2, %3, %1 offen nt lds\n\ts_add_u32 m0, m0, %5\n\ts_add_u32 %1, %1, 0x800\n\tbuffer_load_dwordx4 %2, %3, %1 offen nt lds\n\ts_add_u32 m0, m0, %5\n\ts_add_u32 %1, %1, 0x800\n\t" \
      "buffer_load_dwordx4 %2, %3, %1 offen nt lds\n\ts_add_u32 m0, m0, %5\n\ts_add_u32 %1, %1, 0x800\n\tbuffer_load_dwordx4 %2, %3, %1 offen nt lds\n\ts_mov_b32 m0, %0" \
      : "=&s"(keep), "+s"(wos) : "v"(wov), "s"(so_), "s"(wl0), "i"(WBLK) : "memory", "scc"); } while (0)
#define WD2() do { unsigned keep; asm volatile("s_mov_b32 %0, m0\n\ts_mov_b32 m0, %4\n\ts_nop 0\n\t" \
      "buffer_load_dwordx4 %2, %3, %1 offen nt lds\n\ts_add_u32 m0, m0, %5\n\ts_nop 0\n\tbuffer_load_dwordx4 %2, %3, %6 offen nt lds\n\ts_mov_b32 m0, %0" \
      : "=&s"(keep) : "s"(w2s), "v"(wvo), "s"(s2_), "s"(wl1), "i"(WBLK), "s"(w2t) : "memory", "scc"); } while (0)
    { const rsrc_t rk = mk(kc, 8u * SK * 256);
      if (a >= 4) __builtin_amdgcn_s_barrier();
      SB();
      kv[2] = ldw(rk, kof + 512, kvo); kv[3] = ldw(rk, kof + 768, kvo);
      SB();
      if (a < 4) __builtin_amdgcn_s_barrier(); }
    WAITV(3);
    SB();
    if (a >= 4) { WAITV(1); __builtin_amdgcn_s_barrier(); }
    SB();
    v4u q0 = LD16(qvo, qso), q1 = LD16(qvo, qso + 2048);
    SB();
    if (a >= 4) __builtin_amdgcn_s_sleep(1);
    v4u vv[4];
#pragma unroll
    for (int i = 0; i < 4; ++i) vv[i] = ldw(rv, l16 + i * 1024, kvo);
    SB();
    if (a < 4) __builtin_amdgcn_s_barrier();
    SB();
    WAITV(4);
#define QSUM() do { const v4f u_ = __builtin_bit_cast(v4f, q0 & 0xFFFFFF00u) + __builtin_bit_cast(v4f, q1 & 0xFFFFFF00u); float c_, d_; const float a_ = sw32(u_[0], u_[2], c_), b_ = sw32(u_[1], u_[3], d_); \
        *(LAS unsigned*)(L + qwr) = pk(a_ + c_, b_ + d_); } while (0)
    if (a >= 4) {
      QSUM();
      SB();
      if (__builtin_expect(ANY(DBAD(q0) | DBAD(q1)), 0)) {
        UNTIL(DBAD(q0) | DBAD(q1), (q0 = LD16(qvo, qso), q1 = LD16(qvo, qso + 2048)));
        QSUM();
      }
    }
    SB();
    if (a < 4) {
#pragma unroll
      for (int i = 0; i < 4; ++i) *(LAS v4u*)(L + vwr + i * 4 * VSTR) = vv[i];
    }
    if (a == 0 && b < 8 && l == 0) __hip_atomic_store((GAS unsigned*)(ws + (3u << 20)) + (__builtin_amdgcn_s_getreg(20 | (3 << 11)) & 7) * 64, c0_, __ATOMIC_RELAXED, __HIP_MEMORY_SCOPE_AGENT);
    BAR();
    {
      const v4u qf = *(LAS v4u*)(L + qadr);
      if (a < 4) WD2();
      v4f sc[4];
#pragma unroll
      for (int i = 0; i < 4; ++i) { sc[i] = mfma(kv[i], qf, v4f{0.f, 0.f, 0.f, 0.f}); SB(); }
      float t, m;
#define DADD(src, acc, rot, bank) "v_add_f32_dpp %0, " src ", " acc " row_ror:" rot " row_mask:0xf bank_mask:" bank "\n\t"
      asm volatile("s_nop 4\n\t"
                   DADD("%3", "%2", "12", "0x1") DADD("%6", "%7", "4", "0x2") DADD("%10", "%12", "8", "0x4") DADD("%14", "%17", "12", "0x8")
                   DADD("%4", "%0", "8", "0x1") DADD("%8", "%0", "12", "0x2") DADD("%11", "%0", "4", "0x4") DADD("%15", "%0", "8", "0x8")
                   DADD("%5", "%0", "4", "0x1") DADD("%9", "%0", "8", "0x2") DADD("%13", "%0", "12", "0x4") DADD("%16", "%0", "4", "0x8")
                   "v_cvt_pk_bf16_f32 %0, 0, %0\n\tv_mul_f32 %0, 0x3db504f3, %0\n\tv_cvt_pk_bf16_f32 %0, 0, %0\n\tv_mul_f32 %0, 0x3fb8aa3b, %0\n\ts_nop 1\n\t"
                   "v_max_f32_dpp %1, %0, %0 row_ror:8 row_mask:0xf bank_mask:0xf\n\ts_nop 1\n\t"
                   "v_max_f32_dpp %1, %1, %1 row_ror:4 row_mask:0xf bank_mask:0xf\n\t"
                   "v_sub_f32 %0, %0, %1\n\tv_exp_f32 %0, %0\n\t"
                   "s_mov_b64 exec, %21\n\tds_max_f32 %18, %1\n\tds_write_b32 %19, %1\n\ts_mov_b64 exec, -1\n\tds_write_b32 %20, %0"
                   : "=&v"(t), "=&v"(m)
                   : "v"(sc[0][0]), "v"(sc[0][1]), "v"(sc[0][2]), "v"(sc[0][3]), "v"(sc[1][0]), "v"(sc[1][1]), "v"(sc[1][2]), "v"(sc[1][3]),
                     "v"(sc[2][0]), "v"(sc[2][1]), "v"(sc[2][2]), "v"(sc[2][3]), "v"(sc[3][0]), "v"(sc[3][1]), "v"(sc[3][2]), "v"(sc[3][3]),
                     "v"(mad), "v"(mxw), "v"(sadr), "s"(lead)
                   : "memory");
#pragma unroll
      for (int i = 0; i < 5; ++i) __builtin_amdgcn_s_setprio(2);
    }
    v2u vt[16];
    if (a < 4) {
#pragma unroll
      for (int i = 0; i < 8; ++i) vt[(i & 3) + (i >> 2) * 8] = trd(L + tadr + (i & 3) * 16 * VSTR + (i >> 2) * 32);
    }
    BAR();
    if (a < 4) {
      const v2u mm = *(LAS v2u*)(L + L_MX + l8);
      const float m = *(LAS float*)(L + mrd);
      const v4f s0 = *(LAS v4f*)(L + L_S + l16), s1 = *(LAS v4f*)(L + L_S + 1024 + l16);
      const v4f p0 = s0 * ex2(uf(mm[0]) - m), p1 = s1 * ex2(uf(mm[1]) - m);
      const v4u pb = {pk(p0[0], p0[1]), pk(p0[2], p0[3]), pk(p1[0], p1[1]), pk(p1[2], p1[3])};
      const v4f z = {0.f, 0.f, 0.f, 0.f};
      v4f d0 = mfma(cat(vt[0], vt[1]), pb, z), d1 = mfma(cat(vt[8], vt[9]), pb, z);
      const v4u pb1 = dpp4<0x55>(pb), pb2 = dpp4<0xAA>(pb);
      SB();
      d0 = mfma(cat(vt[2], vt[3]), pb1, d0); d1 = mfma(cat(vt[10], vt[11]), pb1, d1);
      v4u pb3 = dpp4<0xFF>(pb);
      asm volatile("" : "+v"(pb3) : "v"(pb1), "v"(vt[0]), "v"(vt[1]), "v"(vt[8]), "v"(vt[9]));
      SB();
      WDO();
      BAR();
#pragma unroll
      for (int i = 0; i < 8; ++i) vt[4 + (i & 1) + ((i >> 2) & 1) * 2 + ((i >> 1) & 1) * 8] = trd(L + tadr + (4 + (i & 1) + ((i >> 2) & 1) * 2) * 16 * VSTR + ((i >> 1) & 1) * 32);
      d0 = mfma(cat(vt[4], vt[5]), pb2, d0); d1 = mfma(cat(vt[12], vt[13]), pb2, d1);
      d0 = mfma(cat(vt[6], vt[7]), pb3, d0); d1 = mfma(cat(vt[14], vt[15]), pb3, d1);
      o0[0] = pk(d0[0], d0[1]); o0[2] = pk(d0[2], d0[3]); o1[0] = pk(d1[0], d1[1]); o1[2] = pk(d1[2], d1[3]);
      ST16(o0, obo, obs);
      ST16(o1, obo + 64, obs);
      asm volatile("s_waitcnt vmcnt(2)" ::: "memory");
    }
    SB();
    if (a >= 4) {
#pragma unroll
      for (int i = 0; i < 4; ++i) *(LAS v4u*)(L + vwr + i * 4 * VSTR) = vv[i];
      BAR();
      __builtin_amdgcn_s_sleep(20);
    }
    SB();
    v4u pv = LD16(pvo, pso);
    BAR();
    if (a >= 4 && ANY(TBAD(pv))) pv = LD16(pvo, pso);
    SB();
    v2u wt[4];
    const int wrd = L_W + gq * 2 * WBLK + (n >> 2) * 256 + (a ^ (n >> 2)) * 32 + (n & 3) * 8;
#pragma unroll
    for (int i = 0; i < 4; ++i) wt[i] = trd(L + L_W + (gq * 4 + (i & 1) * 2 + (n >> 3)) * WBLK + ((n >> 2) & 1) * 512 + ((a * 2 + (i >> 1)) ^ ((n >> 2) & 1)) * 32 + (n & 3) * 8);
    SB();
    WAITV(0);
    if (a < 4) {
      const v4u hf = *(LAS v4u*)(L + L_H + gq * 16);
      LAS unsigned char* p = L + wrd + 16 * WBLK;
      const v4f z = {0.f, 0.f, 0.f, 0.f};
      const v4f f0 = mfma(hf, cat(trd(p), trd(p + WBLK)), z), f1 = mfma(hf, cat(trd(p + 128), trd(p + 128 + WBLK)), z);
      ST8((v2u{__float_as_uint(gq ? f1[0] : f0[0]), tag}), l < 32 ? gq * 32768 + (n >> 3) * 4096 + (n & 7) * 8 : OOB, fbs);
    }
    SB();
    if (a >= 4) {
#define OWR() (((LAS unsigned*)(L + owr))[0] = pv[0], ((LAS unsigned*)(L + owr))[1] = pv[2])
      OWR();
      UNTIL(TBAD(pv), (pv = LD16(pvo, pso), OWR()));
    }
    BAR();
    {
      const v4u of0 = *(LAS v4u*)(L + oadr), of1 = *(LAS v4u*)(L + oadr + 16 * 80);
      const v4f w0 = *(LAS v4f*)(L + L_WG + gq * 16), w1_ = *(LAS v4f*)(L + L_WG + 64 + gq * 16);
      SB();
      const v4f z = {0.f, 0.f, 0.f, 0.f};
      const v4f d00 = mfma(of0, cat(wt[0], wt[1]), z), d01 = mfma(of0, cat(wt[2], wt[3]), z), d10 = mfma(of1, cat(wt[0], wt[1]), z), d11 = mfma(of1, cat(wt[2], wt[3]), z);
      SB();
      v2f c0 = d00.hi * w0.hi + d00.lo * w0.lo, c1 = d01.hi * w0.hi + d01.lo * w0.lo;
      c0 = d10.hi * w1_.hi + (d10.lo * w1_.lo + c0); c1 = d11.hi * w1_.hi + (d11.lo * w1_.lo + c1);
      float e = fold32(c0[0] + c0[1], c1[0] + c1[1]);
      e = fold16(e, e);
      ST8((v2u{__float_as_uint(e), tag}), sbo, sbs);
    }
    if (b < 64) {
      __builtin_amdgcn_s_sleep(10); if (a == g) __builtin_amdgcn_s_sleep(10);
      v4u sv = LD16(l16, fso);
#define SWR() (((LAS unsigned*)(L + fadr))[0] = sv[0], ((LAS unsigned*)(L + fadr))[576] = sv[2])
      SWR();
      UNTIL(TBAD(sv), (sv = LD16(l16, fso), SWR()));
      BAR();
      if (a == 0 && b == 0) { int t_[8]; for (int i_ = 0; i_ < 8; ++i_) t_[i_] = (int)(__builtin_amdgcn_readfirstlane(__hip_atomic_load((GAS unsigned*)(ws + (3u << 20)) + i_ * 64, __ATOMIC_RELAXED, __HIP_MEMORY_SCOPE_AGENT)) - c0_);
        const int lo_ = min(min(min(t_[0], t_[1]), min(t_[2], t_[3])), min(min(t_[4], t_[5]), min(t_[6], t_[7]))), hi_ = max(max(t_[0], t_[1]), max(t_[2], t_[3]));
        const unsigned d_ = 10 * (unsigned)(hi_ - lo_), e_ = (unsigned)__builtin_amdgcn_s_memrealtime(); while ((unsigned)__builtin_amdgcn_s_memrealtime() - e_ < d_ && d_ < 20000u) {} }
    }
  }
}

extern "C" void kernel_launch(void* const* d_in, const int* in_sizes, int n_in, void* d_out, int out_size, void* d_ws, size_t ws_size, hipStream_t stream) {
  fused_decode<<<256, NTHR, 0, stream>>>((const u16*)d_in[0], (const u16*)d_in[3], (const u16*)d_in[6], (const u16*)d_in[2], (unsigned char*)d_ws, (const u16*)d_in[1],
                                         (const u16*)d_in[4], (const u16*)d_in[8], (const u16*)d_in[7], (const u16*)d_in[9], (u16*)d_out);
}
```

```cpp
#include <hip/hip_runtime.h>
#include <hip/hip_bf16.h>

typedef unsigned short u16;
typedef unsigned long long u64;
typedef unsigned v4u __attribute__((ext_vector_type(4)));
typedef unsigned v2u __attribute__((ext_vector_type(2)));
typedef float v4f __attribute__((ext_vector_type(4)));
typedef short s16x4 __attribute__((ext_vector_type(4)));
typedef __bf16 bf16x2 __attribute__((ext_vector_type(2)));
typedef __bf16 bf16x8 __attribute__((ext_vector_type(8)));
typedef __amdgpu_buffer_rsrc_t rsrc_t;
#define GAS __attribute__((address_space(1)))
#define CAS __attribute__((address_space(4)))
#define LAS __attribute__((address_space(3)))

constexpr int SK = 32768, WIN = 4096, FF = 1792;
constexpr int NTHR = 576;
constexpr unsigned WS_QG = 4096;
constexpr unsigned WS_HG = WS_QG + 65536;
constexpr unsigned WS_ML = WS_HG + 57344;
constexpr unsigned WS_OB = WS_ML + 16384;
constexpr unsigned WS_SB = WS_OB + 524288;
constexpr unsigned WS_FB = WS_SB + 524288;
constexpr unsigned WS_END = WS_FB + 262144;
constexpr int VSTR = 288, L_V = 0, L_O = 128 * VSTR, L_Q = L_O + 4096, QSTR = 320, L_S = L_Q + 4 * QSTR, L_XN = L_S + 2048, L_MX = L_XN + 1024, L_M = L_MX + 512, L_H = L_M + 32,
              L_WG = L_H + 64, L_W = L_WG + 128, WBLK = 1088, L_END = L_W + 24 * WBLK;
constexpr unsigned SPIN_CAP = 1u << 18;
constexpr float LOG2E = 1.4426950408889634f;
constexpr int OOB = 0x40000000;

__device__ __forceinline__ rsrc_t mk(const void* p, unsigned bytes) { return __builtin_amdgcn_make_buffer_rsrc((void*)p, 0, bytes, 0x00020000); }
__device__ __forceinline__ v4u ldw(rsrc_t r, int voff, int soff) { return __builtin_amdgcn_raw_buffer_load_b128(r, voff, soff, 2); }
__device__ __forceinline__ float uf(unsigned u) { return __uint_as_float(u); }
__device__ __forceinline__ float bflo(unsigned u) { return __uint_as_float(u << 16); }
__device__ __forceinline__ float bfhi(unsigned u) { return __uint_as_float(u & 0xFFFF0000u); }
__device__ __forceinline__ unsigned pk(float lo, float hi) { bf16x2 r = {(__bf16)lo, (__bf16)hi}; return __builtin_bit_cast(unsigned, r); }
typedef float v2f __attribute__((ext_vector_type(2)));
__device__ __forceinline__ float rb(float f) { return __builtin_bit_cast(float, __builtin_convertvector((v2f{0.f, f}), bf16x2)); }
__device__ __forceinline__ v4f mfma(v4u a, v4u b, v4f c) { return __builtin_amdgcn_mfma_f32_16x16x32_bf16(__builtin_bit_cast(bf16x8, a), __builtin_bit_cast(bf16x8, b), c, 0, 0, 0); }
template <int CTRL> __device__ __forceinline__ unsigned dppu(unsigned x) { return (unsigned)__builtin_amdgcn_mov_dpp((int)x, CTRL, 0xf, 0xf, true); }
template <int CTRL> __device__ __forceinline__ float dpp(float x) { return __uint_as_float(dppu<CTRL>(__float_as_uint(x))); }
template <int CTRL> __device__ __forceinline__ v4u dpp4(v4u x) { return v4u{dppu<CTRL>(x[0]), dppu<CTRL>(x[1]), dppu<CTRL>(x[2]), dppu<CTRL>(x[3])}; }
constexpr int X1 = 0xB1, X2 = 0x4E, HM = 0x141, R4 = 0x124, R8 = 0x128;
__device__ __forceinline__ float sw16(float a, float b, float& o) { auto r = __builtin_amdgcn_permlane16_swap(__float_as_uint(a), __float_as_uint(b), false, false); o = uf(r[1]); return uf(r[0]); }
__device__ __forceinline__ float sw32(float a, float b, float& o) { auto r = __builtin_amdgcn_permlane32_swap(__float_as_uint(a), __float_as_uint(b), false, false); o = uf(r[1]); return uf(r[0]); }
__device__ __forceinline__ float fold16(float a, float b) { float o; const float p = sw16(a, b, o); return p + o; }
__device__ __forceinline__ float fold32(float a, float b) { float o; const float p = sw32(a, b, o); return p + o; }
__device__ __forceinline__ float fmx(float a, float b) { return __builtin_elementwise_maximum(a, b); }
__device__ __forceinline__ float max16(float a) { float o; const float p = sw16(a, a, o); return fmx(p, o); }
#define MAXDPP(name, ctl) __device__ __forceinline__ float name(float m) { float r; asm("s_nop 1\n\tv_max_f32_dpp %0, %1, %1 " ctl " row_mask:0xf bank_mask:0xf" : "=v"(r) : "v"(m)); return r; }
MAXDPP(mx_r8, "row_ror:8") MAXDPP(mx_r4, "row_ror:4") MAXDPP(mx_x1, "quad_perm:[1,0,3,2]") MAXDPP(mx_x2, "quad_perm:[2,3,0,1]")
__device__ __forceinline__ float sum64(float v) { v += dpp<X1>(v); v += dpp<X2>(v); v += dpp<HM>(v); v += dpp<R8>(v); v = fold16(v, v); return fold32(v, v); }
__device__ __forceinline__ v2u trd(LAS unsigned char* p) { return __builtin_bit_cast(v2u, __builtin_amdgcn_ds_read_tr16_b64_v4i16((LAS s16x4*)p)); }
__device__ __forceinline__ v4u cat(v2u a, v2u b) { return v4u{a[0], a[1], b[0], b[1]}; }
__device__ __forceinline__ float ex2(float x) { return __builtin_amdgcn_exp2f(x); }
__device__ __forceinline__ float rcp(float x) { return __builtin_amdgcn_rcpf(x); }
__device__ __forceinline__ float gval(unsigned d) { return __uint_as_float(d & 0xFFFFFF00u); }
__device__ __forceinline__ unsigned d4(float f, unsigned tagbyte) { return ((__float_as_uint(f) + 0x80u) & 0xFFFFFF00u) | tagbyte; }
#define SB() __builtin_amdgcn_sched_barrier(0)
#define BAR() do { asm volatile("s_waitcnt lgkmcnt(0)" ::: "memory"); __builtin_amdgcn_s_barrier(); asm volatile("" ::: "memory"); } while (0)
#define FENCE() asm volatile("" ::: "memory")
#define WAITV(n) __builtin_amdgcn_s_waitcnt(0x0F70 | (n))
#define LD16(off, soff) __builtin_amdgcn_raw_buffer_load_b128(rs, (off), (soff), 16)
#define ST16(v, off, soff) __builtin_amdgcn_raw_buffer_store_b128((v), rs, (off), (soff), 16)
#define ST8(v, off, soff) __builtin_amdgcn_raw_buffer_store_b64((v), rs, (off), (soff), 16)
#define TBAD(v) __builtin_amdgcn_bitop3_b32((v)[1], (v)[3], tag, 0x7E)
#define ANY(t) (__builtin_amdgcn_ballot_w64((t) != 0) != 0)
#define DBAD(v) __builtin_amdgcn_bitop3_b32(__builtin_amdgcn_perm((v)[1], (v)[0], 0x0c0c0400u), __builtin_amdgcn_perm((v)[3], (v)[2], 0x04000c0cu), tag, 0x56)
#define EPOCH() asm volatile("" : "+s"(ep0)); const unsigned tag = ((ep0 & 0x7F7F7F7Fu) + 0x01010101u) ^ (ep0 & 0x80808080u)
#define UNTIL(BADEXPR, RELOAD) do { if (ANY(BADEXPR)) { int spins_ = SPIN_CAP; _Pragma("nounroll") do { FENCE(); RELOAD; if (!ANY(BADEXPR)) break; asm volatile("" : "+s"(spins_)); } while (--spins_); } } while (0)

__device__ __forceinline__ void mksel(unsigned (&sel)[8], int l) {
#pragma unroll
  for (int d = 0; d < 4; ++d) { const bool on = ((l & 7) >> 1) == d;
    sel[d] = on ? ((l & 1) ? 0x01000c0cu : 0x0c0c0100u) : 0x0c0c0c0cu; sel[4 + d] = on ? ((l & 1) ? 0x03020c0cu : 0x0c0c0302u) : 0x0c0c0c0cu; }
}
__device__ __forceinline__ v4u diag(unsigned pair, const unsigned (&sel)[8], int hi) {
  return v4u{__builtin_amdgcn_perm(0u, pair, sel[4 * hi]), __builtin_amdgcn_perm(0u, pair, sel[4 * hi + 1]), __builtin_amdgcn_perm(0u, pair, sel[4 * hi + 2]), __builtin_amdgcn_perm(0u, pair, sel[4 * hi + 3])};
}
__device__ __forceinline__ float upr8(float v) { return __builtin_bit_cast(float, __builtin_amdgcn_update_dpp(__builtin_bit_cast(int, v), __builtin_bit_cast(int, v), R8, 0xC, 0xF, false)); }
__device__ __forceinline__ v2u diag_out(v4f d, unsigned tb0, unsigned tb1) {
  const float t0 = upr8(d[0]), t1 = upr8(d[1]), t2 = upr8(d[2]), t3 = upr8(d[3]);
  return v2u{d4(fold32(t0, t2), tb0), d4(fold32(t1, t3), tb1)};
}

__global__ void __launch_bounds__(NTHR, 3) fused_decode(const u16* __restrict__ x, const u16* __restrict__ wq, const u16* __restrict__ w1, const u16* __restrict__ vc, unsigned char* ws,
                                                        const u16* __restrict__ kc, const u16* __restrict__ wo, const u16* __restrict__ w2, const u16* __restrict__ b1,
                                                        const u16* __restrict__ b2, u16* __restrict__ out) {
  __shared__ __attribute__((aligned(128))) unsigned char lds_[L_END];
  LAS unsigned char* L = (LAS unsigned char*)lds_;
  const int b = blockIdx.x, g = b & 7, j = b >> 3, l = threadIdx.x & 63;
  int w = __builtin_amdgcn_readfirstlane(threadIdx.x >> 6);
  const unsigned c0_ = (unsigned)__builtin_amdgcn_s_memrealtime();
  { u64 pc; unsigned keep, t_;
    asm volatile("s_cmp_lg_u32 %3, 4\n\ts_cbranch_scc1 1f\n\ts_getpc_b64 %0\n\ts_mov_b32 %1, m0\n\ts_mov_b32 m0, %5\n\tv_lshlrev_b32 %2, 7, %4\n\t"
                 "global_load_lds_dword %2, %0\n\ts_mov_b32 m0, %1\n1:"
                 : "=&s"(pc), "=&s"(keep), "=&v"(t_) : "s"(w), "v"((j * 4 + (l & 3)) & 63), "i"(L_O) : "scc", "memory"); }
  const int kq = j & 3, rb_ = j >> 2, gp = g ^ 4;
  const int gq = l >> 4, n = l & 15;
  const bool hasw1 = rb_ != 3; const int jw = rb_ == 7 ? j - 16 : j;
  int kvo = (g * SK + (SK - WIN) + j * 128 + (((w - 1) * 2 + ((w - 1) >> 2) + j) & 7) * 16) * 256, l16 = l * 16, kof = (n >> 2) * 1024 + gq * 64 + (n & 3) * 16;
  int vo = (l >> 3) * (16 * 8192) + (l & 7) * 16, so = kq * (128 * 8192) + g * 1024 + rb_ * 128;
  int vo1 = hasw1 ? (l >> 3) * (8 * FF * 2) + (l & 7) * 16 : OOB, so1 = g * (64 * FF * 2) + jw * 128;
  asm volatile("" : "+s"(kvo), "+s"(so), "+s"(so1));
  asm volatile("s_setprio 3" : "+v"(l16), "+v"(vo), "+v"(vo1), "+v"(kof));
  asm volatile("" :: "s"(x), "s"(kc), "s"(vc), "s"(wq), "s"(wo), "s"(w1), "s"(b1), "s"(w2), "s"(b2), "s"(out), "s"(ws));
  unsigned ep0 = 0;
  const rsrc_t rs = mk(ws, WS_END);
  v4u kv[4];
  if (w != 0) {
    const rsrc_t rk = mk(kc, 8u * SK * 256);
    if (w == 3) {
      unsigned keep;
      const unsigned po = (j * 16 + 8 + (l & 7)) * 8192 + g * 1024 + 384 + ((l >> 3) & 1) * 512;
      asm volatile("s_mov_b32 %0, m0\n\ts_mov_b32 m0, %3\n\ts_nop 0\n\tglobal_load_lds_dword %1, %2 nt\n\ts_mov_b32 m0, %0" : "=&s"(keep) : "v"(po), "s"(wq), "i"(L_O + 256) : "memory");
    }
    ep0 = *(const CAS unsigned*)ws;
    __builtin_amdgcn_s_setprio(2);
    __builtin_amdgcn_s_barrier();
    SB();
    if (w <= 4) {
#pragma unroll
      for (int k = 0; k < 4; ++k) kv[k] = ldw(rk, kof + k * 256, kvo); }
  }
  asm volatile("" : "+s"(w));
  if (__builtin_expect(w == 0, 1)) {
    v4u r16[16], f8[8];
    const v4u xv = *(const GAS v4u*)((const GAS unsigned char*)x + (unsigned)l16);
    const unsigned epv = __builtin_amdgcn_raw_buffer_load_b32(rs, 0, 0, 0);
    { const rsrc_t r = mk(wq, 512 * 4096 * 2), r1 = mk(w1, 512 * FF * 2);
#pragma unroll
      for (int i = 0; i < 16; ++i) r16[i] = ldw(r, vo, so + i * 8192);
#pragma unroll
      for (int i = 0; i < 8; ++i) f8[i] = ldw(r1, vo1, so1 + i * (FF * 2)); }
    SB();
    __builtin_amdgcn_s_barrier();
    SB();
    unsigned sel[8]; mksel(sel, l);
    int gvo = (l & 8) ? OOB : (l & 7) * 32 + (l & 16) + (l >> 5) * 8, xra = L_XN + kq * 256 + (l >> 3) * 32, xrc = L_XN + g * 128 + (l >> 3) * 16;
    asm volatile("" : "+v"(sel[0]), "+v"(sel[1]), "+v"(sel[2]), "+v"(sel[3]), "+v"(sel[4]), "+v"(sel[5]), "+v"(sel[6]), "+v"(sel[7]), "+v"(gvo), "+v"(xra), "+v"(xrc));
    SB();
    float xf[8];
#pragma unroll
    for (int i = 0; i < 4; ++i) { xf[2 * i] = bflo(xv[i]); xf[2 * i + 1] = bfhi(xv[i]); }
    const float mean = sum64(((xf[0] + xf[1]) + (xf[2] + xf[3])) + ((xf[4] + xf[5]) + (xf[6] + xf[7]))) * (1.f / 512);
    float vs = 0.f;
#pragma unroll
    for (int i = 0; i < 8; ++i) { const float d = xf[i] - mean; vs = fmaf(d, d, vs); }
    const float var = sum64(vs) * (1.f / 512);
    const float mb = rb(mean), rsd = rcp(rb(__builtin_amdgcn_sqrtf(rb(rb(var) + 1e-5f))));
    v4u xn;
#pragma unroll
    for (int i = 0; i < 4; ++i) { const unsigned d = pk(xf[2 * i] - mb, xf[2 * i + 1] - mb); xn[i] = pk(bflo(d) * rsd, bfhi(d) * rsd); }
    *(LAS v4u*)(L + L_XN + l * 16) = xn;
    *(LAS float*)(L + L_M + (l & 3) * 4) = -__builtin_inff();
    const v4u xa = *(LAS v4u*)(L + xra), xb = *(LAS v4u*)(L + xra + 16), xc = *(LAS v4u*)(L + xrc);
    ep0 = __builtin_amdgcn_readfirstlane(epv);
    EPOCH();
    const unsigned tb0 = (tag >> ((l >> 5) * 16)) & 0xFFu, tb1 = (tag >> ((l >> 5) * 16 + 8)) & 0xFFu;
    { v4u bx[8];
#pragma unroll
      for (int i = 0; i < 8; ++i) bx[i] = diag(xa[i >> 1], sel, i & 1);
      SB();
      v4f d = {0.f, 0.f, 0.f, 0.f};
#pragma unroll
      for (int i = 0; i < 8; ++i) { d = mfma(bx[i], r16[i], d); bx[i] = diag(xb[i >> 1], sel, i & 1); SB(); }
#pragma unroll
      for (int i = 0; i < 8; ++i) d = mfma(bx[i], r16[8 + i], d);
      ST8(diag_out(d, tb0, tb1), gvo, WS_QG + g * 8192 + kq * 2048 + rb_ * 256);
      SB();
      __builtin_amdgcn_s_barrier();
      SB();
    }
    { v4f d = {0.f, 0.f, 0.f, 0.f};
#pragma unroll
      for (int i = 0; i < 8; ++i) d = mfma(diag(xc[i >> 1], sel, i & 1), f8[i], d);
      ST8(diag_out(d, tb0, tb1), hasw1 ? gvo : OOB, WS_HG + g * (FF * 4) + jw * 256); }
    const int hr0 = gp * 224 + rb_ * 28, hu = min(l & 7, 6), ocol = (b & 63) * 8 + (l & 3) * 2 + (l >> 5);
    int mlo = (l & 51) == 0 ? (n >> 2) * 512 : OOB, mls = WS_ML + g * 2048 + j * 16, hvo = (l >> 3) * (FF * 4) + hu * 16, hso = WS_HG + hr0 * 4;
    int pvo = (l & 31) * 16, pso = WS_ML + gp * 2048 + (j >> 3) * 512;
    asm volatile("" : "+v"(mlo), "+v"(hvo), "+v"(pvo));
    asm volatile("" : "+s"(mls), "+s"(hso), "+s"(pso));
    const float b1v = bflo(b1[hr0 + hu * 4 + gq]), b2v = bflo(b2[ocol]), xo = bflo(x[ocol]);
    BAR();
    BAR();
    v4u hg = LD16(hvo, hso);
    SB();
    BAR();
    { const v2u mm = *(LAS v2u*)(L + L_MX + l * 8);
      const v4f s0 = *(LAS v4f*)(L + L_S + l * 16), s1 = *(LAS v4f*)(L + L_S + 1024 + l * 16);
      const float m = *(LAS float*)(L + L_M + (n >> 2) * 4);
      float ls = fmaf((s0[0] + s0[1]) + (s0[2] + s0[3]), ex2(uf(mm[0]) - m), ((s1[0] + s1[1]) + (s1[2] + s1[3])) * ex2(uf(mm[1]) - m));
      ls += dpp<X1>(ls); ls += dpp<X2>(ls); ls = fold16(ls, ls); ls = fold32(ls, ls);
      ST16((v4u{__float_as_uint(m), tag, __float_as_uint(ls), tag}), mlo, mls);
    }
    BAR();
    {
      if (__builtin_expect(ANY(DBAD(hg)), 0)) UNTIL(DBAD(hg), hg = LD16(hvo, hso));
      float hs = fold16(fold32(gval(hg[0]), gval(hg[2])), fold32(gval(hg[1]), gval(hg[3]))); hs += dpp<R8>(hs);
      const float hp = rb(rb(hs) + b1v);
      const float u2 = 1.5957691216057308f * fmaf(0.044715f * hp * hp, hp, hp);
      const float ge = hp * rcp(1.f + ex2(-u2 * LOG2E));
      *(LAS u16*)(L + L_H + (l & 7) * 8 + gq * 2) = ((l & 7) < 7) ? (u16)pk(ge, ge) : (u16)0;
    }
    __builtin_amdgcn_s_sleep(8);
    v4u pv = LD16(pvo, pso);
    SB();
    BAR();
    {
      UNTIL(TBAD(pv), pv = LD16(pvo, pso));
      const float m = uf(pv[0]);
      const float M = max16(mx_r4(mx_r8(mx_x2(mx_x1(m)))));
      const float e = ex2(m - M);
      float ls = e * uf(pv[2]);
      ls += dpp<X1>(ls); ls += dpp<X2>(ls); ls += dpp<HM>(ls); ls += dpp<R8>(ls); ls = fold16(ls, ls);
      *(LAS float*)(L + L_WG + (l & 31) * 4) = e * rcp(ls);
    }
    BAR();
    if (b < 64) {
      const int fo = WS_FB + b * 4096;
      v4u f0 = LD16(l16, fo), f1 = LD16(l16 + 1024, fo), f2 = LD16(l16 + 2048, fo), f3 = LD16(l16 + 3072, fo);
      UNTIL(TBAD(f0) | TBAD(f1) | TBAD(f2) | TBAD(f3), (f0 = LD16(l16, fo), f1 = LD16(l16 + 1024, fo), f2 = LD16(l16 + 2048, fo), f3 = LD16(l16 + 3072, fo)));
      float t = fold32((uf(f0[0]) + uf(f1[0])) + (uf(f2[0]) + uf(f3[0])), (uf(f0[2]) + uf(f1[2])) + (uf(f2[2]) + uf(f3[2])));
      t = fold16(t, t); t += dpp<R8>(t); t += dpp<R4>(t);
      const float f = rb(rb(t) + b2v);
      float gt = rb(f * rcp(1.f + ex2(-f * LOG2E)));
      int frd = L_V + ((l >> 5) * 576 + (l & 3) * 136 + ((l >> 2) & 7) * 16) * 4;
      int ovo = (l & 28) ? OOB : ocol * 2; float xo_ = xo;
      asm volatile("" : "+v"(gt), "+v"(frd), "+v"(ovo), "+v"(xo_));
      BAR();
      const v4f u0 = *(LAS v4f*)(L + frd), u1 = *(LAS v4f*)(L + frd + 16);
      const v4f u = (u0 + u1) + (*(LAS v4f*)(L + frd + 32) + *(LAS v4f*)(L + frd + 48));
      float v = (u[0] + u[2]) + (u[1] + u[3]);
      v = fold16(v, v); v += dpp<R8>(v); v += dpp<R4>(v);
      const float o = xo_ + rb(rb(v) * gt);
      __builtin_amdgcn_raw_buffer_store_b16((u16)pk(o, o), mk(out, 1024), ovo, 0, 16);
      if (b == 0 && l == 0) __hip_atomic_store((GAS unsigned*)ws, tag, __ATOMIC_RELAXED, __HIP_MEMORY_SCOPE_AGENT);
    }
  } else {
    const int a = w - 1, a3 = a & 3;
    int obo = (n & 3) == 0 ? (n >> 2) * 16384 + gq * 16 : OOB;
    int sbo = (l & 16) ? OOB : (l >> 5) * 16384 + (n >> 3) * 8192 + (n & 7) * 8;
    int obs = WS_OB + g * 65536 + a3 * 4096 + j * 128, sbs = WS_SB + ((j & 1) * 32 + a * 4) * 8192 + (g * 16 + (j >> 1)) * 64;
    int fbs = WS_FB + (kq * 16 + a * 2) * 4096 + (g * 8 + rb_) * 64;
    int sadr = L_S + (a & 1) * 1024 + gq * 256 + (n & 3) * 64 + (a >> 1) * 16 + (n >> 2) * 4, qadr = L_Q + (n & 3) * QSTR + gq * 64 + (n >> 2) * 16;
    int pso = WS_OB + gp * 65536 + (j >> 1) * 4096 + a3 * 1024, pvo = a >= 4 ? l * 16 : OOB;
    int oadr = L_O + n * 80 + gq * 16, owr = L_O + (a3 * 8 + (l >> 3)) * 80 + (l & 7) * 8;
    int mad = L_M + (n & 3) * 4, l8 = l * 8, mrd = L_M + (n >> 2) * 4;
    asm volatile("" : "+v"(mad), "+v"(l8), "+v"(mrd));
    int fso = WS_SB + b * 8192 + a * 1024, fadr = L_V + ((l & 3) * 136 + a * 16 + (l >> 2)) * 4;
    int vwr = L_V + (a * 16 + gq) * VSTR + n * 16;
    int qso = WS_QG + g * 8192 + a3 * 512, qvo = a >= 4 ? (l >> 5) * 4096 + (l & 31) * 16 : OOB;
    int mxw = L_MX + gq * 128 + (n & 3) * 32 + a * 4, qwr = L_Q + a3 * QSTR + (l & 31) * 8 + (l >> 5) * 4;
    int wov = (l >> 5) * 1024 + ((l & 31) ^ ((l >> 5) * 2)) * 16;
    asm volatile("" : "+v"(wov));
    int wos = (gp * 512 + (j >> 1) * 32 + a3 * 8) * 1024 + (j & 1) * 512, w2s = (gp * 224 + rb_ * 28 + a3 * 8) * 1024 + kq * 256, wvo = gq * 1024 + (n ^ (gq * 2)) * 16;
    int w2t = w2s + (a3 == 3 ? 0 : 4096);
    int wl0 = L_W + a3 * 4 * WBLK, wl1 = L_W + (16 + a3 * 2) * WBLK;
    v4u so_ = {(unsigned)(u64)wo, (unsigned)((u64)wo >> 32) & 0xFFFFu, 4096u * 512 * 2, 0x20000u}, s2_ = {(unsigned)(u64)w2, (unsigned)((u64)w2 >> 32) & 0xFFFFu, FF * 512u * 2, 0x20000u};
    asm volatile("" : "+v"(obo), "+v"(sbo), "+v"(sadr), "+v"(qadr), "+v"(oadr), "+v"(owr), "+v"(vwr), "+v"(fadr), "+v"(qvo), "+v"(mxw), "+v"(qwr), "+v"(pvo), "+v"(wvo));
    asm volatile("" : "+s"(obs), "+s"(sbs), "+s"(pso), "+s"(qso), "+s"(fso), "+s"(wos), "+s"(w2s), "+s"(so_), "+s"(s2_), "+s"(wl0), "+s"(wl1), "+s"(w2t));
    const rsrc_t rv = mk(vc, 8u * SK * 256);
    int tadr = L_V + (gq * 4 + (n >> 2)) * VSTR + a3 * 64 + (n & 3) * 8;
    asm volatile("" : "+v"(tadr));
    EPOCH();
    v4u o0 = {0u, tag, 0u, tag}, o1 = o0; u64 lead = 0x000F000F000F000Full;
    asm volatile("" : "+v"(o0), "+v"(o1)); asm volatile("" : "+s"(lead));
    SB();
#define WDO() do { unsigned keep; asm volatile("s_mov_b32 %0, m0\n\ts_mov_b32 m0, %4\n\ts_nop 0\n\t" \
      "buffer_load_dwordx4 %2, %3, %1 offen nt lds\n\ts_add_u32 m0, m0, %5\n\ts_add_u32 %1, %1, 0x800\n\tbuffer_load_dwordx4 %2, %3, %1 offen nt lds\n\ts_add_u32 m0, m0, %5\n\ts_add_u32 %1, %1, 0x800\n\t" \
      "buffer_load_dwordx4 %2, %3, %1 offen nt lds\n\ts_add_u32 m0, m0, %5\n\ts_add_u32 %1, %1, 0x800\n\tbuffer_load_dwordx4 %2, %3, %1 offen nt lds\n\ts_mov_b32 m0, %0" \
      : "=&s"(keep), "+s"(wos) : "v"(wov), "s"(so_), "s"(wl0), "i"(WBLK) : "memory", "scc"); } while (0)
#define WD2() do { unsigned keep; asm volatile("s_mov_b32 %0, m0\n\ts_mov_b32 m0, %4\n\ts_nop 0\n\t" \
      "buffer_load_dwordx4 %2, %3, %1 offen nt lds\n\ts_add_u32 m0, m0, %5\n\ts_nop 0\n\tbuffer_load_dwordx4 %2, %3, %6 offen nt lds\n\ts_mov_b32 m0, %0" \
      : "=&s"(keep) : "s"(w2s), "v"(wvo), "s"(s2_), "s"(wl1), "i"(WBLK), "s"(w2t) : "memory", "scc"); } while (0)
    __builtin_amdgcn_s_barrier();
    SB();
    if (a >= 4) { const rsrc_t rk = mk(kc, 8u * SK * 256);
#pragma unroll
      for (int k = 0; k < 4; ++k) kv[k] = ldw(rk, kof + k * 256, kvo);
    } else __builtin_amdgcn_s_sleep(2);
    SB();
    WAITV(3);
    SB();
    if (a >= 4) { WAITV(2); __builtin_amdgcn_s_barrier(); }
    SB();
    v4u q0 = LD16(qvo, qso), q1 = LD16(qvo, qso + 2048);
    SB();
    if (a >= 4) __builtin_amdgcn_s_sleep(1);
    v4u vv[4];
#pragma unroll
    for (int i = 0; i < 4; ++i) vv[i] = ldw(rv, l16 + i * 1024, kvo);
    SB();
    if (a < 4) __builtin_amdgcn_s_barrier();
    SB();
    WAITV(4);
#define QSUM() do { const v4f u_ = __builtin_bit_cast(v4f, q0 & 0xFFFFFF00u) + __builtin_bit_cast(v4f, q1 & 0xFFFFFF00u); float c_, d_; const float a_ = sw32(u_[0], u_[2], c_), b_ = sw32(u_[1], u_[3], d_); \
        *(LAS unsigned*)(L + qwr) = pk(a_ + c_, b_ + d_); } while (0)
    if (a >= 4) {
      QSUM();
      SB();
      if (__builtin_expect(ANY(DBAD(q0) | DBAD(q1)), 0)) {
        UNTIL(DBAD(q0) | DBAD(q1), (q0 = LD16(qvo, qso), q1 = LD16(qvo, qso + 2048)));
        QSUM();
      }
    }
    SB();
    if (a < 4) {
#pragma unroll
      for (int i = 0; i < 4; ++i) *(LAS v4u*)(L + vwr + i * 4 * VSTR) = vv[i];
    }
    BAR();
    {
      const v4u qf = *(LAS v4u*)(L + qadr);
      if (a < 4) WD2();
      v4f sc[4];
#pragma unroll
      for (int i = 0; i < 4; ++i) { sc[i] = mfma(kv[i], qf, v4f{0.f, 0.f, 0.f, 0.f}); SB(); }
      float t, m;
#define DADD(src, acc, rot, bank) "v_add_f32_dpp %0, " src ", " acc " row_ror:" rot " row_mask:0xf bank_mask:" bank "\n\t"
      asm volatile("s_nop 4\n\t"
                   DADD("%3", "%2", "12", "0x1") DADD("%6", "%7", "4", "0x2") DADD("%10", "%12", "8", "0x4") DADD("%14", "%17", "12", "0x8")
                   DADD("%4", "%0", "8", "0x1") DADD("%8", "%0", "12", "0x2") DADD("%11", "%0", "4", "0x4") DADD("%15", "%0", "8", "0x8")
                   DADD("%5", "%0", "4", "0x1") DADD("%9", "%0", "8", "0x2") DADD("%13", "%0", "12", "0x4") DADD("%16", "%0", "4", "0x8")
                   "v_cvt_pk_bf16_f32 %0, 0, %0\n\tv_mul_f32 %0, 0x3db504f3, %0\n\tv_cvt_pk_bf16_f32 %0, 0, %0\n\tv_mul_f32 %0, 0x3fb8aa3b, %0\n\ts_nop 1\n\t"
                   "v_max_f32_dpp %1, %0, %0 row_ror:8 row_mask:0xf bank_mask:0xf\n\ts_nop 1\n\t"
                   "v_max_f32_dpp %1, %1, %1 row_ror:4 row_mask:0xf bank_mask:0xf\n\t"
                   "v_sub_f32 %0, %0, %1\n\tv_exp_f32 %0, %0\n\t"
                   "s_mov_b64 exec, %21\n\tds_max_f32 %18, %1\n\tds_write_b32 %19, %1\n\ts_mov_b64 exec, -1\n\tds_write_b32 %20, %0"
                   : "=&v"(t), "=&v"(m)
                   : "v"(sc[0][0]), "v"(sc[0][1]), "v"(sc[0][2]), "v"(sc[0][3]), "v"(sc[1][0]), "v"(sc[1][1]), "v"(sc[1][2]), "v"(sc[1][3]),
                     "v"(sc[2][0]), "v"(sc[2][1]), "v"(sc[2][2]), "v"(sc[2][3]), "v"(sc[3][0]), "v"(sc[3][1]), "v"(sc[3][2]), "v"(sc[3][3]),
                     "v"(mad), "v"(mxw), "v"(sadr), "s"(lead)
                   : "memory");
#pragma unroll
      for (int i = 0; i < 5; ++i) __builtin_amdgcn_s_setprio(2);
    }
    v2u vt[16];
    if (a < 4) {
#pragma unroll
      for (int i = 0; i < 8; ++i) vt[(i & 3) + (i >> 2) * 8] = trd(L + tadr + (i & 3) * 16 * VSTR + (i >> 2) * 32);
    }
    BAR();
    if (a < 4) {
      const v2u mm = *(LAS v2u*)(L + L_MX + l8);
      const float m = *(LAS float*)(L + mrd);
      const v4f s0 = *(LAS v4f*)(L + L_S + l16), s1 = *(LAS v4f*)(L + L_S + 1024 + l16);
      const v4f p0 = s0 * ex2(uf(mm[0]) - m), p1 = s1 * ex2(uf(mm[1]) - m);
      const v4u pb = {pk(p0[0], p0[1]), pk(p0[2], p0[3]), pk(p1[0], p1[1]), pk(p1[2], p1[3])};
      const v4f z = {0.f, 0.f, 0.f, 0.f};
      v4f d0 = mfma(cat(vt[0], vt[1]), pb, z), d1 = mfma(cat(vt[8], vt[9]), pb, z);
      const v4u pb1 = dpp4<0x55>(pb), pb2 = dpp4<0xAA>(pb);
      SB();
      d0 = mfma(cat(vt[2], vt[3]), pb1, d0); d1 = mfma(cat(vt[10], vt[11]), pb1, d1);
      v4u pb3 = dpp4<0xFF>(pb);
      asm volatile("" : "+v"(pb3) : "v"(pb1), "v"(vt[0]), "v"(vt[1]), "v"(vt[8]), "v"(vt[9]));
      SB();
      WDO();
      BAR();
#pragma unroll
      for (int i = 0; i < 8; ++i) vt[4 + (i & 1) + ((i >> 2) & 1) * 2 + ((i >> 1) & 1) * 8] = trd(L + tadr + (4 + (i & 1) + ((i >> 2) & 1) * 2) * 16 * VSTR + ((i >> 1) & 1) * 32);
      d0 = mfma(cat(vt[4], vt[5]), pb2, d0); d1 = mfma(cat(vt[12], vt[13]), pb2, d1);
      d0 = mfma(cat(vt[6], vt[7]), pb3, d0); d1 = mfma(cat(vt[14], vt[15]), pb3, d1);
      o0[0] = pk(d0[0], d0[1]); o0[2] = pk(d0[2], d0[3]); o1[0] = pk(d1[0], d1[1]); o1[2] = pk(d1[2], d1[3]);
      ST16(o0, obo, obs);
      ST16(o1, obo + 64, obs);
      asm volatile("s_waitcnt vmcnt(2)" ::: "memory");
    }
    SB();
    if (a >= 4) {
#pragma unroll
      for (int i = 0; i < 4; ++i) *(LAS v4u*)(L + vwr + i * 4 * VSTR) = vv[i];
      BAR();
      __builtin_amdgcn_s_sleep(12);
    }
    SB();
    v4u pv = LD16(pvo, pso);
    BAR();
    if (a >= 4 && ANY(TBAD(pv))) pv = LD16(pvo, pso);
    SB();
    v2u wt[4];
    const int wrd = L_W + gq * 2 * WBLK + (n >> 2) * 256 + (a ^ (n >> 2)) * 32 + (n & 3) * 8;
#pragma unroll
    for (int i = 0; i < 4; ++i) wt[i] = trd(L + L_W + (gq * 4 + (i & 1) * 2 + (n >> 3)) * WBLK + ((n >> 2) & 1) * 512 + ((a * 2 + (i >> 1)) ^ ((n >> 2) & 1)) * 32 + (n & 3) * 8);
    SB();
    WAITV(0);
    if (a < 4) {
      const v4u hf = *(LAS v4u*)(L + L_H + gq * 16);
      LAS unsigned char* p = L + wrd + 16 * WBLK;
      const v4f z = {0.f, 0.f, 0.f, 0.f};
      const v4f f0 = mfma(hf, cat(trd(p), trd(p + WBLK)), z), f1 = mfma(hf, cat(trd(p + 128), trd(p + 128 + WBLK)), z);
      ST8((v2u{__float_as_uint(gq ? f1[0] : f0[0]), tag}), l < 32 ? gq * 32768 + (n >> 3) * 4096 + (n & 7) * 8 : OOB, fbs);
    }
    SB();
    if (a >= 4) {
#define OWR() (((LAS unsigned*)(L + owr))[0] = pv[0], ((LAS unsigned*)(L + owr))[1] = pv[2])
      OWR();
      UNTIL(TBAD(pv), (pv = LD16(pvo, pso), OWR()));
    }
    BAR();
    {
      const v4u of0 = *(LAS v4u*)(L + oadr), of1 = *(LAS v4u*)(L + oadr + 16 * 80);
      const v4f w0 = *(LAS v4f*)(L + L_WG + gq * 16), w1_ = *(LAS v4f*)(L + L_WG + 64 + gq * 16);
      SB();
      const v4f z = {0.f, 0.f, 0.f, 0.f};
      const v4f d00 = mfma(of0, cat(wt[0], wt[1]), z), d01 = mfma(of0, cat(wt[2], wt[3]), z), d10 = mfma(of1, cat(wt[0], wt[1]), z), d11 = mfma(of1, cat(wt[2], wt[3]), z);
      SB();
      v2f c0 = d00.hi * w0.hi + d00.lo * w0.lo, c1 = d01.hi * w0.hi + d01.lo * w0.lo;
      c0 = d10.hi * w1_.hi + (d10.lo * w1_.lo + c0); c1 = d11.hi * w1_.hi + (d11.lo * w1_.lo + c1);
      float e = fold32(c0[0] + c0[1], c1[0] + c1[1]);
      e = fold16(e, e);
      ST8((v2u{__float_as_uint(e), tag}), sbo, sbs);
    }
    if (b < 64) {
      __builtin_amdgcn_s_sleep(10); if (a == g) __builtin_amdgcn_s_sleep(10);
      v4u sv = LD16(l16, fso);
#define SWR() (((LAS unsigned*)(L + fadr))[0] = sv[0], ((LAS unsigned*)(L + fadr))[576] = sv[2])
      SWR();
      UNTIL(TBAD(sv), (sv = LD16(l16, fso), SWR()));
      BAR();
      if (a == 0) { const unsigned e_ = (unsigned)__builtin_amdgcn_s_memrealtime(), d_ = 4 * (e_ - c0_); while ((unsigned)__builtin_amdgcn_s_memrealtime() - e_ < d_ && d_ < 100000u) {} }
    }
  }
}

extern "C" void kernel_launch(void* const* d_in, const int* in_sizes, int n_in, void* d_out, int out_size, void* d_ws, size_t ws_size, hipStream_t stream) {
  fused_decode<<<256, NTHR, 0, stream>>>((const u16*)d_in[0], (const u16*)d_in[3], (const u16*)d_in[6], (const u16*)d_in[2], (unsigned char*)d_ws, (const u16*)d_in[1],
                                         (const u16*)d_in[4], (const u16*)d_in[8], (const u16*)d_in[7], (const u16*)d_in[9], (u16*)d_out);
}
```

```cpp
#define LK 2
#define KY 0
#define YT 1
#define PD 20
#define XQ 1
#define XO 1
#define WS 1
#define MLD 8
#include <hip/hip_runtime.h>
#include <hip/hip_bf16.h>

typedef unsigned short u16;
typedef unsigned long long u64;
typedef unsigned v4u __attribute__((ext_vector_type(4)));
typedef unsigned v2u __attribute__((ext_vector_type(2)));
typedef float v4f __attribute__((ext_vector_type(4)));
typedef short s16x4 __attribute__((ext_vector_type(4)));
typedef __bf16 bf16x2 __attribute__((ext_vector_type(2)));
typedef __bf16 bf16x8 __attribute__((ext_vector_type(8)));
typedef __amdgpu_buffer_rsrc_t rsrc_t;
#define GAS __attribute__((address_space(1)))
#define CAS __attribute__((address_space(4)))
#define LAS __attribute__((address_space(3)))

#define TQ 0
#define YV 0
#define OV 4
#define OT 0
#define EV 1
#define TW 0
#define TX TW
#define Q32 0
#define PFQ 1
#define SH 0
#define FORCE -1
#define VD 2
#define WP 1
#define STAMP(i)
#define FLUSH()
constexpr int SK = 32768, WIN = 4096, FF = 1792;
constexpr int NTHR = 576;
constexpr unsigned WS_QG = 4096;
constexpr unsigned WS_HG = WS_QG + 131072;
constexpr unsigned WS_ML = WS_HG + 57344;
constexpr unsigned WS_OB = WS_ML + 16384;
constexpr unsigned WS_SB = WS_OB + 524288;
constexpr unsigned WS_FB = WS_SB + 524288;
constexpr unsigned WS_END = WS_FB + 262144;
constexpr int VSTR = 288, L_V = 0, L_O = 128 * VSTR, L_Q = L_O + 4096, QSTR = 320, L_S = L_Q + 4 * QSTR, L_XN = (L_S + 2048 + 1023) / 1024 * 1024, L_MX = L_XN + 1024, L_M = L_MX + 512, L_H = L_M + 32,
              L_WG = L_H + 64, L_W = L_WG + 128, WBLK = 1088, L_END = L_W + 24 * WBLK;
static_assert(L_O % 128 == 0, "the o image is addressed with ^ 32 and ^ 64");
constexpr unsigned SPIN_CAP = 1u << 18;
constexpr float LOG2E = 1.4426950408889634f;
constexpr int OOB = 0x40000000;

__device__ __forceinline__ rsrc_t mk(const void* p, unsigned bytes) { return __builtin_amdgcn_make_buffer_rsrc((void*)p, 0, bytes, 0x00020000); }
__device__ __forceinline__ v4u ldw(rsrc_t r, int voff, int soff) { return __builtin_amdgcn_raw_buffer_load_b128(r, voff, soff, 2); }
__device__ __forceinline__ float uf(unsigned u) { return __uint_as_float(u); }
__device__ __forceinline__ float bflo(unsigned u) { return __uint_as_float(u << 16); }
__device__ __forceinline__ float bfhi(unsigned u) { return __uint_as_float(u & 0xFFFF0000u); }
__device__ __forceinline__ unsigned pk(float lo, float hi) { bf16x2 r = {(__bf16)lo, (__bf16)hi}; return __builtin_bit_cast(unsigned, r); }
typedef float v2f __attribute__((ext_vector_type(2)));
__device__ __forceinline__ float rb(float f) { return __builtin_bit_cast(float, __builtin_convertvector((v2f{0.f, f}), bf16x2)); }
__device__ __forceinline__ v4f mfma(v4u a, v4u b, v4f c) { return __builtin_amdgcn_mfma_f32_16x16x32_bf16(__builtin_bit_cast(bf16x8, a), __builtin_bit_cast(bf16x8, b), c, 0, 0, 0); }
template <int CTRL> __device__ __forceinline__ unsigned dppu(unsigned x) { return (unsigned)__builtin_amdgcn_mov_dpp((int)x, CTRL, 0xf, 0xf, true); }
template <int CTRL> __device__ __forceinline__ float dpp(float x) { return __uint_as_float(dppu<CTRL>(__float_as_uint(x))); }
template <int CTRL> __device__ __forceinline__ v4u dpp4(v4u x) { return v4u{dppu<CTRL>(x[0]), dppu<CTRL>(x[1]), dppu<CTRL>(x[2]), dppu<CTRL>(x[3])}; }
constexpr int X1 = 0xB1, X2 = 0x4E, HM = 0x141, R4 = 0x124, R8 = 0x128;
__device__ __forceinline__ float sw16(float a, float b, float& o) { auto r = __builtin_amdgcn_permlane16_swap(__float_as_uint(a), __float_as_uint(b), false, false); o = uf(r[1]); return uf(r[0]); }
__device__ __forceinline__ float sw32(float a, float b, float& o) { auto r = __builtin_amdgcn_permlane32_swap(__float_as_uint(a), __float_as_uint(b), false, false); o = uf(r[1]); return uf(r[0]); }
__device__ __forceinline__ float fold16(float a, float b) { float o; const float p = sw16(a, b, o); return p + o; }
__device__ __forceinline__ float fold32(float a, float b) { float o; const float p = sw32(a, b, o); return p + o; }
__device__ __forceinline__ float fmx(float a, float b) { return __builtin_elementwise_maximum(a, b); }
__device__ __forceinline__ float max16(float a) { float o; const float p = sw16(a, a, o); return fmx(p, o); }
#define MAXDPP(name, ctl) __device__ __forceinline__ float name(float m) { float r; asm("s_nop 1\n\tv_max_f32_dpp %0, %1, %1 " ctl " row_mask:0xf bank_mask:0xf" : "=v"(r) : "v"(m)); return r; }
MAXDPP(mx_r8, "row_ror:8") MAXDPP(mx_r4, "row_ror:4") MAXDPP(mx_x1, "quad_perm:[1,0,3,2]") MAXDPP(mx_x2, "quad_perm:[2,3,0,1]")
__device__ __forceinline__ float sum64(float v) { v += dpp<X1>(v); v += dpp<X2>(v); v += dpp<HM>(v); v += dpp<R8>(v); v = fold16(v, v); return fold32(v, v); }
__device__ __forceinline__ v2u trd(LAS unsigned char* p) { return __builtin_bit_cast(v2u, __builtin_amdgcn_ds_read_tr16_b64_v4i16((LAS s16x4*)p)); }
__device__ __forceinline__ v4u cat(v2u a, v2u b) { return v4u{a[0], a[1], b[0], b[1]}; }
__device__ __forceinline__ float ex2(float x) { return __builtin_amdgcn_exp2f(x); }
__device__ __forceinline__ float rcp(float x) { return __builtin_amdgcn_rcpf(x); }
__device__ __forceinline__ float gval(unsigned d) { return __uint_as_float(d & 0xFFFFFF00u); }
__device__ __forceinline__ unsigned d4(float f, unsigned tagbyte) { return ((__float_as_uint(f) + 0x80u) & 0xFFFFFF00u) | tagbyte; }
#define SB() __builtin_amdgcn_sched_barrier(0)
#define BAR() do { asm volatile("s_waitcnt lgkmcnt(0)" ::: "memory"); __builtin_amdgcn_s_barrier(); asm volatile("" ::: "memory"); } while (0)
#define FENCE() asm volatile("" ::: "memory")
#define WAITV(n) __builtin_amdgcn_s_waitcnt(0x0F70 | (n))
#define LD16(off, soff) __builtin_amdgcn_raw_buffer_load_b128(rs, (off), (soff), 16)
#define ST16(v, off, soff) __builtin_amdgcn_raw_buffer_store_b128((v), rs, (off), (soff), 16)
#define ST8(v, off, soff) __builtin_amdgcn_raw_buffer_store_b64((v), rs, (off), (soff), 16)
#define TBAD(v) __builtin_amdgcn_bitop3_b32((v)[1], (v)[3], tag, 0x7E)
#define ANY(t) (__builtin_amdgcn_ballot_w64((t) != 0) != 0)
#define DBAD(v) __builtin_amdgcn_bitop3_b32(__builtin_amdgcn_perm((v)[1], (v)[0], 0x0c0c0400u), __builtin_amdgcn_perm((v)[3], (v)[2], 0x04000c0cu), tag, 0x56)
#define EPOCH() asm volatile("" : "+s"(ep0)); const unsigned tag = ((ep0 & 0x7F7F7F7Fu) + 0x01010101u) ^ (ep0 & 0x80808080u)
#define UNTIL(BADEXPR, RELOAD) do { if (ANY(BADEXPR)) { int spins_ = SPIN_CAP; _Pragma("nounroll") do { FENCE(); RELOAD; if (!ANY(BADEXPR)) break; asm volatile("" : "+s"(spins_)); } while (--spins_); } } while (0)

__device__ __forceinline__ void mksel(unsigned (&sel)[8], int l) {
#pragma unroll
  for (int d = 0; d < 4; ++d) { const bool on = ((l & 7) >> 1) == d;
    sel[d] = on ? ((l & 1) ? 0x01000c0cu : 0x0c0c0100u) : 0x0c0c0c0cu; sel[4 + d] = on ? ((l & 1) ? 0x03020c0cu : 0x0c0c0302u) : 0x0c0c0c0cu; }
}
__device__ __forceinline__ v4u diag(unsigned pair, const unsigned (&sel)[8], int hi) {
  return v4u{__builtin_amdgcn_perm(0u, pair, sel[4 * hi]), __builtin_amdgcn_perm(0u, pair, sel[4 * hi + 1]), __builtin_amdgcn_perm(0u, pair, sel[4 * hi + 2]), __builtin_amdgcn_perm(0u, pair, sel[4 * hi + 3])};
}
__device__ __forceinline__ float upr8(float v) { return __builtin_bit_cast(float, __builtin_amdgcn_update_dpp(__builtin_bit_cast(int, v), __builtin_bit_cast(int, v), R8, 0xC, 0xF, false)); }
__device__ __forceinline__ v2u diag_out(v4f d, unsigned tb0, unsigned tb1) {
  const float t0 = upr8(d[0]), t1 = upr8(d[1]), t2 = upr8(d[2]), t3 = upr8(d[3]);
  return v2u{d4(fold32(t0, t2), tb0), d4(fold32(t1, t3), tb1)};
}

__global__ void __launch_bounds__(NTHR, 3) fused_decode(const u16* __restrict__ x, const u16* __restrict__ wq, const u16* __restrict__ w1, const u16* __restrict__ vc, unsigned char* ws,
                                                        const u16* __restrict__ kc, const u16* __restrict__ wo, const u16* __restrict__ w2, const u16* __restrict__ b1,
                                                        const u16* __restrict__ b2, u16* __restrict__ out) {
  __shared__ __attribute__((aligned(128))) unsigned char lds_[L_END];
  LAS unsigned char* L = (LAS unsigned char*)lds_;
  const int b = blockIdx.x, g = b & 7, j = b >> 3, l = threadIdx.x & 63;
  int w = __builtin_amdgcn_readfirstlane(threadIdx.x >> 6);
  const unsigned t0 = (TQ | TW) ? (unsigned)__builtin_amdgcn_s_memrealtime() : 0u;
  const unsigned c0_ = (unsigned)__builtin_amdgcn_s_memrealtime();
  { u64 pc; unsigned keep, t_;
    asm volatile("s_cmp_lg_u32 %3, 4\n\ts_cbranch_scc1 1f\n\ts_getpc_b64 %0\n\ts_mov_b32 %1, m0\n\ts_mov_b32 m0, %5\n\tv_lshlrev_b32 %2, 7, %4\n\t"
                 "global_load_lds_dword %2, %0\n\ts_mov_b32 m0, %1\n1:"
                 : "=&s"(pc), "=&s"(keep), "=&v"(t_) : "s"(w), "v"(min((j * 5 + (l & 7) % 5) % 80, SH ? 70 : 64)), "i"(L_O) : "scc", "memory"); }
  const int kq = j & 3, rb_ = j >> 2, gn = (g + XQ) & 7, gp = (g - XO) & 7;
  const int gq = l >> 4, n = l & 15;
  const bool hasw1 = rb_ != 3; const int jw = rb_ == 7 ? j - 16 : j;
  int kvo = (g * SK + (SK - WIN) + j * 128 + (((w - 1) * 2 + ((w - 1) >> 2) + j) & 7) * 16) * 256, l16 = l * 16, kof = (n >> 2) * 1024 + gq * 64 + (n & 3) * 16;
  int vo = (l >> 3) * (16 * 8192) + (l & 7) * 16, so = kq * (128 * 8192) + gn * 1024 + rb_ * 128;
  const bool early = !SH || (FORCE >= 0 ? FORCE == 1 : (__builtin_amdgcn_s_getreg(20 | (3 << 11)) & 4) == 0);
  int vo1 = (hasw1 && early) ? (l >> 3) * (8 * FF * 2) + (l & 7) * 16 : OOB, so1 = g * (64 * FF * 2) + jw * 128;
  asm volatile("" : "+s"(kvo), "+s"(so), "+s"(so1));
  asm volatile("s_setprio 3" : "+v"(l16), "+v"(vo), "+v"(vo1), "+v"(kof));
  asm volatile("" :: "s"(x), "s"(kc), "s"(vc), "s"(wq), "s"(wo), "s"(w1), "s"(b1), "s"(w2), "s"(b2), "s"(out), "s"(ws));
  unsigned ep0 = 0; if (!EV) ep0 = *(const CAS unsigned*)ws;
  const rsrc_t rs = mk(ws, WS_END);
  v4u kv[4];
  if (w != 0) {
    const rsrc_t rk = mk(kc, 8u * SK * 256);
    if (PFQ && w == 3) {
      unsigned keep;
      const unsigned po = PFQ == 1 ? (j * 16 + 8 + (l & 7)) * 8192 + gn * 1024 + 384 + ((l >> 3) & 1) * 512
                        : PFQ == 2 ? (j * 16 + (l & 15)) * 8192 + gn * 1024 + 384 + ((l >> 4) & 1) * 512
                        : PFQ == 3 ? (j * 16 + 4 + (l & 15) % 12) * 8192 + gn * 1024 + 384
                        : (j * 16 + 8 + (l & 7)) * 8192 + gn * 1024 + 128 + ((l >> 3) & 3) * 256;
      asm volatile("s_mov_b32 %0, m0\n\ts_mov_b32 m0, %3\n\ts_nop 0\n\tglobal_load_lds_dword %1, %2 nt\n\ts_mov_b32 m0, %0" : "=&s"(keep) : "v"(po), "s"(wq), "i"(L_O + 256) : "memory");
    }
    if (EV) ep0 = *(const CAS unsigned*)ws;
    __builtin_amdgcn_s_setprio(2);
    __builtin_amdgcn_s_barrier();
    SB();
#pragma unroll
    for (int k = 0; k < KY; ++k) kv[k] = ldw(rk, kof + k * 256, kvo);
    SB();
#define RESTK(RK) do { \
    if (KY < 4 && w > 4) __builtin_amdgcn_s_barrier(); \
    SB(); \
    _Pragma("unroll") \
    for (int k = KY; k < 4; ++k) kv[k] = ldw(RK, kof + k * 256, kvo); \
    SB(); \
    if (KY < 4 && w <= 4) __builtin_amdgcn_s_barrier(); \
 } while (0)
    if (!LK) RESTK(rk);
    if (LK == 2 && w <= 4) {
#pragma unroll
      for (int k = KY; k < 4; ++k) kv[k] = ldw(rk, kof + k * 256, kvo); }
  }
  asm volatile("" : "+s"(w));
  if (__builtin_expect(w == 0, 1)) {
    v4u r16[16], f8[8];
    const v4u xv = *(const GAS v4u*)((const GAS unsigned char*)x + (unsigned)l16);
    const unsigned epv = EV ? __builtin_amdgcn_raw_buffer_load_b32(rs, 0, 0, 0) : 0u;
    { const rsrc_t r = mk(wq, 512 * 4096 * 2), r1 = mk(w1, 512 * FF * 2);
#pragma unroll
      for (int i = 0; i < 16; ++i) r16[i] = ldw(r, vo, so + i * 8192);
#pragma unroll
      for (int i = 0; i < 8; ++i) f8[i] = ldw(r1, vo1, so1 + i * (FF * 2)); }
    SB();
    __builtin_amdgcn_s_barrier();
    SB();
    unsigned sel[8]; mksel(sel, l);
    int gvo = (l & 8) ? OOB : (l & 7) * 32 + (l & 16) + (l >> 5) * 8, xra = L_XN + kq * 256 + (l >> 3) * 32, xrc = L_XN + g * 128 + (l >> 3) * 16;
    asm volatile("" : "+v"(sel[0]), "+v"(sel[1]), "+v"(sel[2]), "+v"(sel[3]), "+v"(sel[4]), "+v"(sel[5]), "+v"(sel[6]), "+v"(sel[7]), "+v"(gvo), "+v"(xra), "+v"(xrc));
    SB();
    float xf[8];
#pragma unroll
    for (int i = 0; i < 4; ++i) { xf[2 * i] = bflo(xv[i]); xf[2 * i + 1] = bfhi(xv[i]); }
    const float mean = sum64(((xf[0] + xf[1]) + (xf[2] + xf[3])) + ((xf[4] + xf[5]) + (xf[6] + xf[7]))) * (1.f / 512);
    float vs = 0.f;
#pragma unroll
    for (int i = 0; i < 8; ++i) { const float d = xf[i] - mean; vs = fmaf(d, d, vs); }
    const float var = sum64(vs) * (1.f / 512);
    const float mb = rb(mean), rsd = rcp(rb(__builtin_amdgcn_sqrtf(rb(rb(var) + 1e-5f))));
    v4u xn;
#pragma unroll
    for (int i = 0; i < 4; ++i) { const unsigned d = pk(xf[2 * i] - mb, xf[2 * i + 1] - mb); xn[i] = pk(bflo(d) * rsd, bfhi(d) * rsd); }
    *(LAS v4u*)(L + L_XN + l * 16) = xn;
    *(LAS float*)(L + L_M + (l & 3) * 4) = -__builtin_inff();
    const v4u xa = *(LAS v4u*)(L + xra), xb = *(LAS v4u*)(L + xra + 16), xc = *(LAS v4u*)(L + xrc);
    if (EV) ep0 = __builtin_amdgcn_readfirstlane(epv);
    EPOCH();
    const unsigned tb0 = (tag >> ((l >> 5) * 16)) & 0xFFu, tb1 = (tag >> ((l >> 5) * 16 + 8)) & 0xFFu;
    { v4u bx[8];
#pragma unroll
      for (int i = 0; i < 8; ++i) bx[i] = diag(xa[i >> 1], sel, i & 1);
      SB();
      v4f d = {0.f, 0.f, 0.f, 0.f};
#pragma unroll
      for (int i = 0; i < 8; ++i) { d = mfma(bx[i], r16[i], d); bx[i] = diag(xb[i >> 1], sel, i & 1); SB(); }
#pragma unroll
      for (int i = 0; i < 8; ++i) d = mfma(bx[i], r16[8 + i], d);
      ST8(diag_out(d, tb0, tb1), gvo, WS_QG + gn * 8192 + kq * 2048 + rb_ * 256);
      STAMP(3);
      SB();
      if (SH) { const rsrc_t r1 = mk(w1, 512 * FF * 2);
#pragma unroll
        for (int i = 0; i < 8; ++i) r16[i] = ldw(r1, vo1, so1 + ((g ^ 4) - g) * (64 * FF * 2) + i * (FF * 2)); }
      SB();
      if (KY < 4) __builtin_amdgcn_s_barrier();
      SB();
    }
    { v4f d = {0.f, 0.f, 0.f, 0.f};
#pragma unroll
      for (int i = 0; i < 8; ++i) d = mfma(diag(xc[i >> 1], sel, i & 1), f8[i], d);
      ST8(diag_out(d, tb0, tb1), (hasw1 && early) ? gvo : OOB, WS_HG + g * (FF * 4) + jw * 256); }
    const int hr0 = gp * 224 + rb_ * 28, hu = min(l & 7, 6), ocol = (b & 63) * 8 + (l & 3) * 2 + (l >> 5);
    int mlo = (l & 51) == 0 ? (n >> 2) * 512 : OOB, mls = WS_ML + g * 2048 + j * 16, hvo = (l >> 3) * (FF * 4) + hu * 16, hso = WS_HG + hr0 * 4;
    int pvo = (l & 31) * 16, pso = WS_ML + gp * 2048 + (j >> 3) * 512;
    asm volatile("" : "+v"(mlo), "+v"(hvo), "+v"(pvo));
    asm volatile("" : "+s"(mls), "+s"(hso), "+s"(pso));
    const float b1v = bflo(b1[hr0 + hu * 4 + gq]), b2v = bflo(b2[ocol]), xo = bflo(x[ocol]);
    if (SH) { const v4u xd = *(LAS v4u*)(L + (xrc ^ 512)); v4f d = {0.f, 0.f, 0.f, 0.f};
#pragma unroll
      for (int i = 0; i < 8; ++i) d = mfma(diag(xd[i >> 1], sel, i & 1), r16[i], d);
      ST8(diag_out(d, tb0, tb1), (hasw1 && early) ? gvo : OOB, WS_HG + (g ^ 4) * (FF * 4) + jw * 256); }
    BAR();
    BAR();
    v4u hg; if (!SH) hg = LD16(hvo, hso);
    SB();
    BAR();
    { const v2u mm = *(LAS v2u*)(L + L_MX + l * 8);
      const v4f s0 = *(LAS v4f*)(L + L_S + l * 16), s1 = *(LAS v4f*)(L + L_S + 1024 + l * 16);
      const float m = *(LAS float*)(L + L_M + (n >> 2) * 4);
      float ls = fmaf((s0[0] + s0[1]) + (s0[2] + s0[3]), ex2(uf(mm[0]) - m), ((s1[0] + s1[1]) + (s1[2] + s1[3])) * ex2(uf(mm[1]) - m));
      ls += dpp<X1>(ls); ls += dpp<X2>(ls); ls = fold16(ls, ls); ls = fold32(ls, ls);
      ST16((v4u{__float_as_uint(m), tag, __float_as_uint(ls), tag}), mlo, mls);
    }
    STAMP(6);
    v4u own;
    if (SH) { own = LD16(n * 16, WS_HG + g * (FF * 4) + jw * 256); hg = LD16(hvo, hso); SB(); }
    BAR();
    if (SH && __builtin_expect(hasw1 && !early && ANY(DBAD(own)), 0)) { int tries = 32;
      _Pragma("nounroll") do { FENCE(); own = LD16(n * 16, WS_HG + g * (FF * 4) + jw * 256); } while (ANY(DBAD(own)) && --tries);
      if (!tries) {
      const rsrc_t r1 = mk(w1, 512 * FF * 2); v4f d = {0.f, 0.f, 0.f, 0.f};
#pragma unroll
      for (int i = 0; i < 8; ++i) f8[i] = ldw(r1, (l >> 3) * (8 * FF * 2) + (l & 7) * 16, so1 + i * (FF * 2));
#pragma unroll
      for (int i = 0; i < 8; ++i) d = mfma(diag(xc[i >> 1], sel, i & 1), f8[i], d);
      ST8(diag_out(d, tb0, tb1), gvo, WS_HG + g * (FF * 4) + jw * 256);
    } }
    {
      if (__builtin_expect(ANY(DBAD(hg)), 0)) UNTIL(DBAD(hg), hg = LD16(hvo, hso));
      float hs = fold16(fold32(gval(hg[0]), gval(hg[2])), fold32(gval(hg[1]), gval(hg[3]))); hs += dpp<R8>(hs);
      const float hp = rb(rb(hs) + b1v);
      const float u2 = 1.5957691216057308f * fmaf(0.044715f * hp * hp, hp, hp);
      const float ge = hp * rcp(1.f + ex2(-u2 * LOG2E));
      *(LAS u16*)(L + L_H + (l & 7) * 8 + gq * 2) = ((l & 7) < 7) ? (u16)pk(ge, ge) : (u16)0;
    }
    __builtin_amdgcn_s_sleep(MLD);
    v4u pv = LD16(pvo, pso);
    SB();
    BAR();
    {
      UNTIL(TBAD(pv), pv = LD16(pvo, pso));
      const float m = uf(pv[0]);
      const float M = max16(mx_r4(mx_r8(mx_x2(mx_x1(m)))));
      const float e = ex2(m - M);
      float ls = e * uf(pv[2]);
      ls += dpp<X1>(ls); ls += dpp<X2>(ls); ls += dpp<HM>(ls); ls += dpp<R8>(ls); ls = fold16(ls, ls);
      *(LAS float*)(L + L_WG + (l & 31) * 4) = e * rcp(ls);
    }
    STAMP(8);
    BAR();
    if (b < 64) {
      const int fo = WS_FB + b * 4096;
      v4u f0 = LD16(l16, fo), f1 = LD16(l16 + 1024, fo), f2 = LD16(l16 + 2048, fo), f3 = LD16(l16 + 3072, fo);
      UNTIL(TBAD(f0) | TBAD(f1) | TBAD(f2) | TBAD(f3), (f0 = LD16(l16, fo), f1 = LD16(l16 + 1024, fo), f2 = LD16(l16 + 2048, fo), f3 = LD16(l16 + 3072, fo)));
      float t = fold32((uf(f0[0]) + uf(f1[0])) + (uf(f2[0]) + uf(f3[0])), (uf(f0[2]) + uf(f1[2])) + (uf(f2[2]) + uf(f3[2])));
      t = fold16(t, t); t += dpp<R8>(t); t += dpp<R4>(t);
      const float f = rb(rb(t) + b2v);
      float gt = rb(f * rcp(1.f + ex2(-f * LOG2E)));
      int frd = L_V + ((l >> 5) * 576 + (l & 3) * 136 + ((l >> 2) & 7) * 16) * 4;
      int ovo = (l & 28) ? OOB : ocol * 2; float xo_ = xo;
      asm volatile("" : "+v"(gt), "+v"(frd), "+v"(ovo), "+v"(xo_));
      BAR();
      const v4f u0 = *(LAS v4f*)(L + frd), u1 = *(LAS v4f*)(L + frd + 16);
      const v4f u = (u0 + u1) + (*(LAS v4f*)(L + frd + 32) + *(LAS v4f*)(L + frd + 48));
      float v = (u[0] + u[2]) + (u[1] + u[3]);
      v = fold16(v, v); v += dpp<R8>(v); v += dpp<R4>(v);
      const float o = xo_ + rb(rb(v) * gt);
      __builtin_amdgcn_raw_buffer_store_b16((u16)pk(o, o), mk(out, 1024), ovo, 0, 16);
      if (b == 0 && l == 0) __hip_atomic_store((GAS unsigned*)ws, tag, __ATOMIC_RELAXED, __HIP_MEMORY_SCOPE_AGENT);
    }
    STAMP(10);
    FLUSH();
  } else {
    const int a = w - 1, a3 = a & 3;
    int obo = (n & 3) == 0 ? (n >> 2) * 16384 + gq * 16 : OOB;
    int sbo = (l & 16) ? OOB : (l >> 5) * 16384 + (n >> 3) * 8192 + (n & 7) * 8;
    int obs = WS_OB + g * 65536 + a3 * 4096 + j * 128, sbs = WS_SB + ((j & 1) * 32 + a * 4) * 8192 + (g * 16 + (j >> 1)) * 64;
    int fbs = WS_FB + (kq * 16 + a * 2) * 4096 + (g * 8 + rb_) * 64;
    int sadr = L_S + (a & 1) * 1024 + gq * 256 + (n & 3) * 64 + (a >> 1) * 16 + (n >> 2) * 4, qadr = L_Q + (n & 3) * QSTR + gq * 64 + (n >> 2) * 16;
    int pso = WS_OB + gp * 65536 + (j >> 1) * 4096 + a3 * 1024, pvo = a >= 4 ? l * 16 : OOB;
    int oadr = L_O + n * 80 + gq * 16, owr = L_O + (a3 * 8 + (l >> 3)) * 80 + (l & 7) * 8;
    int mad = L_M + (n & 3) * 4, l8 = l * 8, mrd = L_M + (n >> 2) * 4;
    asm volatile("" : "+v"(mad), "+v"(l8), "+v"(mrd));
    int fso = WS_SB + b * 8192 + a * 1024, fadr = L_V + ((l & 3) * 136 + a * 16 + (l >> 2)) * 4;
    int vwr = L_V + (a * 16 + gq) * VSTR + n * 16;
    int qso = Q32 ? WS_QG + g * 16384 + a3 * 1024 : WS_QG + g * 8192 + a3 * 512, qvo = a >= 4 ? (Q32 ? l * 16 : (l >> 5) * 4096 + (l & 31) * 16) : OOB;
    int mxw = L_MX + gq * 128 + (n & 3) * 32 + a * 4, qwr = L_Q + a3 * QSTR + (Q32 ? l * 4 : (l & 31) * 8 + (l >> 5) * 4);
    int wov = (l >> 5) * 1024 + ((l & 31) ^ ((l >> 5) * 2)) * 16;
    asm volatile("" : "+v"(wov));
    int wos = (gp * 512 + (j >> 1) * 32 + a3 * 8) * 1024 + (j & 1) * 512, w2s = (gp * 224 + rb_ * 28 + a3 * 8) * 1024 + kq * 256, wvo = gq * 1024 + (n ^ (gq * 2)) * 16;
    int w2t = w2s + (a3 == 3 ? 0 : 4096);
    int wl0 = L_W + a3 * 4 * WBLK, wl1 = L_W + (16 + a3 * 2) * WBLK;
    v4u so_ = {(unsigned)(u64)wo, (unsigned)((u64)wo >> 32) & 0xFFFFu, 4096u * 512 * 2, 0x20000u}, s2_ = {(unsigned)(u64)w2, (unsigned)((u64)w2 >> 32) & 0xFFFFu, FF * 512u * 2, 0x20000u};
    asm volatile("" : "+v"(obo), "+v"(sbo), "+v"(sadr), "+v"(qadr), "+v"(oadr), "+v"(owr), "+v"(vwr), "+v"(fadr), "+v"(qvo), "+v"(mxw), "+v"(qwr), "+v"(pvo), "+v"(wvo));
    asm volatile("" : "+s"(obs), "+s"(sbs), "+s"(pso), "+s"(qso), "+s"(fso), "+s"(wos), "+s"(w2s), "+s"(so_), "+s"(s2_), "+s"(wl0), "+s"(wl1), "+s"(w2t));
    const rsrc_t rv = mk(vc, 8u * SK * 256);
    int tadr = L_V + (gq * 4 + (n >> 2)) * VSTR + a3 * 64 + (n & 3) * 8;
    asm volatile("" : "+v"(tadr));
    EPOCH();
    v4u o0 = {0u, tag, 0u, tag}, o1 = o0; u64 lead = 0x000F000F000F000Full;
    asm volatile("" : "+v"(o0), "+v"(o1)); asm volatile("" : "+s"(lead));
    SB();
#define DMA1(VO, SRD, SO) "buffer_load_dwordx4 " VO ", " SRD ", " SO " offen nt lds\n\ts_add_u32 m0, m0, %8\n\ts_add_u32 " SO ", " SO ", 0x800\n\t"
#define WDMA() do { unsigned keep; asm volatile("s_mov_b32 %0, m0\n\ts_mov_b32 m0, %6\n\ts_nop 0\n\t" DMA1("%10", "%4", "%1") DMA1("%10", "%4", "%1") DMA1("%10", "%4", "%1") DMA1("%10", "%4", "%1") \
                   "s_mov_b32 m0, %7\n\ts_nop 0\n\t" DMA1("%3", "%5", "%2") "buffer_load_dwordx4 %3, %5, %9 offen nt lds\n\ts_mov_b32 m0, %0" \
                   : "=&s"(keep), "+s"(wos), "+s"(w2s) : "v"(wvo), "s"(so_), "s"(s2_), "s"(wl0), "s"(wl1), "i"(WBLK), "s"(w2t), "v"(wov) : "memory", "scc"); } while (0)
#define WDO() do { unsigned keep; asm volatile("s_mov_b32 %0, m0\n\ts_mov_b32 m0, %4\n\ts_nop 0\n\t" \
      "buffer_load_dwordx4 %2, %3, %1 offen nt lds\n\ts_add_u32 m0, m0, %5\n\ts_add_u32 %1, %1, 0x800\n\tbuffer_load_dwordx4 %2, %3, %1 offen nt lds\n\ts_add_u32 m0, m0, %5\n\ts_add_u32 %1, %1, 0x800\n\t" \
      "buffer_load_dwordx4 %2, %3, %1 offen nt lds\n\ts_add_u32 m0, m0, %5\n\ts_add_u32 %1, %1, 0x800\n\tbuffer_load_dwordx4 %2, %3, %1 offen nt lds\n\ts_mov_b32 m0, %0" \
      : "=&s"(keep), "+s"(wos) : "v"(wov), "s"(so_), "s"(wl0), "i"(WBLK) : "memory", "scc"); } while (0)
#define WD2() do { unsigned keep; asm volatile("s_mov_b32 %0, m0\n\ts_mov_b32 m0, %4\n\ts_nop 0\n\t" \
      "buffer_load_dwordx4 %2, %3, %1 offen nt lds\n\ts_add_u32 m0, m0, %5\n\ts_nop 0\n\tbuffer_load_dwordx4 %2, %3, %6 offen nt lds\n\ts_mov_b32 m0, %0" \
      : "=&s"(keep) : "s"(w2s), "v"(wvo), "s"(s2_), "s"(wl1), "i"(WBLK), "s"(w2t) : "memory", "scc"); } while (0)
    if (LK == 1) { const rsrc_t rk2 = mk(kc, 8u * SK * 256); RESTK(rk2); }
    if (LK == 2) { const rsrc_t rk2 = mk(kc, 8u * SK * 256); __builtin_amdgcn_s_barrier(); SB();
      if (a >= 4) {
#pragma unroll
        for (int k = KY; k < 4; ++k) kv[k] = ldw(rk2, kof + k * 256, kvo); } else if (VD) __builtin_amdgcn_s_sleep(VD);
      SB(); }
    v4u vv[4];
    WAITV(3 - OT);
    SB();
#pragma unroll
    for (int i = 0; i < YV; ++i) vv[i] = ldw(rv, l16 + i * 1024, kvo);
    SB();
    if (a >= 4) {
      WAITV(3 - YT + YV);
      if (TQ) { while ((unsigned)__builtin_amdgcn_s_memrealtime() - t0 < TQ) __builtin_amdgcn_s_sleep(1); }
      __builtin_amdgcn_s_barrier();
    }
    SB();
    v4u q0, q1, q2, q3;
#define QLD() (q0 = LD16(qvo, qso), q1 = LD16(qvo, qso + 2048))
#define QBAD() (DBAD(q0) | DBAD(q1))
    QLD();
    SB();
    STAMP(9);
    if (a >= 4) __builtin_amdgcn_s_sleep(1);
#pragma unroll
    for (int i = YV; i < OV; ++i) vv[i] = ldw(rv, l16 + i * 1024, kvo);
    SB();
    if (a < 4) { __builtin_amdgcn_s_barrier(); if (OV < 4) __builtin_amdgcn_s_sleep(1); }
    SB();
#pragma unroll
    for (int i = OV; i < 4; ++i) vv[i] = ldw(rv, l16 + i * 1024, kvo);
    SB();
    WAITV(4 - YV);
    STAMP(2);
#define QSUM() do { const v4f u_ = __builtin_bit_cast(v4f, q0 & 0xFFFFFF00u) + __builtin_bit_cast(v4f, q1 & 0xFFFFFF00u); float c_, d_; const float a_ = sw32(u_[0], u_[2], c_), b_ = sw32(u_[1], u_[3], d_); \
        *(LAS unsigned*)(L + qwr) = pk(a_ + c_, b_ + d_); } while (0)
    if (a >= 4) {
      QSUM();
      SB();
      if (__builtin_expect(ANY(QBAD()), 0)) {
        UNTIL(QBAD(), QLD());
        QSUM();
      }
    }
    SB();
    if (a < 4) {
#pragma unroll
      for (int i = 0; i < 4; ++i) *(LAS v4u*)(L + vwr + i * 4 * VSTR) = vv[i];
      if (WP == 0) WDMA();
      if (WS == 5 || WS == 6) WD2();
    }
    const bool wnow = !TW || (unsigned)__builtin_amdgcn_s_memrealtime() - t0 >= TW;
    STAMP(4);
    BAR();
    {
      const v4u qf = *(LAS v4u*)(L + qadr);
      if (WS == 0 && WP == 1 && a < 4 && wnow) WDMA();
      if ((WS == 1 || WS == 3 || WS == 7) && a < 4) WD2();
      if ((WS == 2 || WS == 4) && a < 4) WDO();
      v4f sc[4];
#pragma unroll
      for (int i = 0; i < 4; ++i) { sc[i] = mfma(kv[i], qf, v4f{0.f, 0.f, 0.f, 0.f}); SB(); }
      float t, m;
#define DADD(src, acc, rot, bank) "v_add_f32_dpp %0, " src ", " acc " row_ror:" rot " row_mask:0xf bank_mask:" bank "\n\t"
      asm volatile("s_nop 4\n\t"
                   DADD("%3", "%2", "12", "0x1") DADD("%6", "%7", "4", "0x2") DADD("%10", "%12", "8", "0x4") DADD("%14", "%17", "12", "0x8")
                   DADD("%4", "%0", "8", "0x1") DADD("%8", "%0", "12", "0x2") DADD("%11", "%0", "4", "0x4") DADD("%15", "%0", "8", "0x8")
                   DADD("%5", "%0", "4", "0x1") DADD("%9", "%0", "8", "0x2") DADD("%13", "%0", "12", "0x4") DADD("%16", "%0", "4", "0x8")
                   "v_cvt_pk_bf16_f32 %0, 0, %0\n\tv_mul_f32 %0, 0x3db504f3, %0\n\tv_cvt_pk_bf16_f32 %0, 0, %0\n\tv_mul_f32 %0, 0x3fb8aa3b, %0\n\ts_nop 1\n\t"
                   "v_max_f32_dpp %1, %0, %0 row_ror:8 row_mask:0xf bank_mask:0xf\n\ts_nop 1\n\t"
                   "v_max_f32_dpp %1, %1, %1 row_ror:4 row_mask:0xf bank_mask:0xf\n\t"
                   "v_sub_f32 %0, %0, %1\n\tv_exp_f32 %0, %0\n\t"
                   "s_mov_b64 exec, %21\n\tds_max_f32 %18, %1\n\tds_write_b32 %19, %1\n\ts_mov_b64 exec, -1\n\tds_write_b32 %20, %0"
                   : "=&v"(t), "=&v"(m)
                   : "v"(sc[0][0]), "v"(sc[0][1]), "v"(sc[0][2]), "v"(sc[0][3]), "v"(sc[1][0]), "v"(sc[1][1]), "v"(sc[1][2]), "v"(sc[1][3]),
                     "v"(sc[2][0]), "v"(sc[2][1]), "v"(sc[2][2]), "v"(sc[2][3]), "v"(sc[3][0]), "v"(sc[3][1]), "v"(sc[3][2]), "v"(sc[3][3]),
                     "v"(mad), "v"(mxw), "v"(sadr), "s"(lead)
                   : "memory");
#pragma unroll
      for (int i = 0; i < 5; ++i) __builtin_amdgcn_s_setprio(2);
    }
    v2u vt[16];
    if (a < 4) {
#pragma unroll
      for (int i = 0; i < 8; ++i) vt[(i & 3) + (i >> 2) * 8] = trd(L + tadr + (i & 3) * 16 * VSTR + (i >> 2) * 32);
      if (WP == 2) WDMA();
      if (WS == 6 || WS == 7) WDO();
    }
    STAMP(5);
    BAR();
    if (a < 4) {
      const v2u mm = *(LAS v2u*)(L + L_MX + l8);
      const float m = *(LAS float*)(L + mrd);
      const v4f s0 = *(LAS v4f*)(L + L_S + l16), s1 = *(LAS v4f*)(L + L_S + 1024 + l16);
      const v4f p0 = s0 * ex2(uf(mm[0]) - m), p1 = s1 * ex2(uf(mm[1]) - m);
      const v4u pb = {pk(p0[0], p0[1]), pk(p0[2], p0[3]), pk(p1[0], p1[1]), pk(p1[2], p1[3])};
      const v4f z = {0.f, 0.f, 0.f, 0.f};
      v4f d0 = mfma(cat(vt[0], vt[1]), pb, z), d1 = mfma(cat(vt[8], vt[9]), pb, z);
      const v4u pb1 = dpp4<0x55>(pb), pb2 = dpp4<0xAA>(pb);
      SB();
      d0 = mfma(cat(vt[2], vt[3]), pb1, d0); d1 = mfma(cat(vt[10], vt[11]), pb1, d1);
      v4u pb3 = dpp4<0xFF>(pb);
      asm volatile("" : "+v"(pb3) : "v"(pb1), "v"(vt[0]), "v"(vt[1]), "v"(vt[8]), "v"(vt[9]));
      SB();
      if (WS == 1 || WS == 5) WDO();
      if (WS == 2) WD2();
      STAMP(7);
      BAR();
#pragma unroll
      for (int i = 0; i < 8; ++i) vt[4 + (i & 1) + ((i >> 2) & 1) * 2 + ((i >> 1) & 1) * 8] = trd(L + tadr + (4 + (i & 1) + ((i >> 2) & 1) * 2) * 16 * VSTR + ((i >> 1) & 1) * 32);
      d0 = mfma(cat(vt[4], vt[5]), pb2, d0); d1 = mfma(cat(vt[12], vt[13]), pb2, d1);
      d0 = mfma(cat(vt[6], vt[7]), pb3, d0); d1 = mfma(cat(vt[14], vt[15]), pb3, d1);
      o0[0] = pk(d0[0], d0[1]); o0[2] = pk(d0[2], d0[3]); o1[0] = pk(d1[0], d1[1]); o1[2] = pk(d1[2], d1[3]);
      ST16(o0, obo, obs);
      ST16(o1, obo + 64, obs);
      STAMP(6);
      if (WS == 3) { WDO(); asm volatile("s_waitcnt vmcnt(0)" ::: "memory"); } else if (WS == 4) { WD2(); asm volatile("s_waitcnt vmcnt(0)" ::: "memory"); }
      else if (WP == 3 || !wnow) { if (TW) while ((unsigned)__builtin_amdgcn_s_memrealtime() - t0 < TX) __builtin_amdgcn_s_sleep(1); WDMA(); asm volatile("s_waitcnt vmcnt(0)" ::: "memory"); }
      else asm volatile("s_waitcnt vmcnt(2)" ::: "memory");
      STAMP(1);
    }
    SB();
    if (a >= 4) {
#pragma unroll
      for (int i = 0; i < 4; ++i) *(LAS v4u*)(L + vwr + i * 4 * VSTR) = vv[i];
      STAMP(3);
      BAR();
      __builtin_amdgcn_s_sleep(PD);
    }
    SB();
    v4u pv = LD16(pvo, pso);
    BAR();
    if (a >= 4 && ANY(TBAD(pv))) pv = LD16(pvo, pso);
    SB();
    v2u wt[4];
    const int wrd = L_W + gq * 2 * WBLK + (n >> 2) * 256 + (a ^ (n >> 2)) * 32 + (n & 3) * 8;
#pragma unroll
    for (int i = 0; i < 4; ++i) wt[i] = trd(L + L_W + (gq * 4 + (i & 1) * 2 + (n >> 3)) * WBLK + ((n >> 2) & 1) * 512 + ((a * 2 + (i >> 1)) ^ ((n >> 2) & 1)) * 32 + (n & 3) * 8);
    SB();
    WAITV(0);
    if (a < 4) {
      const v4u hf = *(LAS v4u*)(L + L_H + gq * 16);
      LAS unsigned char* p = L + wrd + 16 * WBLK;
      const v4f z = {0.f, 0.f, 0.f, 0.f};
      const v4f f0 = mfma(hf, cat(trd(p), trd(p + WBLK)), z), f1 = mfma(hf, cat(trd(p + 128), trd(p + 128 + WBLK)), z);
      ST8((v2u{__float_as_uint(gq ? f1[0] : f0[0]), tag}), l < 32 ? gq * 32768 + (n >> 3) * 4096 + (n & 7) * 8 : OOB, fbs);
    }
    SB();
    if (a >= 4) {
#define OWR() (((LAS unsigned*)(L + owr))[0] = pv[0], ((LAS unsigned*)(L + owr))[1] = pv[2])
      OWR();
      UNTIL(TBAD(pv), (pv = LD16(pvo, pso), OWR()));
      STAMP(8);
    }
    BAR();
    {
      const v4u of0 = *(LAS v4u*)(L + oadr), of1 = *(LAS v4u*)(L + oadr + 16 * 80);
      const v4f w0 = *(LAS v4f*)(L + L_WG + gq * 16), w1_ = *(LAS v4f*)(L + L_WG + 64 + gq * 16);
      SB();
      const v4f z = {0.f, 0.f, 0.f, 0.f};
      const v4f d00 = mfma(of0, cat(wt[0], wt[1]), z), d01 = mfma(of0, cat(wt[2], wt[3]), z), d10 = mfma(of1, cat(wt[0], wt[1]), z), d11 = mfma(of1, cat(wt[2], wt[3]), z);
      SB();
      v2f c0 = d00.hi * w0.hi + d00.lo * w0.lo, c1 = d01.hi * w0.hi + d01.lo * w0.lo;
      c0 = d10.hi * w1_.hi + (d10.lo * w1_.lo + c0); c1 = d11.hi * w1_.hi + (d11.lo * w1_.lo + c1);
      float e = fold32(c0[0] + c0[1], c1[0] + c1[1]);
      e = fold16(e, e);
      ST8((v2u{__float_as_uint(e), tag}), sbo, sbs);
    }
    STAMP(10);
    if (b < 64) {
      __builtin_amdgcn_s_sleep(10); if (a == g) __builtin_amdgcn_s_sleep(10);
      v4u sv = LD16(l16, fso);
#define SWR() (((LAS unsigned*)(L + fadr))[0] = sv[0], ((LAS unsigned*)(L + fadr))[576] = sv[2])
      SWR();
      UNTIL(TBAD(sv), (sv = LD16(l16, fso), SWR()));
      BAR();
      if (a == 0) { const unsigned e_ = (unsigned)__builtin_amdgcn_s_memrealtime(), d_ = 4 * (e_ - c0_); while ((unsigned)__builtin_amdgcn_s_memrealtime() - e_ < d_ && d_ < 100000u) {} }
    }
  }
}

extern "C" void kernel_launch(void* const* d_in, const int* in_sizes, int n_in, void* d_out, int out_size, void* d_ws, size_t ws_size, hipStream_t stream) {
  fused_decode<<<256, NTHR, 0, stream>>>((const u16*)d_in[0], (const u16*)d_in[3], (const u16*)d_in[6], (const u16*)d_in[2], (unsigned char*)d_ws, (const u16*)d_in[1],
                                         (const u16*)d_in[4], (const u16*)d_in[8], (const u16*)d_in[7], (const u16*)d_in[9], (u16*)d_out);
}
```

```cpp
#define LK 1
#define KY 2
#define YT 2
#define PD 12
#define XQ 1
#define XO 1
#define WS 1
#define MLD 8
#include <hip/hip_runtime.h>
#include <hip/hip_bf16.h>

typedef unsigned short u16;
typedef unsigned long long u64;
typedef unsigned v4u __attribute__((ext_vector_type(4)));
typedef unsigned v2u __attribute__((ext_vector_type(2)));
typedef float v4f __attribute__((ext_vector_type(4)));
typedef short s16x4 __attribute__((ext_vector_type(4)));
typedef __bf16 bf16x2 __attribute__((ext_vector_type(2)));
typedef __bf16 bf16x8 __attribute__((ext_vector_type(8)));
typedef __amdgpu_buffer_rsrc_t rsrc_t;
#define GAS __attribute__((address_space(1)))
#define CAS __attribute__((address_space(4)))
#define LAS __attribute__((address_space(3)))

#define TQ 0
#define YV 0
#define OV 4
#define OT 0
#define EV 1
#define TW 0
#define TX TW
#define Q32 0
#define PFQ 1
#define SH 0
#define FORCE -1
#define VD 2
#define WP 1
#define STAMP(i) do { if ((i) == 3) { SB(); td_ = (unsigned)__builtin_amdgcn_s_memrealtime(); SB(); } } while (0)
#define FLUSH() do { if (w == 0 && td_) { const unsigned d_ = 10 * (td_ - c0_); while ((unsigned)__builtin_amdgcn_s_memrealtime() - c0_ < d_ && d_ < 100000u) __builtin_amdgcn_s_sleep(4); } } while (0)
constexpr int SK = 32768, WIN = 4096, FF = 1792;
constexpr int NTHR = 576;
constexpr unsigned WS_QG = 4096;
constexpr unsigned WS_HG = WS_QG + 131072;
constexpr unsigned WS_ML = WS_HG + 57344;
constexpr unsigned WS_OB = WS_ML + 16384;
constexpr unsigned WS_SB = WS_OB + 524288;
constexpr unsigned WS_FB = WS_SB + 524288;
constexpr unsigned WS_END = WS_FB + 262144;
constexpr int VSTR = 288, L_V = 0, L_O = 128 * VSTR, L_Q = L_O + 4096, QSTR = 320, L_S = L_Q + 4 * QSTR, L_XN = (L_S + 2048 + 1023) / 1024 * 1024, L_MX = L_XN + 1024, L_M = L_MX + 512, L_H = L_M + 32,
              L_WG = L_H + 64, L_W = L_WG + 128, WBLK = 1088, L_END = L_W + 24 * WBLK;
static_assert(L_O % 128 == 0, "the o image is addressed with ^ 32 and ^ 64");
constexpr unsigned SPIN_CAP = 1u << 18;
constexpr float LOG2E = 1.4426950408889634f;
constexpr int OOB = 0x40000000;

__device__ __forceinline__ rsrc_t mk(const void* p, unsigned bytes) { return __builtin_amdgcn_make_buffer_rsrc((void*)p, 0, bytes, 0x00020000); }
__device__ __forceinline__ v4u ldw(rsrc_t r, int voff, int soff) { return __builtin_amdgcn_raw_buffer_load_b128(r, voff, soff, 2); }
__device__ __forceinline__ float uf(unsigned u) { return __uint_as_float(u); }
__device__ __forceinline__ float bflo(unsigned u) { return __uint_as_float(u << 16); }
__device__ __forceinline__ float bfhi(unsigned u) { return __uint_as_float(u & 0xFFFF0000u); }
__device__ __forceinline__ unsigned pk(float lo, float hi) { bf16x2 r = {(__bf16)lo, (__bf16)hi}; return __builtin_bit_cast(unsigned, r); }
typedef float v2f __attribute__((ext_vector_type(2)));
__device__ __forceinline__ float rb(float f) { return __builtin_bit_cast(float, __builtin_convertvector((v2f{0.f, f}), bf16x2)); }
__device__ __forceinline__ v4f mfma(v4u a, v4u b, v4f c) { return __builtin_amdgcn_mfma_f32_16x16x32_bf16(__builtin_bit_cast(bf16x8, a), __builtin_bit_cast(bf16x8, b), c, 0, 0, 0); }
template <int CTRL> __device__ __forceinline__ unsigned dppu(unsigned x) { return (unsigned)__builtin_amdgcn_mov_dpp((int)x, CTRL, 0xf, 0xf, true); }
template <int CTRL> __device__ __forceinline__ float dpp(float x) { return __uint_as_float(dppu<CTRL>(__float_as_uint(x))); }
template <int CTRL> __device__ __forceinline__ v4u dpp4(v4u x) { return v4u{dppu<CTRL>(x[0]), dppu<CTRL>(x[1]), dppu<CTRL>(x[2]), dppu<CTRL>(x[3])}; }
constexpr int X1 = 0xB1, X2 = 0x4E, HM = 0x141, R4 = 0x124, R8 = 0x128;
__device__ __forceinline__ float sw16(float a, float b, float& o) { auto r = __builtin_amdgcn_permlane16_swap(__float_as_uint(a), __float_as_uint(b), false, false); o = uf(r[1]); return uf(r[0]); }
__device__ __forceinline__ float sw32(float a, float b, float& o) { auto r = __builtin_amdgcn_permlane32_swap(__float_as_uint(a), __float_as_uint(b), false, false); o = uf(r[1]); return uf(r[0]); }
__device__ __forceinline__ float fold16(float a, float b) { float o; const float p = sw16(a, b, o); return p + o; }
__device__ __forceinline__ float fold32(float a, float b) { float o; const float p = sw32(a, b, o); return p + o; }
__device__ __forceinline__ float fmx(float a, float b) { return __builtin_elementwise_maximum(a, b); }
__device__ __forceinline__ float max16(float a) { float o; const float p = sw16(a, a, o); return fmx(p, o); }
#define MAXDPP(name, ctl) __device__ __forceinline__ float name(float m) { float r; asm("s_nop 1\n\tv_max_f32_dpp %0, %1, %1 " ctl " row_mask:0xf bank_mask:0xf" : "=v"(r) : "v"(m)); return r; }
MAXDPP(mx_r8, "row_ror:8") MAXDPP(mx_r4, "row_ror:4") MAXDPP(mx_x1, "quad_perm:[1,0,3,2]") MAXDPP(mx_x2, "quad_perm:[2,3,0,1]")
__device__ __forceinline__ float sum64(float v) { v += dpp<X1>(v); v += dpp<X2>(v); v += dpp<HM>(v); v += dpp<R8>(v); v = fold16(v, v); return fold32(v, v); }
__device__ __forceinline__ v2u trd(LAS unsigned char* p) { return __builtin_bit_cast(v2u, __builtin_amdgcn_ds_read_tr16_b64_v4i16((LAS s16x4*)p)); }
__device__ __forceinline__ v4u cat(v2u a, v2u b) { return v4u{a[0], a[1], b[0], b[1]}; }
__device__ __forceinline__ float ex2(float x) { return __builtin_amdgcn_exp2f(x); }
__device__ __forceinline__ float rcp(float x) { return __builtin_amdgcn_rcpf(x); }
__device__ __forceinline__ float gval(unsigned d) { return __uint_as_float(d & 0xFFFFFF00u); }
__device__ __forceinline__ unsigned d4(float f, unsigned tagbyte) { return ((__float_as_uint(f) + 0x80u) & 0xFFFFFF00u) | tagbyte; }
#define SB() __builtin_amdgcn_sched_barrier(0)
#define BAR() do { asm volatile("s_waitcnt lgkmcnt(0)" ::: "memory"); __builtin_amdgcn_s_barrier(); asm volatile("" ::: "memory"); } while (0)
#define FENCE() asm volatile("" ::: "memory")
#define WAITV(n) __builtin_amdgcn_s_waitcnt(0x0F70 | (n))
#define LD16(off, soff) __builtin_amdgcn_raw_buffer_load_b128(rs, (off), (soff), 16)
#define ST16(v, off, soff) __builtin_amdgcn_raw_buffer_store_b128((v), rs, (off), (soff), 16)
#define ST8(v, off, soff) __builtin_amdgcn_raw_buffer_store_b64((v), rs, (off), (soff), 16)
#define TBAD(v) __builtin_amdgcn_bitop3_b32((v)[1], (v)[3], tag, 0x7E)
#define ANY(t) (__builtin_amdgcn_ballot_w64((t) != 0) != 0)
#define DBAD(v) __builtin_amdgcn_bitop3_b32(__builtin_amdgcn_perm((v)[1], (v)[0], 0x0c0c0400u), __builtin_amdgcn_perm((v)[3], (v)[2], 0x04000c0cu), tag, 0x56)
#define EPOCH() asm volatile("" : "+s"(ep0)); const unsigned tag = ((ep0 & 0x7F7F7F7Fu) + 0x01010101u) ^ (ep0 & 0x80808080u)
#define UNTIL(BADEXPR, RELOAD) do { if (ANY(BADEXPR)) { int spins_ = SPIN_CAP; _Pragma("nounroll") do { FENCE(); RELOAD; if (!ANY(BADEXPR)) break; asm volatile("" : "+s"(spins_)); } while (--spins_); } } while (0)

__device__ __forceinline__ void mksel(unsigned (&sel)[8], int l) {
#pragma unroll
  for (int d = 0; d < 4; ++d) { const bool on = ((l & 7) >> 1) == d;
    sel[d] = on ? ((l & 1) ? 0x01000c0cu : 0x0c0c0100u) : 0x0c0c0c0cu; sel[4 + d] = on ? ((l & 1) ? 0x03020c0cu : 0x0c0c0302u) : 0x0c0c0c0cu; }
}
__device__ __forceinline__ v4u diag(unsigned pair, const unsigned (&sel)[8], int hi) {
  return v4u{__builtin_amdgcn_perm(0u, pair, sel[4 * hi]), __builtin_amdgcn_perm(0u, pair, sel[4 * hi + 1]), __builtin_amdgcn_perm(0u, pair, sel[4 * hi + 2]), __builtin_amdgcn_perm(0u, pair, sel[4 * hi + 3])};
}
__device__ __forceinline__ float upr8(float v) { return __builtin_bit_cast(float, __builtin_amdgcn_update_dpp(__builtin_bit_cast(int, v), __builtin_bit_cast(int, v), R8, 0xC, 0xF, false)); }
__device__ __forceinline__ v2u diag_out(v4f d, unsigned tb0, unsigned tb1) {
  const float t0 = upr8(d[0]), t1 = upr8(d[1]), t2 = upr8(d[2]), t3 = upr8(d[3]);
  return v2u{d4(fold32(t0, t2), tb0), d4(fold32(t1, t3), tb1)};
}

__global__ void __launch_bounds__(NTHR, 3) fused_decode(const u16* __restrict__ x, const u16* __restrict__ wq, const u16* __restrict__ w1, const u16* __restrict__ vc, unsigned char* ws,
                                                        const u16* __restrict__ kc, const u16* __restrict__ wo, const u16* __restrict__ w2, const u16* __restrict__ b1,
                                                        const u16* __restrict__ b2, u16* __restrict__ out) {
  __shared__ __attribute__((aligned(128))) unsigned char lds_[L_END];
  LAS unsigned char* L = (LAS unsigned char*)lds_;
  const int b = blockIdx.x, g = b & 7, j = b >> 3, l = threadIdx.x & 63;
  int w = __builtin_amdgcn_readfirstlane(threadIdx.x >> 6);
  const unsigned t0 = (TQ | TW) ? (unsigned)__builtin_amdgcn_s_memrealtime() : 0u;
  unsigned c0_, td_ = 0; { u64 t_; asm volatile("s_memrealtime %0\n s_waitcnt lgkmcnt(0)" : "=s"(t_) :: "memory"); c0_ = (unsigned)t_; }
  { u64 pc; unsigned keep, t_;
    asm volatile("s_cmp_lg_u32 %3, 4\n\ts_cbranch_scc1 1f\n\ts_getpc_b64 %0\n\ts_mov_b32 %1, m0\n\ts_mov_b32 m0, %5\n\tv_lshlrev_b32 %2, 7, %4\n\t"
                 "global_load_lds_dword %2, %0\n\ts_mov_b32 m0, %1\n1:"
                 : "=&s"(pc), "=&s"(keep), "=&v"(t_) : "s"(w), "v"(min((j * 5 + (l & 7) % 5) % 80, SH ? 70 : 64)), "i"(L_O) : "scc", "memory"); }
  const int kq = j & 3, rb_ = j >> 2, gn = (g + XQ) & 7, gp = (g - XO) & 7;
  const int gq = l >> 4, n = l & 15;
  const bool hasw1 = rb_ != 3; const int jw = rb_ == 7 ? j - 16 : j;
  int kvo = (g * SK + (SK - WIN) + j * 128 + (((w - 1) * 2 + ((w - 1) >> 2) + j) & 7) * 16) * 256, l16 = l * 16, kof = (n >> 2) * 1024 + gq * 64 + (n & 3) * 16;
  int vo = (l >> 3) * (16 * 8192) + (l & 7) * 16, so = kq * (128 * 8192) + gn * 1024 + rb_ * 128;
  const bool early = !SH || (FORCE >= 0 ? FORCE == 1 : (__builtin_amdgcn_s_getreg(20 | (3 << 11)) & 4) == 0);
  int vo1 = (hasw1 && early) ? (l >> 3) * (8 * FF * 2) + (l & 7) * 16 : OOB, so1 = g * (64 * FF * 2) + jw * 128;
  asm volatile("" : "+s"(kvo), "+s"(so), "+s"(so1));
  asm volatile("s_setprio 3" : "+v"(l16), "+v"(vo), "+v"(vo1), "+v"(kof));
  asm volatile("" :: "s"(x), "s"(kc), "s"(vc), "s"(wq), "s"(wo), "s"(w1), "s"(b1), "s"(w2), "s"(b2), "s"(out), "s"(ws));
  unsigned ep0 = 0; if (!EV) ep0 = *(const CAS unsigned*)ws;
  const rsrc_t rs = mk(ws, WS_END);
  v4u kv[4];
  if (w != 0) {
    const rsrc_t rk = mk(kc, 8u * SK * 256);
    if (PFQ && w == 3) {
      unsigned keep;
      const unsigned po = PFQ == 1 ? (j * 16 + 8 + (l & 7)) * 8192 + gn * 1024 + 384 + ((l >> 3) & 1) * 512
                        : PFQ == 2 ? (j * 16 + (l & 15)) * 8192 + gn * 1024 + 384 + ((l >> 4) & 1) * 512
                        : PFQ == 3 ? (j * 16 + 4 + (l & 15) % 12) * 8192 + gn * 1024 + 384
                        : (j * 16 + 8 + (l & 7)) * 8192 + gn * 1024 + 128 + ((l >> 3) & 3) * 256;
      asm volatile("s_mov_b32 %0, m0\n\ts_mov_b32 m0, %3\n\ts_nop 0\n\tglobal_load_lds_dword %1, %2 nt\n\ts_mov_b32 m0, %0" : "=&s"(keep) : "v"(po), "s"(wq), "i"(L_O + 256) : "memory");
    }
    if (EV) ep0 = *(const CAS unsigned*)ws;
    __builtin_amdgcn_s_setprio(2);
    __builtin_amdgcn_s_barrier();
    SB();
#pragma unroll
    for (int k = 0; k < KY; ++k) kv[k] = ldw(rk, kof + k * 256, kvo);
    SB();
#define RESTK(RK) do { \
    if (KY < 4 && w > 4) __builtin_amdgcn_s_barrier(); \
    SB(); \
    _Pragma("unroll") \
    for (int k = KY; k < 4; ++k) kv[k] = ldw(RK, kof + k * 256, kvo); \
    SB(); \
    if (KY < 4 && w <= 4) __builtin_amdgcn_s_barrier(); \
 } while (0)
    if (!LK) RESTK(rk);
    if (LK == 2 && w <= 4) {
#pragma unroll
      for (int k = KY; k < 4; ++k) kv[k] = ldw(rk, kof + k * 256, kvo); }
  }
  asm volatile("" : "+s"(w));
  if (__builtin_expect(w == 0, 1)) {
    v4u r16[16], f8[8];
    const v4u xv = *(const GAS v4u*)((const GAS unsigned char*)x + (unsigned)l16);
    const unsigned epv = EV ? __builtin_amdgcn_raw_buffer_load_b32(rs, 0, 0, 0) : 0u;
    { const rsrc_t r = mk(wq, 512 * 4096 * 2), r1 = mk(w1, 512 * FF * 2);
#pragma unroll
      for (int i = 0; i < 16; ++i) r16[i] = ldw(r, vo, so + i * 8192);
#pragma unroll
      for (int i = 0; i < 8; ++i) f8[i] = ldw(r1, vo1, so1 + i * (FF * 2)); }
    SB();
    __builtin_amdgcn_s_barrier();
    SB();
    unsigned sel[8]; mksel(sel, l);
    int gvo = (l & 8) ? OOB : (l & 7) * 32 + (l & 16) + (l >> 5) * 8, xra = L_XN + kq * 256 + (l >> 3) * 32, xrc = L_XN + g * 128 + (l >> 3) * 16;
    asm volatile("" : "+v"(sel[0]), "+v"(sel[1]), "+v"(sel[2]), "+v"(sel[3]), "+v"(sel[4]), "+v"(sel[5]), "+v"(sel[6]), "+v"(sel[7]), "+v"(gvo), "+v"(xra), "+v"(xrc));
    SB();
    float xf[8];
#pragma unroll
    for (int i = 0; i < 4; ++i) { xf[2 * i] = bflo(xv[i]); xf[2 * i + 1] = bfhi(xv[i]); }
    const float mean = sum64(((xf[0] + xf[1]) + (xf[2] + xf[3])) + ((xf[4] + xf[5]) + (xf[6] + xf[7]))) * (1.f / 512);
    float vs = 0.f;
#pragma unroll
    for (int i = 0; i < 8; ++i) { const float d = xf[i] - mean; vs = fmaf(d, d, vs); }
    const float var = sum64(vs) * (1.f / 512);
    const float mb = rb(mean), rsd = rcp(rb(__builtin_amdgcn_sqrtf(rb(rb(var) + 1e-5f))));
    v4u xn;
#pragma unroll
    for (int i = 0; i < 4; ++i) { const unsigned d = pk(xf[2 * i] - mb, xf[2 * i + 1] - mb); xn[i] = pk(bflo(d) * rsd, bfhi(d) * rsd); }
    *(LAS v4u*)(L + L_XN + l * 16) = xn;
    *(LAS float*)(L + L_M + (l & 3) * 4) = -__builtin_inff();
    const v4u xa = *(LAS v4u*)(L + xra), xb = *(LAS v4u*)(L + xra + 16), xc = *(LAS v4u*)(L + xrc);
    if (EV) ep0 = __builtin_amdgcn_readfirstlane(epv);
    EPOCH();
    const unsigned tb0 = (tag >> ((l >> 5) * 16)) & 0xFFu, tb1 = (tag >> ((l >> 5) * 16 + 8)) & 0xFFu;
    { v4u bx[8];
#pragma unroll
      for (int i = 0; i < 8; ++i) bx[i] = diag(xa[i >> 1], sel, i & 1);
      SB();
      v4f d = {0.f, 0.f, 0.f, 0.f};
#pragma unroll
      for (int i = 0; i < 8; ++i) { d = mfma(bx[i], r16[i], d); bx[i] = diag(xb[i >> 1], sel, i & 1); SB(); }
#pragma unroll
      for (int i = 0; i < 8; ++i) d = mfma(bx[i], r16[8 + i], d);
      ST8(diag_out(d, tb0, tb1), gvo, WS_QG + gn * 8192 + kq * 2048 + rb_ * 256);
      STAMP(3);
      SB();
      if (SH) { const rsrc_t r1 = mk(w1, 512 * FF * 2);
#pragma unroll
        for (int i = 0; i < 8; ++i) r16[i] = ldw(r1, vo1, so1 + ((g ^ 4) - g) * (64 * FF * 2) + i * (FF * 2)); }
      SB();
      if (KY < 4) __builtin_amdgcn_s_barrier();
      SB();
    }
    { v4f d = {0.f, 0.f, 0.f, 0.f};
#pragma unroll
      for (int i = 0; i < 8; ++i) d = mfma(diag(xc[i >> 1], sel, i & 1), f8[i], d);
      ST8(diag_out(d, tb0, tb1), (hasw1 && early) ? gvo : OOB, WS_HG + g * (FF * 4) + jw * 256); }
    const int hr0 = gp * 224 + rb_ * 28, hu = min(l & 7, 6), ocol = (b & 63) * 8 + (l & 3) * 2 + (l >> 5);
    int mlo = (l & 51) == 0 ? (n >> 2) * 512 : OOB, mls = WS_ML + g * 2048 + j * 16, hvo = (l >> 3) * (FF * 4) + hu * 16, hso = WS_HG + hr0 * 4;
    int pvo = (l & 31) * 16, pso = WS_ML + gp * 2048 + (j >> 3) * 512;
    asm volatile("" : "+v"(mlo), "+v"(hvo), "+v"(pvo));
    asm volatile("" : "+s"(mls), "+s"(hso), "+s"(pso));
    const float b1v = bflo(b1[hr0 + hu * 4 + gq]), b2v = bflo(b2[ocol]), xo = bflo(x[ocol]);
    if (SH) { const v4u xd = *(LAS v4u*)(L + (xrc ^ 512)); v4f d = {0.f, 0.f, 0.f, 0.f};
#pragma unroll
      for (int i = 0; i < 8; ++i) d = mfma(diag(xd[i >> 1], sel, i & 1), r16[i], d);
      ST8(diag_out(d, tb0, tb1), (hasw1 && early) ? gvo : OOB, WS_HG + (g ^ 4) * (FF * 4) + jw * 256); }
    BAR();
    BAR();
    v4u hg; if (!SH) hg = LD16(hvo, hso);
    SB();
    BAR();
    { const v2u mm = *(LAS v2u*)(L + L_MX + l * 8);
      const v4f s0 = *(LAS v4f*)(L + L_S + l * 16), s1 = *(LAS v4f*)(L + L_S + 1024 + l * 16);
      const float m = *(LAS float*)(L + L_M + (n >> 2) * 4);
      float ls = fmaf((s0[0] + s0[1]) + (s0[2] + s0[3]), ex2(uf(mm[0]) - m), ((s1[0] + s1[1]) + (s1[2] + s1[3])) * ex2(uf(mm[1]) - m));
      ls += dpp<X1>(ls); ls += dpp<X2>(ls); ls = fold16(ls, ls); ls = fold32(ls, ls);
      ST16((v4u{__float_as_uint(m), tag, __float_as_uint(ls), tag}), mlo, mls);
    }
    STAMP(6);
    v4u own;
    if (SH) { own = LD16(n * 16, WS_HG + g * (FF * 4) + jw * 256); hg = LD16(hvo, hso); SB(); }
    BAR();
    if (SH && __builtin_expect(hasw1 && !early && ANY(DBAD(own)), 0)) { int tries = 32;
      _Pragma("nounroll") do { FENCE(); own = LD16(n * 16, WS_HG + g * (FF * 4) + jw * 256); } while (ANY(DBAD(own)) && --tries);
      if (!tries) {
      const rsrc_t r1 = mk(w1, 512 * FF * 2); v4f d = {0.f, 0.f, 0.f, 0.f};
#pragma unroll
      for (int i = 0; i < 8; ++i) f8[i] = ldw(r1, (l >> 3) * (8 * FF * 2) + (l & 7) * 16, so1 + i * (FF * 2));
#pragma unroll
      for (int i = 0; i < 8; ++i) d = mfma(diag(xc[i >> 1], sel, i & 1), f8[i], d);
      ST8(diag_out(d, tb0, tb1), gvo, WS_HG + g * (FF * 4) + jw * 256);
    } }
    {
      if (__builtin_expect(ANY(DBAD(hg)), 0)) UNTIL(DBAD(hg), hg = LD16(hvo, hso));
      float hs = fold16(fold32(gval(hg[0]), gval(hg[2])), fold32(gval(hg[1]), gval(hg[3]))); hs += dpp<R8>(hs);
      const float hp = rb(rb(hs) + b1v);
      const float u2 = 1.5957691216057308f * fmaf(0.044715f * hp * hp, hp, hp);
      const float ge = hp * rcp(1.f + ex2(-u2 * LOG2E));
      *(LAS u16*)(L + L_H + (l & 7) * 8 + gq * 2) = ((l & 7) < 7) ? (u16)pk(ge, ge) : (u16)0;
    }
    __builtin_amdgcn_s_sleep(MLD);
    v4u pv = LD16(pvo, pso);
    SB();
    BAR();
    {
      UNTIL(TBAD(pv), pv = LD16(pvo, pso));
      const float m = uf(pv[0]);
      const float M = max16(mx_r4(mx_r8(mx_x2(mx_x1(m)))));
      const float e = ex2(m - M);
      float ls = e * uf(pv[2]);
      ls += dpp<X1>(ls); ls += dpp<X2>(ls); ls += dpp<HM>(ls); ls += dpp<R8>(ls); ls = fold16(ls, ls);
      *(LAS float*)(L + L_WG + (l & 31) * 4) = e * rcp(ls);
    }
    STAMP(8);
    BAR();
    if (b < 64) {
      const int fo = WS_FB + b * 4096;
      v4u f0 = LD16(l16, fo), f1 = LD16(l16 + 1024, fo), f2 = LD16(l16 + 2048, fo), f3 = LD16(l16 + 3072, fo);
      UNTIL(TBAD(f0) | TBAD(f1) | TBAD(f2) | TBAD(f3), (f0 = LD16(l16, fo), f1 = LD16(l16 + 1024, fo), f2 = LD16(l16 + 2048, fo), f3 = LD16(l16 + 3072, fo)));
      float t = fold32((uf(f0[0]) + uf(f1[0])) + (uf(f2[0]) + uf(f3[0])), (uf(f0[2]) + uf(f1[2])) + (uf(f2[2]) + uf(f3[2])));
      t = fold16(t, t); t += dpp<R8>(t); t += dpp<R4>(t);
      const float f = rb(rb(t) + b2v);
      float gt = rb(f * rcp(1.f + ex2(-f * LOG2E)));
      int frd = L_V + ((l >> 5) * 576 + (l & 3) * 136 + ((l >> 2) & 7) * 16) * 4;
      int ovo = (l & 28) ? OOB : ocol * 2; float xo_ = xo;
      asm volatile("" : "+v"(gt), "+v"(frd), "+v"(ovo), "+v"(xo_));
      BAR();
      const v4f u0 = *(LAS v4f*)(L + frd), u1 = *(LAS v4f*)(L + frd + 16);
      const v4f u = (u0 + u1) + (*(LAS v4f*)(L + frd + 32) + *(LAS v4f*)(L + frd + 48));
      float v = (u[0] + u[2]) + (u[1] + u[3]);
      v = fold16(v, v); v += dpp<R8>(v); v += dpp<R4>(v);
      const float o = xo_ + rb(rb(v) * gt);
      __builtin_amdgcn_raw_buffer_store_b16((u16)pk(o, o), mk(out, 1024), ovo, 0, 16);
      if (b == 0 && l == 0) __hip_atomic_store((GAS unsigned*)ws, tag, __ATOMIC_RELAXED, __HIP_MEMORY_SCOPE_AGENT);
    }
    STAMP(10);
    FLUSH();
  } else {
    const int a = w - 1, a3 = a & 3;
    int obo = (n & 3) == 0 ? (n >> 2) * 16384 + gq * 16 : OOB;
    int sbo = (l & 16) ? OOB : (l >> 5) * 16384 + (n >> 3) * 8192 + (n & 7) * 8;
    int obs = WS_OB + g * 65536 + a3 * 4096 + j * 128, sbs = WS_SB + ((j & 1) * 32 + a * 4) * 8192 + (g * 16 + (j >> 1)) * 64;
    int fbs = WS_FB + (kq * 16 + a * 2) * 4096 + (g * 8 + rb_) * 64;
    int sadr = L_S + (a & 1) * 1024 + gq * 256 + (n & 3) * 64 + (a >> 1) * 16 + (n >> 2) * 4, qadr = L_Q + (n & 3) * QSTR + gq * 64 + (n >> 2) * 16;
    int pso = WS_OB + gp * 65536 + (j >> 1) * 4096 + a3 * 1024, pvo = a >= 4 ? l * 16 : OOB;
    int oadr = L_O + n * 80 + gq * 16, owr = L_O + (a3 * 8 + (l >> 3)) * 80 + (l & 7) * 8;
    int mad = L_M + (n & 3) * 4, l8 = l * 8, mrd = L_M + (n >> 2) * 4;
    asm volatile("" : "+v"(mad), "+v"(l8), "+v"(mrd));
    int fso = WS_SB + b * 8192 + a * 1024, fadr = L_V + ((l & 3) * 136 + a * 16 + (l >> 2)) * 4;
    int vwr = L_V + (a * 16 + gq) * VSTR + n * 16;
    int qso = Q32 ? WS_QG + g * 16384 + a3 * 1024 : WS_QG + g * 8192 + a3 * 512, qvo = a >= 4 ? (Q32 ? l * 16 : (l >> 5) * 4096 + (l & 31) * 16) : OOB;
    int mxw = L_MX + gq * 128 + (n & 3) * 32 + a * 4, qwr = L_Q + a3 * QSTR + (Q32 ? l * 4 : (l & 31) * 8 + (l >> 5) * 4);
    int wov = (l >> 5) * 1024 + ((l & 31) ^ ((l >> 5) * 2)) * 16;
    asm volatile("" : "+v"(wov));
    int wos = (gp * 512 + (j >> 1) * 32 + a3 * 8) * 1024 + (j & 1) * 512, w2s = (gp * 224 + rb_ * 28 + a3 * 8) * 1024 + kq * 256, wvo = gq * 1024 + (n ^ (gq * 2)) * 16;
    int w2t = w2s + (a3 == 3 ? 0 : 4096);
    int wl0 = L_W + a3 * 4 * WBLK, wl1 = L_W + (16 + a3 * 2) * WBLK;
    v4u so_ = {(unsigned)(u64)wo, (unsigned)((u64)wo >> 32) & 0xFFFFu, 4096u * 512 * 2, 0x20000u}, s2_ = {(unsigned)(u64)w2, (unsigned)((u64)w2 >> 32) & 0xFFFFu, FF * 512u * 2, 0x20000u};
    asm volatile("" : "+v"(obo), "+v"(sbo), "+v"(sadr), "+v"(qadr), "+v"(oadr), "+v"(owr), "+v"(vwr), "+v"(fadr), "+v"(qvo), "+v"(mxw), "+v"(qwr), "+v"(pvo), "+v"(wvo));
    asm volatile("" : "+s"(obs), "+s"(sbs), "+s"(pso), "+s"(qso), "+s"(fso), "+s"(wos), "+s"(w2s), "+s"(so_), "+s"(s2_), "+s"(wl0), "+s"(wl1), "+s"(w2t));
    const rsrc_t rv = mk(vc, 8u * SK * 256);
    int tadr = L_V + (gq * 4 + (n >> 2)) * VSTR + a3 * 64 + (n & 3) * 8;
    asm volatile("" : "+v"(tadr));
    EPOCH();
    v4u o0 = {0u, tag, 0u, tag}, o1 = o0; u64 lead = 0x000F000F000F000Full;
    asm volatile("" : "+v"(o0), "+v"(o1)); asm volatile("" : "+s"(lead));
    SB();
#define DMA1(VO, SRD, SO) "buffer_load_dwordx4 " VO ", " SRD ", " SO " offen nt lds\n\ts_add_u32 m0, m0, %8\n\ts_add_u32 " SO ", " SO ", 0x800\n\t"
#define WDMA() do { unsigned keep; asm volatile("s_mov_b32 %0, m0\n\ts_mov_b32 m0, %6\n\ts_nop 0\n\t" DMA1("%10", "%4", "%1") DMA1("%10", "%4", "%1") DMA1("%10", "%4", "%1") DMA1("%10", "%4", "%1") \
                   "s_mov_b32 m0, %7\n\ts_nop 0\n\t" DMA1("%3", "%5", "%2") "buffer_load_dwordx4 %3, %5, %9 offen nt lds\n\ts_mov_b32 m0, %0" \
                   : "=&s"(keep), "+s"(wos), "+s"(w2s) : "v"(wvo), "s"(so_), "s"(s2_), "s"(wl0), "s"(wl1), "i"(WBLK), "s"(w2t), "v"(wov) : "memory", "scc"); } while (0)
#define WDO() do { unsigned keep; asm volatile("s_mov_b32 %0, m0\n\ts_mov_b32 m0, %4\n\ts_nop 0\n\t" \
      "buffer_load_dwordx4 %2, %3, %1 offen nt lds\n\ts_add_u32 m0, m0, %5\n\ts_add_u32 %1, %1, 0x800\n\tbuffer_load_dwordx4 %2, %3, %1 offen nt lds\n\ts_add_u32 m0, m0, %5\n\ts_add_u32 %1, %1, 0x800\n\t" \
      "buffer_load_dwordx4 %2, %3, %1 offen nt lds\n\ts_add_u32 m0, m0, %5\n\ts_add_u32 %1, %1, 0x800\n\tbuffer_load_dwordx4 %2, %3, %1 offen nt lds\n\ts_mov_b32 m0, %0" \
      : "=&s"(keep), "+s"(wos) : "v"(wov), "s"(so_), "s"(wl0), "i"(WBLK) : "memory", "scc"); } while (0)
#define WD2() do { unsigned keep; asm volatile("s_mov_b32 %0, m0\n\ts_mov_b32 m0, %4\n\ts_nop 0\n\t" \
      "buffer_load_dwordx4 %2, %3, %1 offen nt lds\n\ts_add_u32 m0, m0, %5\n\ts_nop 0\n\tbuffer_load_dwordx4 %2, %3, %6 offen nt lds\n\ts_mov_b32 m0, %0" \
      : "=&s"(keep) : "s"(w2s), "v"(wvo), "s"(s2_), "s"(wl1), "i"(WBLK), "s"(w2t) : "memory", "scc"); } while (0)
    if (LK == 1) { const rsrc_t rk2 = mk(kc, 8u * SK * 256); RESTK(rk2); }
    if (LK == 2) { const rsrc_t rk2 = mk(kc, 8u * SK * 256); __builtin_amdgcn_s_barrier(); SB();
      if (a >= 4) {
#pragma unroll
        for (int k = KY; k < 4; ++k) kv[k] = ldw(rk2, kof + k * 256, kvo); } else if (VD) __builtin_amdgcn_s_sleep(VD);
      SB(); }
    v4u vv[4];
    WAITV(3 - OT);
    SB();
#pragma unroll
    for (int i = 0; i < YV; ++i) vv[i] = ldw(rv, l16 + i * 1024, kvo);
    SB();
    if (a >= 4) {
      WAITV(3 - YT + YV);
      if (TQ) { while ((unsigned)__builtin_amdgcn_s_memrealtime() - t0 < TQ) __builtin_amdgcn_s_sleep(1); }
      __builtin_amdgcn_s_barrier();
    }
    SB();
    v4u q0, q1, q2, q3;
#define QLD() (q0 = LD16(qvo, qso), q1 = LD16(qvo, qso + 2048))
#define QBAD() (DBAD(q0) | DBAD(q1))
    QLD();
    SB();
    STAMP(9);
    if (a >= 4) __builtin_amdgcn_s_sleep(1);
#pragma unroll
    for (int i = YV; i < OV; ++i) vv[i] = ldw(rv, l16 + i * 1024, kvo);
    SB();
    if (a < 4) { __builtin_amdgcn_s_barrier(); if (OV < 4) __builtin_amdgcn_s_sleep(1); }
    SB();
#pragma unroll
    for (int i = OV; i < 4; ++i) vv[i] = ldw(rv, l16 + i * 1024, kvo);
    SB();
    WAITV(4 - YV);
    STAMP(2);
#define QSUM() do { const v4f u_ = __builtin_bit_cast(v4f, q0 & 0xFFFFFF00u) + __builtin_bit_cast(v4f, q1 & 0xFFFFFF00u); float c_, d_; const float a_ = sw32(u_[0], u_[2], c_), b_ = sw32(u_[1], u_[3], d_); \
        *(LAS unsigned*)(L + qwr) = pk(a_ + c_, b_ + d_); } while (0)
    if (a >= 4) {
      QSUM();
      SB();
      if (__builtin_expect(ANY(QBAD()), 0)) {
        UNTIL(QBAD(), QLD());
        QSUM();
      }
    }
    SB();
    if (a < 4) {
#pragma unroll
      for (int i = 0; i < 4; ++i) *(LAS v4u*)(L + vwr + i * 4 * VSTR) = vv[i];
      if (WP == 0) WDMA();
      if (WS == 5 || WS == 6) WD2();
    }
    const bool wnow = !TW || (unsigned)__builtin_amdgcn_s_memrealtime() - t0 >= TW;
    STAMP(4);
    BAR();
    {
      const v4u qf = *(LAS v4u*)(L + qadr);
      if (WS == 0 && WP == 1 && a < 4 && wnow) WDMA();
      if ((WS == 1 || WS == 3 || WS == 7) && a < 4) WD2();
      if ((WS == 2 || WS == 4) && a < 4) WDO();
      v4f sc[4];
#pragma unroll
      for (int i = 0; i < 4; ++i) { sc[i] = mfma(kv[i], qf, v4f{0.f, 0.f, 0.f, 0.f}); SB(); }
      float t, m;
#define DADD(src, acc, rot, bank) "v_add_f32_dpp %0, " src ", " acc " row_ror:" rot " row_mask:0xf bank_mask:" bank "\n\t"
      asm volatile("s_nop 4\n\t"
                   DADD("%3", "%2", "12", "0x1") DADD("%6", "%7", "4", "0x2") DADD("%10", "%12", "8", "0x4") DADD("%14", "%17", "12", "0x8")
                   DADD("%4", "%0", "8", "0x1") DADD("%8", "%0", "12", "0x2") DADD("%11", "%0", "4", "0x4") DADD("%15", "%0", "8", "0x8")
                   DADD("%5", "%0", "4", "0x1") DADD("%9", "%0", "8", "0x2") DADD("%13", "%0", "12", "0x4") DADD("%16", "%0", "4", "0x8")
                   "v_cvt_pk_bf16_f32 %0, 0, %0\n\tv_mul_f32 %0, 0x3db504f3, %0\n\tv_cvt_pk_bf16_f32 %0, 0, %0\n\tv_mul_f32 %0, 0x3fb8aa3b, %0\n\ts_nop 1\n\t"
                   "v_max_f32_dpp %1, %0, %0 row_ror:8 row_mask:0xf bank_mask:0xf\n\ts_nop 1\n\t"
                   "v_max_f32_dpp %1, %1, %1 row_ror:4 row_mask:0xf bank_mask:0xf\n\t"
                   "v_sub_f32 %0, %0, %1\n\tv_exp_f32 %0, %0\n\t"
                   "s_mov_b64 exec, %21\n\tds_max_f32 %18, %1\n\tds_write_b32 %19, %1\n\ts_mov_b64 exec, -1\n\tds_write_b32 %20, %0"
                   : "=&v"(t), "=&v"(m)
                   : "v"(sc[0][0]), "v"(sc[0][1]), "v"(sc[0][2]), "v"(sc[0][3]), "v"(sc[1][0]), "v"(sc[1][1]), "v"(sc[1][2]), "v"(sc[1][3]),
                     "v"(sc[2][0]), "v"(sc[2][1]), "v"(sc[2][2]), "v"(sc[2][3]), "v"(sc[3][0]), "v"(sc[3][1]), "v"(sc[3][2]), "v"(sc[3][3]),
                     "v"(mad), "v"(mxw), "v"(sadr), "s"(lead)
                   : "memory");
#pragma unroll
      for (int i = 0; i < 5; ++i) __builtin_amdgcn_s_setprio(2);
    }
    v2u vt[16];
    if (a < 4) {
#pragma unroll
      for (int i = 0; i < 8; ++i) vt[(i & 3) + (i >> 2) * 8] = trd(L + tadr + (i & 3) * 16 * VSTR + (i >> 2) * 32);
      if (WP == 2) WDMA();
      if (WS == 6 || WS == 7) WDO();
    }
    STAMP(5);
    BAR();
    if (a < 4) {
      const v2u mm = *(LAS v2u*)(L + L_MX + l8);
      const float m = *(LAS float*)(L + mrd);
      const v4f s0 = *(LAS v4f*)(L + L_S + l16), s1 = *(LAS v4f*)(L + L_S + 1024 + l16);
      const v4f p0 = s0 * ex2(uf(mm[0]) - m), p1 = s1 * ex2(uf(mm[1]) - m);
      const v4u pb = {pk(p0[0], p0[1]), pk(p0[2], p0[3]), pk(p1[0], p1[1]), pk(p1[2], p1[3])};
      const v4f z = {0.f, 0.f, 0.f, 0.f};
      v4f d0 = mfma(cat(vt[0], vt[1]), pb, z), d1 = mfma(cat(vt[8], vt[9]), pb, z);
      const v4u pb1 = dpp4<0x55>(pb), pb2 = dpp4<0xAA>(pb);
      SB();
      d0 = mfma(cat(vt[2], vt[3]), pb1, d0); d1 = mfma(cat(vt[10], vt[11]), pb1, d1);
      v4u pb3 = dpp4<0xFF>(pb);
      asm volatile("" : "+v"(pb3) : "v"(pb1), "v"(vt[0]), "v"(vt[1]), "v"(vt[8]), "v"(vt[9]));
      SB();
      if (WS == 1 || WS == 5) WDO();
      if (WS == 2) WD2();
      STAMP(7);
      BAR();
#pragma unroll
      for (int i = 0; i < 8; ++i) vt[4 + (i & 1) + ((i >> 2) & 1) * 2 + ((i >> 1) & 1) * 8] = trd(L + tadr + (4 + (i & 1) + ((i >> 2) & 1) * 2) * 16 * VSTR + ((i >> 1) & 1) * 32);
      d0 = mfma(cat(vt[4], vt[5]), pb2, d0); d1 = mfma(cat(vt[12], vt[13]), pb2, d1);
      d0 = mfma(cat(vt[6], vt[7]), pb3, d0); d1 = mfma(cat(vt[14], vt[15]), pb3, d1);
      o0[0] = pk(d0[0], d0[1]); o0[2] = pk(d0[2], d0[3]); o1[0] = pk(d1[0], d1[1]); o1[2] = pk(d1[2], d1[3]);
      ST16(o0, obo, obs);
      ST16(o1, obo + 64, obs);
      STAMP(6);
      if (WS == 3) { WDO(); asm volatile("s_waitcnt vmcnt(0)" ::: "memory"); } else if (WS == 4) { WD2(); asm volatile("s_waitcnt vmcnt(0)" ::: "memory"); }
      else if (WP == 3 || !wnow) { if (TW) while ((unsigned)__builtin_amdgcn_s_memrealtime() - t0 < TX) __builtin_amdgcn_s_sleep(1); WDMA(); asm volatile("s_waitcnt vmcnt(0)" ::: "memory"); }
      else asm volatile("s_waitcnt vmcnt(2)" ::: "memory");
      STAMP(1);
    }
    SB();
    if (a >= 4) {
#pragma unroll
      for (int i = 0; i < 4; ++i) *(LAS v4u*)(L + vwr + i * 4 * VSTR) = vv[i];
      STAMP(3);
      BAR();
      __builtin_amdgcn_s_sleep(PD);
    }
    SB();
    v4u pv = LD16(pvo, pso);
    BAR();
    if (a >= 4 && ANY(TBAD(pv))) pv = LD16(pvo, pso);
    SB();
    v2u wt[4];
    const int wrd = L_W + gq * 2 * WBLK + (n >> 2) * 256 + (a ^ (n >> 2)) * 32 + (n & 3) * 8;
#pragma unroll
    for (int i = 0; i < 4; ++i) wt[i] = trd(L + L_W + (gq * 4 + (i & 1) * 2 + (n >> 3)) * WBLK + ((n >> 2) & 1) * 512 + ((a * 2 + (i >> 1)) ^ ((n >> 2) & 1)) * 32 + (n & 3) * 8);
    SB();
    WAITV(0);
    if (a < 4) {
      const v4u hf = *(LAS v4u*)(L + L_H + gq * 16);
      LAS unsigned char* p = L + wrd + 16 * WBLK;
      const v4f z = {0.f, 0.f, 0.f, 0.f};
      const v4f f0 = mfma(hf, cat(trd(p), trd(p + WBLK)), z), f1 = mfma(hf, cat(trd(p + 128), trd(p + 128 + WBLK)), z);
      ST8((v2u{__float_as_uint(gq ? f1[0] : f0[0]), tag}), l < 32 ? gq * 32768 + (n >> 3) * 4096 + (n & 7) * 8 : OOB, fbs);
    }
    SB();
    if (a >= 4) {
#define OWR() (((LAS unsigned*)(L + owr))[0] = pv[0], ((LAS unsigned*)(L + owr))[1] = pv[2])
      OWR();
      UNTIL(TBAD(pv), (pv = LD16(pvo, pso), OWR()));
      STAMP(8);
    }
    BAR();
    {
      const v4u of0 = *(LAS v4u*)(L + oadr), of1 = *(LAS v4u*)(L + oadr + 16 * 80);
      const v4f w0 = *(LAS v4f*)(L + L_WG + gq * 16), w1_ = *(LAS v4f*)(L + L_WG + 64 + gq * 16);
      SB();
      const v4f z = {0.f, 0.f, 0.f, 0.f};
      const v4f d00 = mfma(of0, cat(wt[0], wt[1]), z), d01 = mfma(of0, cat(wt[2], wt[3]), z), d10 = mfma(of1, cat(wt[0], wt[1]), z), d11 = mfma(of1, cat(wt[2], wt[3]), z);
      SB();
      v2f c0 = d00.hi * w0.hi + d00.lo * w0.lo, c1 = d01.hi * w0.hi + d01.lo * w0.lo;
      c0 = d10.hi * w1_.hi + (d10.lo * w1_.lo + c0); c1 = d11.hi * w1_.hi + (d11.lo * w1_.lo + c1);
      float e = fold32(c0[0] + c0[1], c1[0] + c1[1]);
      e = fold16(e, e);
      ST8((v2u{__float_as_uint(e), tag}), sbo, sbs);
    }
    STAMP(10);
    if (b < 64) {
      __builtin_amdgcn_s_sleep(10); if (a == g) __builtin_amdgcn_s_sleep(10);
      v4u sv = LD16(l16, fso);
#define SWR() (((LAS unsigned*)(L + fadr))[0] = sv[0], ((LAS unsigned*)(L + fadr))[576] = sv[2])
      SWR();
      UNTIL(TBAD(sv), (sv = LD16(l16, fso), SWR()));
      BAR();
    }
    FLUSH();
  }
}

extern "C" void kernel_launch(void* const* d_in, const int* in_sizes, int n_in, void* d_out, int out_size, void* d_ws, size_t ws_size, hipStream_t stream) {
  fused_decode<<<256, NTHR, 0, stream>>>((const u16*)d_in[0], (const u16*)d_in[3], (const u16*)d_in[6], (const u16*)d_in[2], (unsigned char*)d_ws, (const u16*)d_in[1],
                                         (const u16*)d_in[4], (const u16*)d_in[8], (const u16*)d_in[7], (const u16*)d_in[9], (u16*)d_out);
}
```

```cpp
#define LK 1
#define KY 2
#define YT 2
#define PD 6
#define XQ 1
#define XO 1
#define WS 1
#define MLD 8
#include <hip/hip_runtime.h>
#include <hip/hip_bf16.h>

typedef unsigned short u16;
typedef unsigned long long u64;
typedef unsigned v4u __attribute__((ext_vector_type(4)));
typedef unsigned v2u __attribute__((ext_vector_type(2)));
typedef float v4f __attribute__((ext_vector_type(4)));
typedef short s16x4 __attribute__((ext_vector_type(4)));
typedef __bf16 bf16x2 __attribute__((ext_vector_type(2)));
typedef __bf16 bf16x8 __attribute__((ext_vector_type(8)));
typedef __amdgpu_buffer_rsrc_t rsrc_t;
#define GAS __attribute__((address_space(1)))
#define CAS __attribute__((address_space(4)))
#define LAS __attribute__((address_space(3)))

#define TQ 0
#define YV 0
#define OV 4
#define OT 0
#define EV 1
#define TW 0
#define TX TW
#define Q32 0
#define PFQ 1
#define SH 0
#define FORCE -1
#define VD 2
#define WP 1
#define STAMP(i)
#define FLUSH()
constexpr int SK = 32768, WIN = 4096, FF = 1792;
constexpr int NTHR = 576;
constexpr unsigned WS_QG = 4096;
constexpr unsigned WS_HG = WS_QG + 131072;
constexpr unsigned WS_ML = WS_HG + 57344;
constexpr unsigned WS_OB = WS_ML + 16384;
constexpr unsigned WS_SB = WS_OB + 524288;
constexpr unsigned WS_FB = WS_SB + 524288;
constexpr unsigned WS_END = WS_FB + 262144;
constexpr int VSTR = 288, L_V = 0, L_O = 128 * VSTR, L_Q = L_O + 4096, QSTR = 320, L_S = L_Q + 4 * QSTR, L_XN = (L_S + 2048 + 1023) / 1024 * 1024, L_MX = L_XN + 1024, L_M = L_MX + 512, L_H = L_M + 32,
              L_WG = L_H + 64, L_W = L_WG + 128, WBLK = 1088, L_END = L_W + 24 * WBLK;
static_assert(L_O % 128 == 0, "the o image is addressed with ^ 32 and ^ 64");
constexpr unsigned SPIN_CAP = 1u << 18;
constexpr float LOG2E = 1.4426950408889634f;
constexpr int OOB = 0x40000000;

__device__ __forceinline__ rsrc_t mk(const void* p, unsigned bytes) { return __builtin_amdgcn_make_buffer_rsrc((void*)p, 0, bytes, 0x00020000); }
__device__ __forceinline__ v4u ldw(rsrc_t r, int voff, int soff) { return __builtin_amdgcn_raw_buffer_load_b128(r, voff, soff, 2); }
__device__ __forceinline__ float uf(unsigned u) { return __uint_as_float(u); }
__device__ __forceinline__ float bflo(unsigned u) { return __uint_as_float(u << 16); }
__device__ __forceinline__ float bfhi(unsigned u) { return __uint_as_float(u & 0xFFFF0000u); }
__device__ __forceinline__ unsigned pk(float lo, float hi) { bf16x2 r = {(__bf16)lo, (__bf16)hi}; return __builtin_bit_cast(unsigned, r); }
typedef float v2f __attribute__((ext_vector_type(2)));
__device__ __forceinline__ float rb(float f) { return __builtin_bit_cast(float, __builtin_convertvector((v2f{0.f, f}), bf16x2)); }
__device__ __forceinline__ v4f mfma(v4u a, v4u b, v4f c) { return __builtin_amdgcn_mfma_f32_16x16x32_bf16(__builtin_bit_cast(bf16x8, a), __builtin_bit_cast(bf16x8, b), c, 0, 0, 0); }
template <int CTRL> __device__ __forceinline__ unsigned dppu(unsigned x) { return (unsigned)__builtin_amdgcn_mov_dpp((int)x, CTRL, 0xf, 0xf, true); }
template <int CTRL> __device__ __forceinline__ float dpp(float x) { return __uint_as_float(dppu<CTRL>(__float_as_uint(x))); }
template <int CTRL> __device__ __forceinline__ v4u dpp4(v4u x) { return v4u{dppu<CTRL>(x[0]), dppu<CTRL>(x[1]), dppu<CTRL>(x[2]), dppu<CTRL>(x[3])}; }
constexpr int X1 = 0xB1, X2 = 0x4E, HM = 0x141, R4 = 0x124, R8 = 0x128;
__device__ __forceinline__ float sw16(float a, float b, float& o) { auto r = __builtin_amdgcn_permlane16_swap(__float_as_uint(a), __float_as_uint(b), false, false); o = uf(r[1]); return uf(r[0]); }
__device__ __forceinline__ float sw32(float a, float b, float& o) { auto r = __builtin_amdgcn_permlane32_swap(__float_as_uint(a), __float_as_uint(b), false, false); o = uf(r[1]); return uf(r[0]); }
__device__ __forceinline__ float fold16(float a, float b) { float o; const float p = sw16(a, b, o); return p + o; }
__device__ __forceinline__ float fold32(float a, float b) { float o; const float p = sw32(a, b, o); return p + o; }
__device__ __forceinline__ float fmx(float a, float b) { return __builtin_elementwise_maximum(a, b); }
__device__ __forceinline__ float max16(float a) { float o; const float p = sw16(a, a, o); return fmx(p, o); }
#define MAXDPP(name, ctl) __device__ __forceinline__ float name(float m) { float r; asm("s_nop 1\n\tv_max_f32_dpp %0, %1, %1 " ctl " row_mask:0xf bank_mask:0xf" : "=v"(r) : "v"(m)); return r; }
MAXDPP(mx_r8, "row_ror:8") MAXDPP(mx_r4, "row_ror:4") MAXDPP(mx_x1, "quad_perm:[1,0,3,2]") MAXDPP(mx_x2, "quad_perm:[2,3,0,1]")
__device__ __forceinline__ float sum64(float v) { v += dpp<X1>(v); v += dpp<X2>(v); v += dpp<HM>(v); v += dpp<R8>(v); v = fold16(v, v); return fold32(v, v); }
__device__ __forceinline__ v2u trd(LAS unsigned char* p) { return __builtin_bit_cast(v2u, __builtin_amdgcn_ds_read_tr16_b64_v4i16((LAS s16x4*)p)); }
__device__ __forceinline__ v4u cat(v2u a, v2u b) { return v4u{a[0], a[1], b[0], b[1]}; }
__device__ __forceinline__ float ex2(float x) { return __builtin_amdgcn_exp2f(x); }
__device__ __forceinline__ float rcp(float x) { return __builtin_amdgcn_rcpf(x); }
__device__ __forceinline__ float gval(unsigned d) { return __uint_as_float(d & 0xFFFFFF00u); }
__device__ __forceinline__ unsigned d4(float f, unsigned tagbyte) { return ((__float_as_uint(f) + 0x80u) & 0xFFFFFF00u) | tagbyte; }
#define SB() __builtin_amdgcn_sched_barrier(0)
#define BAR() do { asm volatile("s_waitcnt lgkmcnt(0)" ::: "memory"); __builtin_amdgcn_s_barrier(); asm volatile("" ::: "memory"); } while (0)
#define FENCE() asm volatile("" ::: "memory")
#define WAITV(n) __builtin_amdgcn_s_waitcnt(0x0F70 | (n))
#define LD16(off, soff) __builtin_amdgcn_raw_buffer_load_b128(rs, (off), (soff), 16)
#define ST16(v, off, soff) __builtin_amdgcn_raw_buffer_store_b128((v), rs, (off), (soff), 16)
#define ST8(v, off, soff) __builtin_amdgcn_raw_buffer_store_b64((v), rs, (off), (soff), 16)
#define TBAD(v) __builtin_amdgcn_bitop3_b32((v)[1], (v)[3], tag, 0x7E)
#define ANY(t) (__builtin_amdgcn_ballot_w64((t) != 0) != 0)
#define DBAD(v) __builtin_amdgcn_bitop3_b32(__builtin_amdgcn_perm((v)[1], (v)[0], 0x0c0c0400u), __builtin_amdgcn_perm((v)[3], (v)[2], 0x04000c0cu), tag, 0x56)
#define EPOCH() asm volatile("" : "+s"(ep0)); const unsigned tag = ((ep0 & 0x7F7F7F7Fu) + 0x01010101u) ^ (ep0 & 0x80808080u)
#define UNTIL(BADEXPR, RELOAD) do { if (ANY(BADEXPR)) { int spins_ = SPIN_CAP; _Pragma("nounroll") do { FENCE(); RELOAD; if (!ANY(BADEXPR)) break; asm volatile("" : "+s"(spins_)); } while (--spins_); } } while (0)

__device__ __forceinline__ void mksel(unsigned (&sel)[8], int l) {
#pragma unroll
  for (int d = 0; d < 4; ++d) { const bool on = ((l & 7) >> 1) == d;
    sel[d] = on ? ((l & 1) ? 0x01000c0cu : 0x0c0c0100u) : 0x0c0c0c0cu; sel[4 + d] = on ? ((l & 1) ? 0x03020c0cu : 0x0c0c0302u) : 0x0c0c0c0cu; }
}
__device__ __forceinline__ v4u diag(unsigned pair, const unsigned (&sel)[8], int hi) {
  return v4u{__builtin_amdgcn_perm(0u, pair, sel[4 * hi]), __builtin_amdgcn_perm(0u, pair, sel[4 * hi + 1]), __builtin_amdgcn_perm(0u, pair, sel[4 * hi + 2]), __builtin_amdgcn_perm(0u, pair, sel[4 * hi + 3])};
}
__device__ __forceinline__ float upr8(float v) { return __builtin_bit_cast(float, __builtin_amdgcn_update_dpp(__builtin_bit_cast(int, v), __builtin_bit_cast(int, v), R8, 0xC, 0xF, false)); }
__device__ __forceinline__ v2u diag_out(v4f d, unsigned tb0, unsigned tb1) {
  const float t0 = upr8(d[0]), t1 = upr8(d[1]), t2 = upr8(d[2]), t3 = upr8(d[3]);
  return v2u{d4(fold32(t0, t2), tb0), d4(fold32(t1, t3), tb1)};
}

__global__ void __launch_bounds__(NTHR, 3) fused_decode(const u16* __restrict__ x, const u16* __restrict__ wq, const u16* __restrict__ w1, const u16* __restrict__ vc, unsigned char* ws,
                                                        const u16* __restrict__ kc, const u16* __restrict__ wo, const u16* __restrict__ w2, const u16* __restrict__ b1,
                                                        const u16* __restrict__ b2, u16* __restrict__ out) {
  __shared__ __attribute__((aligned(128))) unsigned char lds_[L_END];
  LAS unsigned char* L = (LAS unsigned char*)lds_;
  const int b = blockIdx.x, g = b & 7, j = b >> 3, l = threadIdx.x & 63;
  int w = __builtin_amdgcn_readfirstlane(threadIdx.x >> 6);
  const unsigned t0 = (TQ | TW) ? (unsigned)__builtin_amdgcn_s_memrealtime() : 0u;
  const unsigned c0_ = (unsigned)__builtin_amdgcn_s_memrealtime();
  { u64 pc; unsigned keep, t_;
    asm volatile("s_cmp_lg_u32 %3, 4\n\ts_cbranch_scc1 1f\n\ts_getpc_b64 %0\n\ts_mov_b32 %1, m0\n\ts_mov_b32 m0, %5\n\tv_lshlrev_b32 %2, 7, %4\n\t"
                 "global_load_lds_dword %2, %0\n\ts_mov_b32 m0, %1\n1:"
                 : "=&s"(pc), "=&s"(keep), "=&v"(t_) : "s"(w), "v"(min((j * 5 + (l & 7) % 5) % 80, SH ? 70 : 64)), "i"(L_O) : "scc", "memory"); }
  const int kq = j & 3, rb_ = j >> 2, gn = (g + XQ) & 7, gp = (g - XO) & 7;
  const int gq = l >> 4, n = l & 15;
  const bool hasw1 = rb_ != 3; const int jw = rb_ == 7 ? j - 16 : j;
  int kvo = (g * SK + (SK - WIN) + j * 128 + (((w - 1) * 2 + ((w - 1) >> 2) + j) & 7) * 16) * 256, l16 = l * 16, kof = (n >> 2) * 1024 + gq * 64 + (n & 3) * 16;
  int vo = (l >> 3) * (16 * 8192) + (l & 7) * 16, so = kq * (128 * 8192) + gn * 1024 + rb_ * 128;
  const bool early = !SH || (FORCE >= 0 ? FORCE == 1 : (__builtin_amdgcn_s_getreg(20 | (3 << 11)) & 4) == 0);
  int vo1 = (hasw1 && early) ? (l >> 3) * (8 * FF * 2) + (l & 7) * 16 : OOB, so1 = g * (64 * FF * 2) + jw * 128;
  asm volatile("" : "+s"(kvo), "+s"(so), "+s"(so1));
  asm volatile("s_setprio 3" : "+v"(l16), "+v"(vo), "+v"(vo1), "+v"(kof));
  asm volatile("" :: "s"(x), "s"(kc), "s"(vc), "s"(wq), "s"(wo), "s"(w1), "s"(b1), "s"(w2), "s"(b2), "s"(out), "s"(ws));
  unsigned ep0 = 0; if (!EV) ep0 = *(const CAS unsigned*)ws;
  const rsrc_t rs = mk(ws, WS_END);
  v4u kv[4];
  if (w != 0) {
    const rsrc_t rk = mk(kc, 8u * SK * 256);
    if (PFQ && w == 3) {
      unsigned keep;
      const unsigned po = PFQ == 1 ? (j * 16 + 8 + (l & 7)) * 8192 + gn * 1024 + 384 + ((l >> 3) & 1) * 512
                        : PFQ == 2 ? (j * 16 + (l & 15)) * 8192 + gn * 1024 + 384 + ((l >> 4) & 1) * 512
                        : PFQ == 3 ? (j * 16 + 4 + (l & 15) % 12) * 8192 + gn * 1024 + 384
                        : (j * 16 + 8 + (l & 7)) * 8192 + gn * 1024 + 128 + ((l >> 3) & 3) * 256;
      asm volatile("s_mov_b32 %0, m0\n\ts_mov_b32 m0, %3\n\ts_nop 0\n\tglobal_load_lds_dword %1, %2 nt\n\ts_mov_b32 m0, %0" : "=&s"(keep) : "v"(po), "s"(wq), "i"(L_O + 256) : "memory");
    }
    if (EV) ep0 = *(const CAS unsigned*)ws;
    __builtin_amdgcn_s_setprio(2);
    __builtin_amdgcn_s_barrier();
    SB();
#pragma unroll
    for (int k = 0; k < KY; ++k) kv[k] = ldw(rk, kof + k * 256, kvo);
    SB();
#define RESTK(RK) do { \
    if (KY < 4 && w > 4) __builtin_amdgcn_s_barrier(); \
    SB(); \
    _Pragma("unroll") \
    for (int k = KY; k < 4; ++k) kv[k] = ldw(RK, kof + k * 256, kvo); \
    SB(); \
    if (KY < 4 && w <= 4) __builtin_amdgcn_s_barrier(); \
 } while (0)
    if (!LK) RESTK(rk);
    if (LK == 2 && w <= 4) {
#pragma unroll
      for (int k = KY; k < 4; ++k) kv[k] = ldw(rk, kof + k * 256, kvo); }
  }
  asm volatile("" : "+s"(w));
  if (__builtin_expect(w == 0, 1)) {
    v4u r16[16], f8[8];
    const v4u xv = *(const GAS v4u*)((const GAS unsigned char*)x + (unsigned)l16);
    const unsigned epv = EV ? __builtin_amdgcn_raw_buffer_load_b32(rs, 0, 0, 0) : 0u;
    { const rsrc_t r = mk(wq, 512 * 4096 * 2), r1 = mk(w1, 512 * FF * 2);
#pragma unroll
      for (int i = 0; i < 16; ++i) r16[i] = ldw(r, vo, so + i * 8192);
#pragma unroll
      for (int i = 0; i < 8; ++i) f8[i] = ldw(r1, vo1, so1 + i * (FF * 2)); }
    SB();
    __builtin_amdgcn_s_barrier();
    SB();
    unsigned sel[8]; mksel(sel, l);
    int gvo = (l & 8) ? OOB : (l & 7) * 32 + (l & 16) + (l >> 5) * 8, xra = L_XN + kq * 256 + (l >> 3) * 32, xrc = L_XN + g * 128 + (l >> 3) * 16;
    asm volatile("" : "+v"(sel[0]), "+v"(sel[1]), "+v"(sel[2]), "+v"(sel[3]), "+v"(sel[4]), "+v"(sel[5]), "+v"(sel[6]), "+v"(sel[7]), "+v"(gvo), "+v"(xra), "+v"(xrc));
    SB();
    float xf[8];
#pragma unroll
    for (int i = 0; i < 4; ++i) { xf[2 * i] = bflo(xv[i]); xf[2 * i + 1] = bfhi(xv[i]); }
    const float mean = sum64(((xf[0] + xf[1]) + (xf[2] + xf[3])) + ((xf[4] + xf[5]) + (xf[6] + xf[7]))) * (1.f / 512);
    float vs = 0.f;
#pragma unroll
    for (int i = 0; i < 8; ++i) { const float d = xf[i] - mean; vs = fmaf(d, d, vs); }
    const float var = sum64(vs) * (1.f / 512);
    const float mb = rb(mean), rsd = rcp(rb(__builtin_amdgcn_sqrtf(rb(rb(var) + 1e-5f))));
    v4u xn;
#pragma unroll
    for (int i = 0; i < 4; ++i) { const unsigned d = pk(xf[2 * i] - mb, xf[2 * i + 1] - mb); xn[i] = pk(bflo(d) * rsd, bfhi(d) * rsd); }
    *(LAS v4u*)(L + L_XN + l * 16) = xn;
    *(LAS float*)(L + L_M + (l & 3) * 4) = -__builtin_inff();
    const v4u xa = *(LAS v4u*)(L + xra), xb = *(LAS v4u*)(L + xra + 16), xc = *(LAS v4u*)(L + xrc);
    if (EV) ep0 = __builtin_amdgcn_readfirstlane(epv);
    EPOCH();
    const unsigned tb0 = (tag >> ((l >> 5) * 16)) & 0xFFu, tb1 = (tag >> ((l >> 5) * 16 + 8)) & 0xFFu;
    { v4u bx[8];
#pragma unroll
      for (int i = 0; i < 8; ++i) bx[i] = diag(xa[i >> 1], sel, i & 1);
      SB();
      v4f d = {0.f, 0.f, 0.f, 0.f};
#pragma unroll
      for (int i = 0; i < 8; ++i) { d = mfma(bx[i], r16[i], d); bx[i] = diag(xb[i >> 1], sel, i & 1); SB(); }
#pragma unroll
      for (int i = 0; i < 8; ++i) d = mfma(bx[i], r16[8 + i], d);
      ST8(diag_out(d, tb0, tb1), gvo, WS_QG + gn * 8192 + kq * 2048 + rb_ * 256);
      STAMP(3);
      SB();
      if (SH) { const rsrc_t r1 = mk(w1, 512 * FF * 2);
#pragma unroll
        for (int i = 0; i < 8; ++i) r16[i] = ldw(r1, vo1, so1 + ((g ^ 4) - g) * (64 * FF * 2) + i * (FF * 2)); }
      SB();
      if (KY < 4) __builtin_amdgcn_s_barrier();
      SB();
    }
    { v4f d = {0.f, 0.f, 0.f, 0.f};
#pragma unroll
      for (int i = 0; i < 8; ++i) d = mfma(diag(xc[i >> 1], sel, i & 1), f8[i], d);
      ST8(diag_out(d, tb0, tb1), (hasw1 && early) ? gvo : OOB, WS_HG + g * (FF * 4) + jw * 256); }
    const int hr0 = gp * 224 + rb_ * 28, hu = min(l & 7, 6), ocol = (b & 63) * 8 + (l & 3) * 2 + (l >> 5);
    int mlo = (l & 51) == 0 ? (n >> 2) * 512 : OOB, mls = WS_ML + g * 2048 + j * 16, hvo = (l >> 3) * (FF * 4) + hu * 16, hso = WS_HG + hr0 * 4;
    int pvo = (l & 31) * 16, pso = WS_ML + gp * 2048 + (j >> 3) * 512;
    asm volatile("" : "+v"(mlo), "+v"(hvo), "+v"(pvo));
    asm volatile("" : "+s"(mls), "+s"(hso), "+s"(pso));
    const float b1v = bflo(b1[hr0 + hu * 4 + gq]), b2v = bflo(b2[ocol]), xo = bflo(x[ocol]);
    if (SH) { const v4u xd = *(LAS v4u*)(L + (xrc ^ 512)); v4f d = {0.f, 0.f, 0.f, 0.f};
#pragma unroll
      for (int i = 0; i < 8; ++i) d = mfma(diag(xd[i >> 1], sel, i & 1), r16[i], d);
      ST8(diag_out(d, tb0, tb1), (hasw1 && early) ? gvo : OOB, WS_HG + (g ^ 4) * (FF * 4) + jw * 256); }
    BAR();
    BAR();
    v4u hg; if (!SH) hg = LD16(hvo, hso);
    SB();
    BAR();
    { const v2u mm = *(LAS v2u*)(L + L_MX + l * 8);
      const v4f s0 = *(LAS v4f*)(L + L_S + l * 16), s1 = *(LAS v4f*)(L + L_S + 1024 + l * 16);
      const float m = *(LAS float*)(L + L_M + (n >> 2) * 4);
      float ls = fmaf((s0[0] + s0[1]) + (s0[2] + s0[3]), ex2(uf(mm[0]) - m), ((s1[0] + s1[1]) + (s1[2] + s1[3])) * ex2(uf(mm[1]) - m));
      ls += dpp<X1>(ls); ls += dpp<X2>(ls); ls = fold16(ls, ls); ls = fold32(ls, ls);
      ST16((v4u{__float_as_uint(m), tag, __float_as_uint(ls), tag}), mlo, mls);
    }
    STAMP(6);
    v4u own;
    if (SH) { own = LD16(n * 16, WS_HG + g * (FF * 4) + jw * 256); hg = LD16(hvo, hso); SB(); }
    BAR();
    if (SH && __builtin_expect(hasw1 && !early && ANY(DBAD(own)), 0)) { int tries = 32;
      _Pragma("nounroll") do { FENCE(); own = LD16(n * 16, WS_HG + g * (FF * 4) + jw * 256); } while (ANY(DBAD(own)) && --tries);
      if (!tries) {
      const rsrc_t r1 = mk(w1, 512 * FF * 2); v4f d = {0.f, 0.f, 0.f, 0.f};
#pragma unroll
      for (int i = 0; i < 8; ++i) f8[i] = ldw(r1, (l >> 3) * (8 * FF * 2) + (l & 7) * 16, so1 + i * (FF * 2));
#pragma unroll
      for (int i = 0; i < 8; ++i) d = mfma(diag(xc[i >> 1], sel, i & 1), f8[i], d);
      ST8(diag_out(d, tb0, tb1), gvo, WS_HG + g * (FF * 4) + jw * 256);
    } }
    {
      if (__builtin_expect(ANY(DBAD(hg)), 0)) UNTIL(DBAD(hg), hg = LD16(hvo, hso));
      float hs = fold16(fold32(gval(hg[0]), gval(hg[2])), fold32(gval(hg[1]), gval(hg[3]))); hs += dpp<R8>(hs);
      const float hp = rb(rb(hs) + b1v);
      const float u2 = 1.5957691216057308f * fmaf(0.044715f * hp * hp, hp, hp);
      const float ge = hp * rcp(1.f + ex2(-u2 * LOG2E));
      *(LAS u16*)(L + L_H + (l & 7) * 8 + gq * 2) = ((l & 7) < 7) ? (u16)pk(ge, ge) : (u16)0;
    }
    __builtin_amdgcn_s_sleep(MLD);
    v4u pv = LD16(pvo, pso);
    SB();
    BAR();
    {
      UNTIL(TBAD(pv), pv = LD16(pvo, pso));
      const float m = uf(pv[0]);
      const float M = max16(mx_r4(mx_r8(mx_x2(mx_x1(m)))));
      const float e = ex2(m - M);
      float ls = e * uf(pv[2]);
      ls += dpp<X1>(ls); ls += dpp<X2>(ls); ls += dpp<HM>(ls); ls += dpp<R8>(ls); ls = fold16(ls, ls);
      *(LAS float*)(L + L_WG + (l & 31) * 4) = e * rcp(ls);
    }
    STAMP(8);
    BAR();
    if (b < 64) {
      const int fo = WS_FB + b * 4096;
      v4u f0 = LD16(l16, fo), f1 = LD16(l16 + 1024, fo), f2 = LD16(l16 + 2048, fo), f3 = LD16(l16 + 3072, fo);
      UNTIL(TBAD(f0) | TBAD(f1) | TBAD(f2) | TBAD(f3), (f0 = LD16(l16, fo), f1 = LD16(l16 + 1024, fo), f2 = LD16(l16 + 2048, fo), f3 = LD16(l16 + 3072, fo)));
      float t = fold32((uf(f0[0]) + uf(f1[0])) + (uf(f2[0]) + uf(f3[0])), (uf(f0[2]) + uf(f1[2])) + (uf(f2[2]) + uf(f3[2])));
      t = fold16(t, t); t += dpp<R8>(t); t += dpp<R4>(t);
      const float f = rb(rb(t) + b2v);
      float gt = rb(f * rcp(1.f + ex2(-f * LOG2E)));
      int frd = L_V + ((l >> 5) * 576 + (l & 3) * 136 + ((l >> 2) & 7) * 16) * 4;
      int ovo = (l & 28) ? OOB : ocol * 2; float xo_ = xo;
      asm volatile("" : "+v"(gt), "+v"(frd), "+v"(ovo), "+v"(xo_));
      BAR();
      const v4f u0 = *(LAS v4f*)(L + frd), u1 = *(LAS v4f*)(L + frd + 16);
      const v4f u = (u0 + u1) + (*(LAS v4f*)(L + frd + 32) + *(LAS v4f*)(L + frd + 48));
      float v = (u[0] + u[2]) + (u[1] + u[3]);
      v = fold16(v, v); v += dpp<R8>(v); v += dpp<R4>(v);
      const float o = xo_ + rb(rb(v) * gt);
      __builtin_amdgcn_raw_buffer_store_b16((u16)pk(o, o), mk(out, 1024), ovo, 0, 16);
      if (b == 0 && l == 0) __hip_atomic_store((GAS unsigned*)ws, tag, __ATOMIC_RELAXED, __HIP_MEMORY_SCOPE_AGENT);
    }
    STAMP(10);
    FLUSH();
  } else {
    const int a = w - 1, a3 = a & 3;
    int obo = (n & 3) == 0 ? (n >> 2) * 16384 + gq * 16 : OOB;
    int sbo = (l & 16) ? OOB : (l >> 5) * 16384 + (n >> 3) * 8192 + (n & 7) * 8;
    int obs = WS_OB + g * 65536 + a3 * 4096 + j * 128, sbs = WS_SB + ((j & 1) * 32 + a * 4) * 8192 + (g * 16 + (j >> 1)) * 64;
    int fbs = WS_FB + (kq * 16 + a * 2) * 4096 + (g * 8 + rb_) * 64;
    int sadr = L_S + (a & 1) * 1024 + gq * 256 + (n & 3) * 64 + (a >> 1) * 16 + (n >> 2) * 4, qadr = L_Q + (n & 3) * QSTR + gq * 64 + (n >> 2) * 16;
    int pso = WS_OB + gp * 65536 + (j >> 1) * 4096 + a3 * 1024, pvo = a >= 4 ? l * 16 : OOB;
    int oadr = L_O + n * 80 + gq * 16, owr = L_O + (a3 * 8 + (l >> 3)) * 80 + (l & 7) * 8;
    int mad = L_M + (n & 3) * 4, l8 = l * 8, mrd = L_M + (n >> 2) * 4;
    asm volatile("" : "+v"(mad), "+v"(l8), "+v"(mrd));
    int fso = WS_SB + b * 8192 + a * 1024, fadr = L_V + ((l & 3) * 136 + a * 16 + (l >> 2)) * 4;
    int vwr = L_V + (a * 16 + gq) * VSTR + n * 16;
    int qso = Q32 ? WS_QG + g * 16384 + a3 * 1024 : WS_QG + g * 8192 + a3 * 512, qvo = a >= 4 ? (Q32 ? l * 16 : (l >> 5) * 4096 + (l & 31) * 16) : OOB;
    int mxw = L_MX + gq * 128 + (n & 3) * 32 + a * 4, qwr = L_Q + a3 * QSTR + (Q32 ? l * 4 : (l & 31) * 8 + (l >> 5) * 4);
    int wov = (l >> 5) * 1024 + ((l & 31) ^ ((l >> 5) * 2)) * 16;
    asm volatile("" : "+v"(wov));
    int wos = (gp * 512 + (j >> 1) * 32 + a3 * 8) * 1024 + (j & 1) * 512, w2s = (gp * 224 + rb_ * 28 + a3 * 8) * 1024 + kq * 256, wvo = gq * 1024 + (n ^ (gq * 2)) * 16;
    int w2t = w2s + (a3 == 3 ? 0 : 4096);
    int wl0 = L_W + a3 * 4 * WBLK, wl1 = L_W + (16 + a3 * 2) * WBLK;
    v4u so_ = {(unsigned)(u64)wo, (unsigned)((u64)wo >> 32) & 0xFFFFu, 4096u * 512 * 2, 0x20000u}, s2_ = {(unsigned)(u64)w2, (unsigned)((u64)w2 >> 32) & 0xFFFFu, FF * 512u * 2, 0x20000u};
    asm volatile("" : "+v"(obo), "+v"(sbo), "+v"(sadr), "+v"(qadr), "+v"(oadr), "+v"(owr), "+v"(vwr), "+v"(fadr), "+v"(qvo), "+v"(mxw), "+v"(qwr), "+v"(pvo), "+v"(wvo));
    asm volatile("" : "+s"(obs), "+s"(sbs), "+s"(pso), "+s"(qso), "+s"(fso), "+s"(wos), "+s"(w2s), "+s"(so_), "+s"(s2_), "+s"(wl0), "+s"(wl1), "+s"(w2t));
    const rsrc_t rv = mk(vc, 8u * SK * 256);
    int tadr = L_V + (gq * 4 + (n >> 2)) * VSTR + a3 * 64 + (n & 3) * 8;
    asm volatile("" : "+v"(tadr));
    EPOCH();
    v4u o0 = {0u, tag, 0u, tag}, o1 = o0; u64 lead = 0x000F000F000F000Full;
    asm volatile("" : "+v"(o0), "+v"(o1)); asm volatile("" : "+s"(lead));
    SB();
#define DMA1(VO, SRD, SO) "buffer_load_dwordx4 " VO ", " SRD ", " SO " offen nt lds\n\ts_add_u32 m0, m0, %8\n\ts_add_u32 " SO ", " SO ", 0x800\n\t"
#define WDMA() do { unsigned keep; asm volatile("s_mov_b32 %0, m0\n\ts_mov_b32 m0, %6\n\ts_nop 0\n\t" DMA1("%10", "%4", "%1") DMA1("%10", "%4", "%1") DMA1("%10", "%4", "%1") DMA1("%10", "%4", "%1") \
                   "s_mov_b32 m0, %7\n\ts_nop 0\n\t" DMA1("%3", "%5", "%2") "buffer_load_dwordx4 %3, %5, %9 offen nt lds\n\ts_mov_b32 m0, %0" \
                   : "=&s"(keep), "+s"(wos), "+s"(w2s) : "v"(wvo), "s"(so_), "s"(s2_), "s"(wl0), "s"(wl1), "i"(WBLK), "s"(w2t), "v"(wov) : "memory", "scc"); } while (0)
#define WDO() do { unsigned keep; asm volatile("s_mov_b32 %0, m0\n\ts_mov_b32 m0, %4\n\ts_nop 0\n\t" \
      "buffer_load_dwordx4 %2, %3, %1 offen nt lds\n\ts_add_u32 m0, m0, %5\n\ts_add_u32 %1, %1, 0x800\n\tbuffer_load_dwordx4 %2, %3, %1 offen nt lds\n\ts_add_u32 m0, m0, %5\n\ts_add_u32 %1, %1, 0x800\n\t" \
      "buffer_load_dwordx4 %2, %3, %1 offen nt lds\n\ts_add_u32 m0, m0, %5\n\ts_add_u32 %1, %1, 0x800\n\tbuffer_load_dwordx4 %2, %3, %1 offen nt lds\n\ts_mov_b32 m0, %0" \
      : "=&s"(keep), "+s"(wos) : "v"(wov), "s"(so_), "s"(wl0), "i"(WBLK) : "memory", "scc"); } while (0)
#define WD2() do { unsigned keep; asm volatile("s_mov_b32 %0, m0\n\ts_mov_b32 m0, %4\n\ts_nop 0\n\t" \
      "buffer_load_dwordx4 %2, %3, %1 offen nt lds\n\ts_add_u32 m0, m0, %5\n\ts_nop 0\n\tbuffer_load_dwordx4 %2, %3, %6 offen nt lds\n\ts_mov_b32 m0, %0" \
      : "=&s"(keep) : "s"(w2s), "v"(wvo), "s"(s2_), "s"(wl1), "i"(WBLK), "s"(w2t) : "memory", "scc"); } while (0)
    if (LK == 1) { const rsrc_t rk2 = mk(kc, 8u * SK * 256); RESTK(rk2); }
    if (LK == 2) { const rsrc_t rk2 = mk(kc, 8u * SK * 256); __builtin_amdgcn_s_barrier(); SB();
      if (a >= 4) {
#pragma unroll
        for (int k = KY; k < 4; ++k) kv[k] = ldw(rk2, kof + k * 256, kvo); } else if (VD) __builtin_amdgcn_s_sleep(VD);
      SB(); }
    v4u vv[4];
    WAITV(3 - OT);
    SB();
#pragma unroll
    for (int i = 0; i < YV; ++i) vv[i] = ldw(rv, l16 + i * 1024, kvo);
    SB();
    if (a >= 4) {
      WAITV(3 - YT + YV);
      if (TQ) { while ((unsigned)__builtin_amdgcn_s_memrealtime() - t0 < TQ) __builtin_amdgcn_s_sleep(1); }
      __builtin_amdgcn_s_barrier();
    }
    SB();
    v4u q0, q1, q2, q3;
#define QLD() (q0 = LD16(qvo, qso), q1 = LD16(qvo, qso + 2048))
#define QBAD() (DBAD(q0) | DBAD(q1))
    QLD();
    SB();
    STAMP(9);
    if (a >= 4) __builtin_amdgcn_s_sleep(1);
#pragma unroll
    for (int i = YV; i < OV; ++i) vv[i] = ldw(rv, l16 + i * 1024, kvo);
    SB();
    if (a < 4) { __builtin_amdgcn_s_barrier(); if (OV < 4) __builtin_amdgcn_s_sleep(1); }
    SB();
#pragma unroll
    for (int i = OV; i < 4; ++i) vv[i] = ldw(rv, l16 + i * 1024, kvo);
    SB();
    WAITV(4 - YV);
    STAMP(2);
#define QSUM() do { const v4f u_ = __builtin_bit_cast(v4f, q0 & 0xFFFFFF00u) + __builtin_bit_cast(v4f, q1 & 0xFFFFFF00u); float c_, d_; const float a_ = sw32(u_[0], u_[2], c_), b_ = sw32(u_[1], u_[3], d_); \
        *(LAS unsigned*)(L + qwr) = pk(a_ + c_, b_ + d_); } while (0)
    if (a >= 4) {
      QSUM();
      SB();
      if (__builtin_expect(ANY(QBAD()), 0)) {
        UNTIL(QBAD(), QLD());
        QSUM();
      }
    }
    SB();
    if (a < 4) {
#pragma unroll
      for (int i = 0; i < 4; ++i) *(LAS v4u*)(L + vwr + i * 4 * VSTR) = vv[i];
      if (WP == 0) WDMA();
      if (WS == 5 || WS == 6) WD2();
    }
    const bool wnow = !TW || (unsigned)__builtin_amdgcn_s_memrealtime() - t0 >= TW;
    STAMP(4);
    BAR();
    {
      const v4u qf = *(LAS v4u*)(L + qadr);
      if (WS == 0 && WP == 1 && a < 4 && wnow) WDMA();
      if ((WS == 1 || WS == 3 || WS == 7) && a < 4) WD2();
      if ((WS == 2 || WS == 4) && a < 4) WDO();
      v4f sc[4];
#pragma unroll
      for (int i = 0; i < 4; ++i) { sc[i] = mfma(kv[i], qf, v4f{0.f, 0.f, 0.f, 0.f}); SB(); }
      float t, m;
#define DADD(src, acc, rot, bank) "v_add_f32_dpp %0, " src ", " acc " row_ror:" rot " row_mask:0xf bank_mask:" bank "\n\t"
      asm volatile("s_nop 4\n\t"
                   DADD("%3", "%2", "12", "0x1") DADD("%6", "%7", "4", "0x2") DADD("%10", "%12", "8", "0x4") DADD("%14", "%17", "12", "0x8")
                   DADD("%4", "%0", "8", "0x1") DADD("%8", "%0", "12", "0x2") DADD("%11", "%0", "4", "0x4") DADD("%15", "%0", "8", "0x8")
                   DADD("%5", "%0", "4", "0x1") DADD("%9", "%0", "8", "0x2") DADD("%13", "%0", "12", "0x4") DADD("%16", "%0", "4", "0x8")
                   "v_cvt_pk_bf16_f32 %0, 0, %0\n\tv_mul_f32 %0, 0x3db504f3, %0\n\tv_cvt_pk_bf16_f32 %0, 0, %0\n\tv_mul_f32 %0, 0x3fb8aa3b, %0\n\ts_nop 1\n\t"
                   "v_max_f32_dpp %1, %0, %0 row_ror:8 row_mask:0xf bank_mask:0xf\n\ts_nop 1\n\t"
                   "v_max_f32_dpp %1, %1, %1 row_ror:4 row_mask:0xf bank_mask:0xf\n\t"
                   "v_sub_f32 %0, %0, %1\n\tv_exp_f32 %0, %0\n\t"
                   "s_mov_b64 exec, %21\n\tds_max_f32 %18, %1\n\tds_write_b32 %19, %1\n\ts_mov_b64 exec, -1\n\tds_write_b32 %20, %0"
                   : "=&v"(t), "=&v"(m)
                   : "v"(sc[0][0]), "v"(sc[0][1]), "v"(sc[0][2]), "v"(sc[0][3]), "v"(sc[1][0]), "v"(sc[1][1]), "v"(sc[1][2]), "v"(sc[1][3]),
                     "v"(sc[2][0]), "v"(sc[2][1]), "v"(sc[2][2]), "v"(sc[2][3]), "v"(sc[3][0]), "v"(sc[3][1]), "v"(sc[3][2]), "v"(sc[3][3]),
                     "v"(mad), "v"(mxw), "v"(sadr), "s"(lead)
                   : "memory");
#pragma unroll
      for (int i = 0; i < 5; ++i) __builtin_amdgcn_s_setprio(2);
    }
    v2u vt[16];
    if (a < 4) {
#pragma unroll
      for (int i = 0; i < 8; ++i) vt[(i & 3) + (i >> 2) * 8] = trd(L + tadr + (i & 3) * 16 * VSTR + (i >> 2) * 32);
      if (WP == 2) WDMA();
      if (WS == 6 || WS == 7) WDO();
    }
    STAMP(5);
    BAR();
    if (a < 4) {
      const v2u mm = *(LAS v2u*)(L + L_MX + l8);
      const float m = *(LAS float*)(L + mrd);
      const v4f s0 = *(LAS v4f*)(L + L_S + l16), s1 = *(LAS v4f*)(L + L_S + 1024 + l16);
      const v4f p0 = s0 * ex2(uf(mm[0]) - m), p1 = s1 * ex2(uf(mm[1]) - m);
      const v4u pb = {pk(p0[0], p0[1]), pk(p0[2], p0[3]), pk(p1[0], p1[1]), pk(p1[2], p1[3])};
      const v4f z = {0.f, 0.f, 0.f, 0.f};
      v4f d0 = mfma(cat(vt[0], vt[1]), pb, z), d1 = mfma(cat(vt[8], vt[9]), pb, z);
      const v4u pb1 = dpp4<0x55>(pb), pb2 = dpp4<0xAA>(pb);
      SB();
      d0 = mfma(cat(vt[2], vt[3]), pb1, d0); d1 = mfma(cat(vt[10], vt[11]), pb1, d1);
      v4u pb3 = dpp4<0xFF>(pb);
      asm volatile("" : "+v"(pb3) : "v"(pb1), "v"(vt[0]), "v"(vt[1]), "v"(vt[8]), "v"(vt[9]));
      SB();
      if (WS == 1 || WS == 5) WDO();
      if (WS == 2) WD2();
      STAMP(7);
      BAR();
#pragma unroll
      for (int i = 0; i < 8; ++i) vt[4 + (i & 1) + ((i >> 2) & 1) * 2 + ((i >> 1) & 1) * 8] = trd(L + tadr + (4 + (i & 1) + ((i >> 2) & 1) * 2) * 16 * VSTR + ((i >> 1) & 1) * 32);
      d0 = mfma(cat(vt[4], vt[5]), pb2, d0); d1 = mfma(cat(vt[12], vt[13]), pb2, d1);
      d0 = mfma(cat(vt[6], vt[7]), pb3, d0); d1 = mfma(cat(vt[14], vt[15]), pb3, d1);
      o0[0] = pk(d0[0], d0[1]); o0[2] = pk(d0[2], d0[3]); o1[0] = pk(d1[0], d1[1]); o1[2] = pk(d1[2], d1[3]);
      ST16(o0, obo, obs);
      ST16(o1, obo + 64, obs);
      STAMP(6);
      if (WS == 3) { WDO(); asm volatile("s_waitcnt vmcnt(0)" ::: "memory"); } else if (WS == 4) { WD2(); asm volatile("s_waitcnt vmcnt(0)" ::: "memory"); }
      else if (WP == 3 || !wnow) { if (TW) while ((unsigned)__builtin_amdgcn_s_memrealtime() - t0 < TX) __builtin_amdgcn_s_sleep(1); WDMA(); asm volatile("s_waitcnt vmcnt(0)" ::: "memory"); }
      else asm volatile("s_waitcnt vmcnt(2)" ::: "memory");
      STAMP(1);
    }
    SB();
    if (a >= 4) {
#pragma unroll
      for (int i = 0; i < 4; ++i) *(LAS v4u*)(L + vwr + i * 4 * VSTR) = vv[i];
      STAMP(3);
      BAR();
      __builtin_amdgcn_s_sleep(PD);
    }
    SB();
    v4u pv = LD16(pvo, pso);
    BAR();
    if (a >= 4 && ANY(TBAD(pv))) pv = LD16(pvo, pso);
    SB();
    v2u wt[4];
    const int wrd = L_W + gq * 2 * WBLK + (n >> 2) * 256 + (a ^ (n >> 2)) * 32 + (n & 3) * 8;
#pragma unroll
    for (int i = 0; i < 4; ++i) wt[i] = trd(L + L_W + (gq * 4 + (i & 1) * 2 + (n >> 3)) * WBLK + ((n >> 2) & 1) * 512 + ((a * 2 + (i >> 1)) ^ ((n >> 2) & 1)) * 32 + (n & 3) * 8);
    SB();
    WAITV(0);
    if (a < 4) {
      const v4u hf = *(LAS v4u*)(L + L_H + gq * 16);
      LAS unsigned char* p = L + wrd + 16 * WBLK;
      const v4f z = {0.f, 0.f, 0.f, 0.f};
      const v4f f0 = mfma(hf, cat(trd(p), trd(p + WBLK)), z), f1 = mfma(hf, cat(trd(p + 128), trd(p + 128 + WBLK)), z);
      ST8((v2u{__float_as_uint(gq ? f1[0] : f0[0]), tag}), l < 32 ? gq * 32768 + (n >> 3) * 4096 + (n & 7) * 8 : OOB, fbs);
    }
    SB();
    if (a >= 4) {
#define OWR() (((LAS unsigned*)(L + owr))[0] = pv[0], ((LAS unsigned*)(L + owr))[1] = pv[2])
      OWR();
      UNTIL(TBAD(pv), (pv = LD16(pvo, pso), OWR()));
      STAMP(8);
    }
    BAR();
    {
      const v4u of0 = *(LAS v4u*)(L + oadr), of1 = *(LAS v4u*)(L + oadr + 16 * 80);
      const v4f w0 = *(LAS v4f*)(L + L_WG + gq * 16), w1_ = *(LAS v4f*)(L + L_WG + 64 + gq * 16);
      SB();
      const v4f z = {0.f, 0.f, 0.f, 0.f};
      const v4f d00 = mfma(of0, cat(wt[0], wt[1]), z), d01 = mfma(of0, cat(wt[2], wt[3]), z), d10 = mfma(of1, cat(wt[0], wt[1]), z), d11 = mfma(of1, cat(wt[2], wt[3]), z);
      SB();
      v2f c0 = d00.hi * w0.hi + d00.lo * w0.lo, c1 = d01.hi * w0.hi + d01.lo * w0.lo;
      c0 = d10.hi * w1_.hi + (d10.lo * w1_.lo + c0); c1 = d11.hi * w1_.hi + (d11.lo * w1_.lo + c1);
      float e = fold32(c0[0] + c0[1], c1[0] + c1[1]);
      e = fold16(e, e);
      ST8((v2u{__float_as_uint(e), tag}), sbo, sbs);
    }
    STAMP(10);
    if (b < 64) {
      __builtin_amdgcn_s_sleep(10); if (a == g) __builtin_amdgcn_s_sleep(10);
      v4u sv = LD16(l16, fso);
#define SWR() (((LAS unsigned*)(L + fadr))[0] = sv[0], ((LAS unsigned*)(L + fadr))[576] = sv[2])
      SWR();
      UNTIL(TBAD(sv), (sv = LD16(l16, fso), SWR()));
      BAR();
      if (a == 0) { const unsigned e_ = (unsigned)__builtin_amdgcn_s_memrealtime(), d_ = 4 * (e_ - c0_); while ((unsigned)__builtin_amdgcn_s_memrealtime() - e_ < d_ && d_ < 100000u) {} }
    }
  }
}

extern "C" void kernel_launch(void* const* d_in, const int* in_sizes, int n_in, void* d_out, int out_size, void* d_ws, size_t ws_size, hipStream_t stream) {
  fused_decode<<<256, NTHR, 0, stream>>>((const u16*)d_in[0], (const u16*)d_in[3], (const u16*)d_in[6], (const u16*)d_in[2], (unsigned char*)d_ws, (const u16*)d_in[1],
                                         (const u16*)d_in[4], (const u16*)d_in[8], (const u16*)d_in[7], (const u16*)d_in[9], (u16*)d_out);
}
```

```cpp
#define LK 1
#define KY 2
#define YT 2
#define PD 12
#define XQ 1
#define XO 1
#define WS 1
#define MLD 8
#define PFA 2
#define ASL 8
#include <hip/hip_runtime.h>
#include <hip/hip_bf16.h>

typedef unsigned short u16;
typedef unsigned long long u64;
typedef unsigned v4u __attribute__((ext_vector_type(4)));
typedef unsigned v2u __attribute__((ext_vector_type(2)));
typedef float v4f __attribute__((ext_vector_type(4)));
typedef short s16x4 __attribute__((ext_vector_type(4)));
typedef __bf16 bf16x2 __attribute__((ext_vector_type(2)));
typedef __bf16 bf16x8 __attribute__((ext_vector_type(8)));
typedef __amdgpu_buffer_rsrc_t rsrc_t;
#define GAS __attribute__((address_space(1)))
#define CAS __attribute__((address_space(4)))
#define LAS __attribute__((address_space(3)))

#define TQ 0
#define YV 0
#define OV 4
#define OT 0
#define EV 1
#define TW 0
#define TX TW
#define Q32 0
#define PFQ 1
#define SH 0
#define FORCE -1
#define VD 2
#define WP 1
#define STAMP(i) do { if ((i) == 3) { SB(); td_ = (unsigned)__builtin_amdgcn_s_memrealtime(); SB(); } } while (0)
#define FLUSH() do { if (w == 0 && td_) { const unsigned d_ = 10 * (td_ - c0_); while ((unsigned)__builtin_amdgcn_s_memrealtime() - c0_ < d_ && d_ < 100000u) __builtin_amdgcn_s_sleep(4); } } while (0)
constexpr int SK = 32768, WIN = 4096, FF = 1792;
constexpr int NTHR = 576;
constexpr unsigned WS_QG = 4096;
constexpr unsigned WS_HG = WS_QG + 131072;
constexpr unsigned WS_ML = WS_HG + 57344;
constexpr unsigned WS_OB = WS_ML + 16384;
constexpr unsigned WS_SB = WS_OB + 524288;
constexpr unsigned WS_FB = WS_SB + 524288;
constexpr unsigned WS_END = WS_FB + 262144;
constexpr int VSTR = 288, L_V = 0, L_O = 128 * VSTR, L_Q = L_O + 4096, QSTR = 320, L_S = L_Q + 4 * QSTR, L_XN = (L_S + 2048 + 1023) / 1024 * 1024, L_MX = L_XN + 1024, L_M = L_MX + 512, L_H = L_M + 32,
              L_WG = L_H + 64, L_W = L_WG + 128, WBLK = 1088, L_END = L_W + 24 * WBLK;
static_assert(L_O % 128 == 0, "the o image is addressed with ^ 32 and ^ 64");
constexpr unsigned SPIN_CAP = 1u << 18;
constexpr float LOG2E = 1.4426950408889634f;
constexpr int OOB = 0x40000000;

__device__ __forceinline__ rsrc_t mk(const void* p, unsigned bytes) { return __builtin_amdgcn_make_buffer_rsrc((void*)p, 0, bytes, 0x00020000); }
__device__ __forceinline__ v4u ldw(rsrc_t r, int voff, int soff) { return __builtin_amdgcn_raw_buffer_load_b128(r, voff, soff, 2); }
__device__ __forceinline__ float uf(unsigned u) { return __uint_as_float(u); }
__device__ __forceinline__ float bflo(unsigned u) { return __uint_as_float(u << 16); }
__device__ __forceinline__ float bfhi(unsigned u) { return __uint_as_float(u & 0xFFFF0000u); }
__device__ __forceinline__ unsigned pk(float lo, float hi) { bf16x2 r = {(__bf16)lo, (__bf16)hi}; return __builtin_bit_cast(unsigned, r); }
typedef float v2f __attribute__((ext_vector_type(2)));
__device__ __forceinline__ float rb(float f) { return __builtin_bit_cast(float, __builtin_convertvector((v2f{0.f, f}), bf16x2)); }
__device__ __forceinline__ v4f mfma(v4u a, v4u b, v4f c) { return __builtin_amdgcn_mfma_f32_16x16x32_bf16(__builtin_bit_cast(bf16x8, a), __builtin_bit_cast(bf16x8, b), c, 0, 0, 0); }
template <int CTRL> __device__ __forceinline__ unsigned dppu(unsigned x) { return (unsigned)__builtin_amdgcn_mov_dpp((int)x, CTRL, 0xf, 0xf, true); }
template <int CTRL> __device__ __forceinline__ float dpp(float x) { return __uint_as_float(dppu<CTRL>(__float_as_uint(x))); }
template <int CTRL> __device__ __forceinline__ v4u dpp4(v4u x) { return v4u{dppu<CTRL>(x[0]), dppu<CTRL>(x[1]), dppu<CTRL>(x[2]), dppu<CTRL>(x[3])}; }
constexpr int X1 = 0xB1, X2 = 0x4E, HM = 0x141, R4 = 0x124, R8 = 0x128;
__device__ __forceinline__ float sw16(float a, float b, float& o) { auto r = __builtin_amdgcn_permlane16_swap(__float_as_uint(a), __float_as_uint(b), false, false); o = uf(r[1]); return uf(r[0]); }
__device__ __forceinline__ float sw32(float a, float b, float& o) { auto r = __builtin_amdgcn_permlane32_swap(__float_as_uint(a), __float_as_uint(b), false, false); o = uf(r[1]); return uf(r[0]); }
__device__ __forceinline__ float fold16(float a, float b) { float o; const float p = sw16(a, b, o); return p + o; }
__device__ __forceinline__ float fold32(float a, float b) { float o; const float p = sw32(a, b, o); return p + o; }
__device__ __forceinline__ float fmx(float a, float b) { return __builtin_elementwise_maximum(a, b); }
__device__ __forceinline__ float max16(float a) { float o; const float p = sw16(a, a, o); return fmx(p, o); }
#define MAXDPP(name, ctl) __device__ __forceinline__ float name(float m) { float r; asm("s_nop 1\n\tv_max_f32_dpp %0, %1, %1 " ctl " row_mask:0xf bank_mask:0xf" : "=v"(r) : "v"(m)); return r; }
MAXDPP(mx_r8, "row_ror:8") MAXDPP(mx_r4, "row_ror:4") MAXDPP(mx_x1, "quad_perm:[1,0,3,2]") MAXDPP(mx_x2, "quad_perm:[2,3,0,1]")
__device__ __forceinline__ float sum64(float v) { v += dpp<X1>(v); v += dpp<X2>(v); v += dpp<HM>(v); v += dpp<R8>(v); v = fold16(v, v); return fold32(v, v); }
__device__ __forceinline__ v2u trd(LAS unsigned char* p) { return __builtin_bit_cast(v2u, __builtin_amdgcn_ds_read_tr16_b64_v4i16((LAS s16x4*)p)); }
__device__ __forceinline__ v4u cat(v2u a, v2u b) { return v4u{a[0], a[1], b[0], b[1]}; }
__device__ __forceinline__ float ex2(float x) { return __builtin_amdgcn_exp2f(x); }
__device__ __forceinline__ float rcp(float x) { return __builtin_amdgcn_rcpf(x); }
__device__ __forceinline__ float gval(unsigned d) { return __uint_as_float(d & 0xFFFFFF00u); }
__device__ __forceinline__ unsigned d4(float f, unsigned tagbyte) { return ((__float_as_uint(f) + 0x80u) & 0xFFFFFF00u) | tagbyte; }
#define SB() __builtin_amdgcn_sched_barrier(0)
#define BAR() do { asm volatile("s_waitcnt lgkmcnt(0)" ::: "memory"); __builtin_amdgcn_s_barrier(); asm volatile("" ::: "memory"); } while (0)
#define FENCE() asm volatile("" ::: "memory")
#define WAITV(n) __builtin_amdgcn_s_waitcnt(0x0F70 | (n))
#define LD16(off, soff) __builtin_amdgcn_raw_buffer_load_b128(rs, (off), (soff), 16)
#define ST16(v, off, soff) __builtin_amdgcn_raw_buffer_store_b128((v), rs, (off), (soff), 16)
#define ST8(v, off, soff) __builtin_amdgcn_raw_buffer_store_b64((v), rs, (off), (soff), 16)
#define TBAD(v) __builtin_amdgcn_bitop3_b32((v)[1], (v)[3], tag, 0x7E)
#define ANY(t) (__builtin_amdgcn_ballot_w64((t) != 0) != 0)
#define DBAD(v) __builtin_amdgcn_bitop3_b32(__builtin_amdgcn_perm((v)[1], (v)[0], 0x0c0c0400u), __builtin_amdgcn_perm((v)[3], (v)[2], 0x04000c0cu), tag, 0x56)
#define EPOCH() asm volatile("" : "+s"(ep0)); const unsigned tag = ((ep0 & 0x7F7F7F7Fu) + 0x01010101u) ^ (ep0 & 0x80808080u)
#define UNTIL(BADEXPR, RELOAD) do { if (ANY(BADEXPR)) { int spins_ = SPIN_CAP; _Pragma("nounroll") do { FENCE(); RELOAD; if (!ANY(BADEXPR)) break; asm volatile("" : "+s"(spins_)); } while (--spins_); } } while (0)

__device__ __forceinline__ void mksel(unsigned (&sel)[8], int l) {
#pragma unroll
  for (int d = 0; d < 4; ++d) { const bool on = ((l & 7) >> 1) == d;
    sel[d] = on ? ((l & 1) ? 0x01000c0cu : 0x0c0c0100u) : 0x0c0c0c0cu; sel[4 + d] = on ? ((l & 1) ? 0x03020c0cu : 0x0c0c0302u) : 0x0c0c0c0cu; }
}
__device__ __forceinline__ v4u diag(unsigned pair, const unsigned (&sel)[8], int hi) {
  return v4u{__builtin_amdgcn_perm(0u, pair, sel[4 * hi]), __builtin_amdgcn_perm(0u, pair, sel[4 * hi + 1]), __builtin_amdgcn_perm(0u, pair, sel[4 * hi + 2]), __builtin_amdgcn_perm(0u, pair, sel[4 * hi + 3])};
}
__device__ __forceinline__ float upr8(float v) { return __builtin_bit_cast(float, __builtin_amdgcn_update_dpp(__builtin_bit_cast(int, v), __builtin_bit_cast(int, v), R8, 0xC, 0xF, false)); }
__device__ __forceinline__ v2u diag_out(v4f d, unsigned tb0, unsigned tb1) {
  const float t0 = upr8(d[0]), t1 = upr8(d[1]), t2 = upr8(d[2]), t3 = upr8(d[3]);
  return v2u{d4(fold32(t0, t2), tb0), d4(fold32(t1, t3), tb1)};
}

__global__ void __launch_bounds__(NTHR, 3) fused_decode(const u16* __restrict__ x, const u16* __restrict__ wq, const u16* __restrict__ w1, const u16* __restrict__ vc, unsigned char* ws,
                                                        const u16* __restrict__ kc, const u16* __restrict__ wo, const u16* __restrict__ w2, const u16* __restrict__ b1,
                                                        const u16* __restrict__ b2, u16* __restrict__ out) {
  __shared__ __attribute__((aligned(128))) unsigned char lds_[L_END];
  LAS unsigned char* L = (LAS unsigned char*)lds_;
  const int b = blockIdx.x, g = b & 7, j = b >> 3, l = threadIdx.x & 63;
  int w = __builtin_amdgcn_readfirstlane(threadIdx.x >> 6);
  const unsigned t0 = (TQ | TW) ? (unsigned)__builtin_amdgcn_s_memrealtime() : 0u;
  unsigned c0_, td_ = 0; { u64 t_; asm volatile("s_memrealtime %0\n s_waitcnt lgkmcnt(0)" : "=s"(t_) :: "memory"); c0_ = (unsigned)t_; }
  { u64 pc; unsigned keep, t_;
    asm volatile("s_cmp_lg_u32 %3, 4\n\ts_cbranch_scc1 1f\n\ts_getpc_b64 %0\n\ts_mov_b32 %1, m0\n\ts_mov_b32 m0, %5\n\tv_lshlrev_b32 %2, 7, %4\n\t"
                 "global_load_lds_dword %2, %0\n\ts_mov_b32 m0, %1\n1:"
                 : "=&s"(pc), "=&s"(keep), "=&v"(t_) : "s"(w), "v"(PFA == 1 ? l + 1 : PFA == 2 ? ((l < 16) ? 8 + l : min((j * 5 + (l & 7) % 5) % 80, 64)) : min((j * 5 + (l & 7) % 5) % 80, SH ? 70 : 64)), "i"(L_O) : "scc", "memory"); }
  const int kq = j & 3, rb_ = j >> 2, gn = (g + XQ) & 7, gp = (g - XO) & 7;
  const int gq = l >> 4, n = l & 15;
  const bool hasw1 = rb_ != 3; const int jw = rb_ == 7 ? j - 16 : j;
  int kvo = (g * SK + (SK - WIN) + j * 128 + (((w - 1) * 2 + ((w - 1) >> 2) + j) & 7) * 16) * 256, l16 = l * 16, kof = (n >> 2) * 1024 + gq * 64 + (n & 3) * 16;
  int vo = (l >> 3) * (16 * 8192) + (l & 7) * 16, so = kq * (128 * 8192) + gn * 1024 + rb_ * 128;
  const bool early = !SH || (FORCE >= 0 ? FORCE == 1 : (__builtin_amdgcn_s_getreg(20 | (3 << 11)) & 4) == 0);
  int vo1 = (hasw1 && early) ? (l >> 3) * (8 * FF * 2) + (l & 7) * 16 : OOB, so1 = g * (64 * FF * 2) + jw * 128;
  asm volatile("" : "+s"(kvo), "+s"(so), "+s"(so1));
  asm volatile("s_setprio 3" : "+v"(l16), "+v"(vo), "+v"(vo1), "+v"(kof));
  asm volatile("" :: "s"(x), "s"(kc), "s"(vc), "s"(wq), "s"(wo), "s"(w1), "s"(b1), "s"(w2), "s"(b2), "s"(out), "s"(ws));
  unsigned ep0 = 0; if (!EV) ep0 = *(const CAS unsigned*)ws;
  const rsrc_t rs = mk(ws, WS_END);
  v4u kv[4];
  if (w != 0) {
    const rsrc_t rk = mk(kc, 8u * SK * 256);
    if (PFQ && w == 3) {
      unsigned keep;
      const unsigned po = PFQ == 1 ? (j * 16 + 8 + (l & 7)) * 8192 + gn * 1024 + 384 + ((l >> 3) & 1) * 512
                        : PFQ == 2 ? (j * 16 + (l & 15)) * 8192 + gn * 1024 + 384 + ((l >> 4) & 1) * 512
                        : PFQ == 3 ? (j * 16 + 4 + (l & 15) % 12) * 8192 + gn * 1024 + 384
                        : (j * 16 + 8 + (l & 7)) * 8192 + gn * 1024 + 128 + ((l >> 3) & 3) * 256;
      asm volatile("s_mov_b32 %0, m0\n\ts_mov_b32 m0, %3\n\ts_nop 0\n\tglobal_load_lds_dword %1, %2 nt\n\ts_mov_b32 m0, %0" : "=&s"(keep) : "v"(po), "s"(wq), "i"(L_O + 256) : "memory");
    }
    if (EV) ep0 = *(const CAS unsigned*)ws;
    __builtin_amdgcn_s_setprio(2);
    __builtin_amdgcn_s_barrier();
    SB();
#pragma unroll
    for (int k = 0; k < KY; ++k) kv[k] = ldw(rk, kof + k * 256, kvo);
    SB();
#define RESTK(RK) do { \
    if (KY < 4 && w > 4) __builtin_amdgcn_s_barrier(); \
    SB(); \
    _Pragma("unroll") \
    for (int k = KY; k < 4; ++k) kv[k] = ldw(RK, kof + k * 256, kvo); \
    SB(); \
    if (KY < 4 && w <= 4) __builtin_amdgcn_s_barrier(); \
 } while (0)
    if (ASL) __builtin_amdgcn_s_sleep(ASL);
    if (!LK) RESTK(rk);
    if (LK == 2 && w <= 4) {
#pragma unroll
      for (int k = KY; k < 4; ++k) kv[k] = ldw(rk, kof + k * 256, kvo); }
  }
  asm volatile("" : "+s"(w));
  if (__builtin_expect(w == 0, 1)) {
    v4u r16[16], f8[8];
    const v4u xv = *(const GAS v4u*)((const GAS unsigned char*)x + (unsigned)l16);
    const unsigned epv = EV ? __builtin_amdgcn_raw_buffer_load_b32(rs, 0, 0, 0) : 0u;
    { const rsrc_t r = mk(wq, 512 * 4096 * 2), r1 = mk(w1, 512 * FF * 2);
#pragma unroll
      for (int i = 0; i < 16; ++i) r16[i] = ldw(r, vo, so + i * 8192);
#pragma unroll
      for (int i = 0; i < 8; ++i) f8[i] = ldw(r1, vo1, so1 + i * (FF * 2)); }
    SB();
    __builtin_amdgcn_s_barrier();
    SB();
    unsigned sel[8]; mksel(sel, l);
    int gvo = (l & 8) ? OOB : (l & 7) * 32 + (l & 16) + (l >> 5) * 8, xra = L_XN + kq * 256 + (l >> 3) * 32, xrc = L_XN + g * 128 + (l >> 3) * 16;
    asm volatile("" : "+v"(sel[0]), "+v"(sel[1]), "+v"(sel[2]), "+v"(sel[3]), "+v"(sel[4]), "+v"(sel[5]), "+v"(sel[6]), "+v"(sel[7]), "+v"(gvo), "+v"(xra), "+v"(xrc));
    SB();
    float xf[8];
#pragma unroll
    for (int i = 0; i < 4; ++i) { xf[2 * i] = bflo(xv[i]); xf[2 * i + 1] = bfhi(xv[i]); }
    const float mean = sum64(((xf[0] + xf[1]) + (xf[2] + xf[3])) + ((xf[4] + xf[5]) + (xf[6] + xf[7]))) * (1.f / 512);
    float vs = 0.f;
#pragma unroll
    for (int i = 0; i < 8; ++i) { const float d = xf[i] - mean; vs = fmaf(d, d, vs); }
    const float var = sum64(vs) * (1.f / 512);
    const float mb = rb(mean), rsd = rcp(rb(__builtin_amdgcn_sqrtf(rb(rb(var) + 1e-5f))));
    v4u xn;
#pragma unroll
    for (int i = 0; i < 4; ++i) { const unsigned d = pk(xf[2 * i] - mb, xf[2 * i + 1] - mb); xn[i] = pk(bflo(d) * rsd, bfhi(d) * rsd); }
    *(LAS v4u*)(L + L_XN + l * 16) = xn;
    *(LAS float*)(L + L_M + (l & 3) * 4) = -__builtin_inff();
    const v4u xa = *(LAS v4u*)(L + xra), xb = *(LAS v4u*)(L + xra + 16), xc = *(LAS v4u*)(L + xrc);
    if (EV) ep0 = __builtin_amdgcn_readfirstlane(epv);
    EPOCH();
    const unsigned tb0 = (tag >> ((l >> 5) * 16)) & 0xFFu, tb1 = (tag >> ((l >> 5) * 16 + 8)) & 0xFFu;
    { v4u bx[8];
#pragma unroll
      for (int i = 0; i < 8; ++i) bx[i] = diag(xa[i >> 1], sel, i & 1);
      SB();
      v4f d = {0.f, 0.f, 0.f, 0.f};
#pragma unroll
      for (int i = 0; i < 8; ++i) { d = mfma(bx[i], r16[i], d); bx[i] = diag(xb[i >> 1], sel, i & 1); SB(); }
#pragma unroll
      for (int i = 0; i < 8; ++i) d = mfma(bx[i], r16[8 + i], d);
      ST8(diag_out(d, tb0, tb1), gvo, WS_QG + gn * 8192 + kq * 2048 + rb_ * 256);
      STAMP(3);
      SB();
      if (SH) { const rsrc_t r1 = mk(w1, 512 * FF * 2);
#pragma unroll
        for (int i = 0; i < 8; ++i) r16[i] = ldw(r1, vo1, so1 + ((g ^ 4) - g) * (64 * FF * 2) + i * (FF * 2)); }
      SB();
      if (KY < 4) __builtin_amdgcn_s_barrier();
      SB();
    }
    { v4f d = {0.f, 0.f, 0.f, 0.f};
#pragma unroll
      for (int i = 0; i < 8; ++i) d = mfma(diag(xc[i >> 1], sel, i & 1), f8[i], d);
      ST8(diag_out(d, tb0, tb1), (hasw1 && early) ? gvo : OOB, WS_HG + g * (FF * 4) + jw * 256); }
    const int hr0 = gp * 224 + rb_ * 28, hu = min(l & 7, 6), ocol = (b & 63) * 8 + (l & 3) * 2 + (l >> 5);
    int mlo = (l & 51) == 0 ? (n >> 2) * 512 : OOB, mls = WS_ML + g * 2048 + j * 16, hvo = (l >> 3) * (FF * 4) + hu * 16, hso = WS_HG + hr0 * 4;
    int pvo = (l & 31) * 16, pso = WS_ML + gp * 2048 + (j >> 3) * 512;
    asm volatile("" : "+v"(mlo), "+v"(hvo), "+v"(pvo));
    asm volatile("" : "+s"(mls), "+s"(hso), "+s"(pso));
    const float b1v = bflo(b1[hr0 + hu * 4 + gq]), b2v = bflo(b2[ocol]), xo = bflo(x[ocol]);
    if (SH) { const v4u xd = *(LAS v4u*)(L + (xrc ^ 512)); v4f d = {0.f, 0.f, 0.f, 0.f};
#pragma unroll
      for (int i = 0; i < 8; ++i) d = mfma(diag(xd[i >> 1], sel, i & 1), r16[i], d);
      ST8(diag_out(d, tb0, tb1), (hasw1 && early) ? gvo : OOB, WS_HG + (g ^ 4) * (FF * 4) + jw * 256); }
    BAR();
    BAR();
    v4u hg; if (!SH) hg = LD16(hvo, hso);
    SB();
    BAR();
    { const v2u mm = *(LAS v2u*)(L + L_MX + l * 8);
      const v4f s0 = *(LAS v4f*)(L + L_S + l * 16), s1 = *(LAS v4f*)(L + L_S + 1024 + l * 16);
      const float m = *(LAS float*)(L + L_M + (n >> 2) * 4);
      float ls = fmaf((s0[0] + s0[1]) + (s0[2] + s0[3]), ex2(uf(mm[0]) - m), ((s1[0] + s1[1]) + (s1[2] + s1[3])) * ex2(uf(mm[1]) - m));
      ls += dpp<X1>(ls); ls += dpp<X2>(ls); ls = fold16(ls, ls); ls = fold32(ls, ls);
      ST16((v4u{__float_as_uint(m), tag, __float_as_uint(ls), tag}), mlo, mls);
    }
    STAMP(6);
    v4u own;
    if (SH) { own = LD16(n * 16, WS_HG + g * (FF * 4) + jw * 256); hg = LD16(hvo, hso); SB(); }
    BAR();
    if (SH && __builtin_expect(hasw1 && !early && ANY(DBAD(own)), 0)) { int tries = 32;
      _Pragma("nounroll") do { FENCE(); own = LD16(n * 16, WS_HG + g * (FF * 4) + jw * 256); } while (ANY(DBAD(own)) && --tries);
      if (!tries) {
      const rsrc_t r1 = mk(w1, 512 * FF * 2); v4f d = {0.f, 0.f, 0.f, 0.f};
#pragma unroll
      for (int i = 0; i < 8; ++i) f8[i] = ldw(r1, (l >> 3) * (8 * FF * 2) + (l & 7) * 16, so1 + i * (FF * 2));
#pragma unroll
      for (int i = 0; i < 8; ++i) d = mfma(diag(xc[i >> 1], sel, i & 1), f8[i], d);
      ST8(diag_out(d, tb0, tb1), gvo, WS_HG + g * (FF * 4) + jw * 256);
    } }
    {
      if (__builtin_expect(ANY(DBAD(hg)), 0)) UNTIL(DBAD(hg), hg = LD16(hvo, hso));
      float hs = fold16(fold32(gval(hg[0]), gval(hg[2])), fold32(gval(hg[1]), gval(hg[3]))); hs += dpp<R8>(hs);
      const float hp = rb(rb(hs) + b1v);
      const float u2 = 1.5957691216057308f * fmaf(0.044715f * hp * hp, hp, hp);
      const float ge = hp * rcp(1.f + ex2(-u2 * LOG2E));
      *(LAS u16*)(L + L_H + (l & 7) * 8 + gq * 2) = ((l & 7) < 7) ? (u16)pk(ge, ge) : (u16)0;
    }
    __builtin_amdgcn_s_sleep(MLD);
    v4u pv = LD16(pvo, pso);
    SB();
    BAR();
    {
      UNTIL(TBAD(pv), pv = LD16(pvo, pso));
      const float m = uf(pv[0]);
      const float M = max16(mx_r4(mx_r8(mx_x2(mx_x1(m)))));
      const float e = ex2(m - M);
      float ls = e * uf(pv[2]);
      ls += dpp<X1>(ls); ls += dpp<X2>(ls); ls += dpp<HM>(ls); ls += dpp<R8>(ls); ls = fold16(ls, ls);
      *(LAS float*)(L + L_WG + (l & 31) * 4) = e * rcp(ls);
    }
    STAMP(8);
    BAR();
    if (b < 64) {
      const int fo = WS_FB + b * 4096;
      v4u f0 = LD16(l16, fo), f1 = LD16(l16 + 1024, fo), f2 = LD16(l16 + 2048, fo), f3 = LD16(l16 + 3072, fo);
      UNTIL(TBAD(f0) | TBAD(f1) | TBAD(f2) | TBAD(f3), (f0 = LD16(l16, fo), f1 = LD16(l16 + 1024, fo), f2 = LD16(l16 + 2048, fo), f3 = LD16(l16 + 3072, fo)));
      float t = fold32((uf(f0[0]) + uf(f1[0])) + (uf(f2[0]) + uf(f3[0])), (uf(f0[2]) + uf(f1[2])) + (uf(f2[2]) + uf(f3[2])));
      t = fold16(t, t); t += dpp<R8>(t); t += dpp<R4>(t);
      const float f = rb(rb(t) + b2v);
      float gt = rb(f * rcp(1.f + ex2(-f * LOG2E)));
      int frd = L_V + ((l >> 5) * 576 + (l & 3) * 136 + ((l >> 2) & 7) * 16) * 4;
      int ovo = (l & 28) ? OOB : ocol * 2; float xo_ = xo;
      asm volatile("" : "+v"(gt), "+v"(frd), "+v"(ovo), "+v"(xo_));
      BAR();
      const v4f u0 = *(LAS v4f*)(L + frd), u1 = *(LAS v4f*)(L + frd + 16);
      const v4f u = (u0 + u1) + (*(LAS v4f*)(L + frd + 32) + *(LAS v4f*)(L + frd + 48));
      float v = (u[0] + u[2]) + (u[1] + u[3]);
      v = fold16(v, v); v += dpp<R8>(v); v += dpp<R4>(v);
      const float o = xo_ + rb(rb(v) * gt);
      __builtin_amdgcn_raw_buffer_store_b16((u16)pk(o, o), mk(out, 1024), ovo, 0, 16);
      if (b == 0 && l == 0) __hip_atomic_store((GAS unsigned*)ws, tag, __ATOMIC_RELAXED, __HIP_MEMORY_SCOPE_AGENT);
    }
    STAMP(10);
    FLUSH();
  } else {
    const int a = w - 1, a3 = a & 3;
    int obo = (n & 3) == 0 ? (n >> 2) * 16384 + gq * 16 : OOB;
    int sbo = (l & 16) ? OOB : (l >> 5) * 16384 + (n >> 3) * 8192 + (n & 7) * 8;
    int obs = WS_OB + g * 65536 + a3 * 4096 + j * 128, sbs = WS_SB + ((j & 1) * 32 + a * 4) * 8192 + (g * 16 + (j >> 1)) * 64;
    int fbs = WS_FB + (kq * 16 + a * 2) * 4096 + (g * 8 + rb_) * 64;
    int sadr = L_S + (a & 1) * 1024 + gq * 256 + (n & 3) * 64 + (a >> 1) * 16 + (n >> 2) * 4, qadr = L_Q + (n & 3) * QSTR + gq * 64 + (n >> 2) * 16;
    int pso = WS_OB + gp * 65536 + (j >> 1) * 4096 + a3 * 1024, pvo = a >= 4 ? l * 16 : OOB;
    int oadr = L_O + n * 80 + gq * 16, owr = L_O + (a3 * 8 + (l >> 3)) * 80 + (l & 7) * 8;
    int mad = L_M + (n & 3) * 4, l8 = l * 8, mrd = L_M + (n >> 2) * 4;
    asm volatile("" : "+v"(mad), "+v"(l8), "+v"(mrd));
    int fso = WS_SB + b * 8192 + a * 1024, fadr = L_V + ((l & 3) * 136 + a * 16 + (l >> 2)) * 4;
    int vwr = L_V + (a * 16 + gq) * VSTR + n * 16;
    int qso = Q32 ? WS_QG + g * 16384 + a3 * 1024 : WS_QG + g * 8192 + a3 * 512, qvo = a >= 4 ? (Q32 ? l * 16 : (l >> 5) * 4096 + (l & 31) * 16) : OOB;
    int mxw = L_MX + gq * 128 + (n & 3) * 32 + a * 4, qwr = L_Q + a3 * QSTR + (Q32 ? l * 4 : (l & 31) * 8 + (l >> 5) * 4);
    int wov = (l >> 5) * 1024 + ((l & 31) ^ ((l >> 5) * 2)) * 16;
    asm volatile("" : "+v"(wov));
    int wos = (gp * 512 + (j >> 1) * 32 + a3 * 8) * 1024 + (j & 1) * 512, w2s = (gp * 224 + rb_ * 28 + a3 * 8) * 1024 + kq * 256, wvo = gq * 1024 + (n ^ (gq * 2)) * 16;
    int w2t = w2s + (a3 == 3 ? 0 : 4096);
    int wl0 = L_W + a3 * 4 * WBLK, wl1 = L_W + (16 + a3 * 2) * WBLK;
    v4u so_ = {(unsigned)(u64)wo, (unsigned)((u64)wo >> 32) & 0xFFFFu, 4096u * 512 * 2, 0x20000u}, s2_ = {(unsigned)(u64)w2, (unsigned)((u64)w2 >> 32) & 0xFFFFu, FF * 512u * 2, 0x20000u};
    asm volatile("" : "+v"(obo), "+v"(sbo), "+v"(sadr), "+v"(qadr), "+v"(oadr), "+v"(owr), "+v"(vwr), "+v"(fadr), "+v"(qvo), "+v"(mxw), "+v"(qwr), "+v"(pvo), "+v"(wvo));
    asm volatile("" : "+s"(obs), "+s"(sbs), "+s"(pso), "+s"(qso), "+s"(fso), "+s"(wos), "+s"(w2s), "+s"(so_), "+s"(s2_), "+s"(wl0), "+s"(wl1), "+s"(w2t));
    const rsrc_t rv = mk(vc, 8u * SK * 256);
    int tadr = L_V + (gq * 4 + (n >> 2)) * VSTR + a3 * 64 + (n & 3) * 8;
    asm volatile("" : "+v"(tadr));
    EPOCH();
    v4u o0 = {0u, tag, 0u, tag}, o1 = o0; u64 lead = 0x000F000F000F000Full;
    asm volatile("" : "+v"(o0), "+v"(o1)); asm volatile("" : "+s"(lead));
    SB();
#define DMA1(VO, SRD, SO) "buffer_load_dwordx4 " VO ", " SRD ", " SO " offen nt lds\n\ts_add_u32 m0, m0, %8\n\ts_add_u32 " SO ", " SO ", 0x800\n\t"
#define WDMA() do { unsigned keep; asm volatile("s_mov_b32 %0, m0\n\ts_mov_b32 m0, %6\n\ts_nop 0\n\t" DMA1("%10", "%4", "%1") DMA1("%10", "%4", "%1") DMA1("%10", "%4", "%1") DMA1("%10", "%4", "%1") \
                   "s_mov_b32 m0, %7\n\ts_nop 0\n\t" DMA1("%3", "%5", "%2") "buffer_load_dwordx4 %3, %5, %9 offen nt lds\n\ts_mov_b32 m0, %0" \
                   : "=&s"(keep), "+s"(wos), "+s"(w2s) : "v"(wvo), "s"(so_), "s"(s2_), "s"(wl0), "s"(wl1), "i"(WBLK), "s"(w2t), "v"(wov) : "memory", "scc"); } while (0)
#define WDO() do { unsigned keep; asm volatile("s_mov_b32 %0, m0\n\ts_mov_b32 m0, %4\n\ts_nop 0\n\t" \
      "buffer_load_dwordx4 %2, %3, %1 offen nt lds\n\ts_add_u32 m0, m0, %5\n\ts_add_u32 %1, %1, 0x800\n\tbuffer_load_dwordx4 %2, %3, %1 offen nt lds\n\ts_add_u32 m0, m0, %5\n\ts_add_u32 %1, %1, 0x800\n\t" \
      "buffer_load_dwordx4 %2, %3, %1 offen nt lds\n\ts_add_u32 m0, m0, %5\n\ts_add_u32 %1, %1, 0x800\n\tbuffer_load_dwordx4 %2, %3, %1 offen nt lds\n\ts_mov_b32 m0, %0" \
      : "=&s"(keep), "+s"(wos) : "v"(wov), "s"(so_), "s"(wl0), "i"(WBLK) : "memory", "scc"); } while (0)
#define WD2() do { unsigned keep; asm volatile("s_mov_b32 %0, m0\n\ts_mov_b32 m0, %4\n\ts_nop 0\n\t" \
      "buffer_load_dwordx4 %2, %3, %1 offen nt lds\n\ts_add_u32 m0, m0, %5\n\ts_nop 0\n\tbuffer_load_dwordx4 %2, %3, %6 offen nt lds\n\ts_mov_b32 m0, %0" \
      : "=&s"(keep) : "s"(w2s), "v"(wvo), "s"(s2_), "s"(wl1), "i"(WBLK), "s"(w2t) : "memory", "scc"); } while (0)
    if (LK == 1) { const rsrc_t rk2 = mk(kc, 8u * SK * 256); RESTK(rk2); }
    if (LK == 2) { const rsrc_t rk2 = mk(kc, 8u * SK * 256); __builtin_amdgcn_s_barrier(); SB();
      if (a >= 4) {
#pragma unroll
        for (int k = KY; k < 4; ++k) kv[k] = ldw(rk2, kof + k * 256, kvo); } else if (VD) __builtin_amdgcn_s_sleep(VD);
      SB(); }
    v4u vv[4];
    WAITV(3 - OT);
    SB();
#pragma unroll
    for (int i = 0; i < YV; ++i) vv[i] = ldw(rv, l16 + i * 1024, kvo);
    SB();
    if (a >= 4) {
      WAITV(3 - YT + YV);
      if (TQ) { while ((unsigned)__builtin_amdgcn_s_memrealtime() - t0 < TQ) __builtin_amdgcn_s_sleep(1); }
      __builtin_amdgcn_s_barrier();
    }
    SB();
    v4u q0, q1, q2, q3;
#define QLD() (q0 = LD16(qvo, qso), q1 = LD16(qvo, qso + 2048))
#define QBAD() (DBAD(q0) | DBAD(q1))
    QLD();
    SB();
    STAMP(9);
    if (a >= 4) __builtin_amdgcn_s_sleep(1);
#pragma unroll
    for (int i = YV; i < OV; ++i) vv[i] = ldw(rv, l16 + i * 1024, kvo);
    SB();
    if (a < 4) { __builtin_amdgcn_s_barrier(); if (OV < 4) __builtin_amdgcn_s_sleep(1); }
    SB();
#pragma unroll
    for (int i = OV; i < 4; ++i) vv[i] = ldw(rv, l16 + i * 1024, kvo);
    SB();
    WAITV(4 - YV);
    STAMP(2);
#define QSUM() do { const v4f u_ = __builtin_bit_cast(v4f, q0 & 0xFFFFFF00u) + __builtin_bit_cast(v4f, q1 & 0xFFFFFF00u); float c_, d_; const float a_ = sw32(u_[0], u_[2], c_), b_ = sw32(u_[1], u_[3], d_); \
        *(LAS unsigned*)(L + qwr) = pk(a_ + c_, b_ + d_); } while (0)
    if (a >= 4) {
      QSUM();
      SB();
      if (__builtin_expect(ANY(QBAD()), 0)) {
        UNTIL(QBAD(), QLD());
        QSUM();
      }
    }
    SB();
    if (a < 4) {
#pragma unroll
      for (int i = 0; i < 4; ++i) *(LAS v4u*)(L + vwr + i * 4 * VSTR) = vv[i];
      if (WP == 0) WDMA();
      if (WS == 5 || WS == 6) WD2();
    }
    const bool wnow = !TW || (unsigned)__builtin_amdgcn_s_memrealtime() - t0 >= TW;
    STAMP(4);
    BAR();
    {
      const v4u qf = *(LAS v4u*)(L + qadr);
      if (WS == 0 && WP == 1 && a < 4 && wnow) WDMA();
      if ((WS == 1 || WS == 3 || WS == 7) && a < 4) WD2();
      if ((WS == 2 || WS == 4) && a < 4) WDO();
      v4f sc[4];
#pragma unroll
      for (int i = 0; i < 4; ++i) { sc[i] = mfma(kv[i], qf, v4f{0.f, 0.f, 0.f, 0.f}); SB(); }
      float t, m;
#define DADD(src, acc, rot, bank) "v_add_f32_dpp %0, " src ", " acc " row_ror:" rot " row_mask:0xf bank_mask:" bank "\n\t"
      asm volatile("s_nop 4\n\t"
                   DADD("%3", "%2", "12", "0x1") DADD("%6", "%7", "4", "0x2") DADD("%10", "%12", "8", "0x4") DADD("%14", "%17", "12", "0x8")
                   DADD("%4", "%0", "8", "0x1") DADD("%8", "%0", "12", "0x2") DADD("%11", "%0", "4", "0x4") DADD("%15", "%0", "8", "0x8")
                   DADD("%5", "%0", "4", "0x1") DADD("%9", "%0", "8", "0x2") DADD("%13", "%0", "12", "0x4") DADD("%16", "%0", "4", "0x8")
                   "v_cvt_pk_bf16_f32 %0, 0, %0\n\tv_mul_f32 %0, 0x3db504f3, %0\n\tv_cvt_pk_bf16_f32 %0, 0, %0\n\tv_mul_f32 %0, 0x3fb8aa3b, %0\n\ts_nop 1\n\t"
                   "v_max_f32_dpp %1, %0, %0 row_ror:8 row_mask:0xf bank_mask:0xf\n\ts_nop 1\n\t"
                   "v_max_f32_dpp %1, %1, %1 row_ror:4 row_mask:0xf bank_mask:0xf\n\t"
                   "v_sub_f32 %0, %0, %1\n\tv_exp_f32 %0, %0\n\t"
                   "s_mov_b64 exec, %21\n\tds_max_f32 %18, %1\n\tds_write_b32 %19, %1\n\ts_mov_b64 exec, -1\n\tds_write_b32 %20, %0"
                   : "=&v"(t), "=&v"(m)
                   : "v"(sc[0][0]), "v"(sc[0][1]), "v"(sc[0][2]), "v"(sc[0][3]), "v"(sc[1][0]), "v"(sc[1][1]), "v"(sc[1][2]), "v"(sc[1][3]),
                     "v"(sc[2][0]), "v"(sc[2][1]), "v"(sc[2][2]), "v"(sc[2][3]), "v"(sc[3][0]), "v"(sc[3][1]), "v"(sc[3][2]), "v"(sc[3][3]),
                     "v"(mad), "v"(mxw), "v"(sadr), "s"(lead)
                   : "memory");
#pragma unroll
      for (int i = 0; i < 5; ++i) __builtin_amdgcn_s_setprio(2);
    }
    v2u vt[16];
    if (a < 4) {
#pragma unroll
      for (int i = 0; i < 8; ++i) vt[(i & 3) + (i >> 2) * 8] = trd(L + tadr + (i & 3) * 16 * VSTR + (i >> 2) * 32);
      if (WP == 2) WDMA();
      if (WS == 6 || WS == 7) WDO();
    }
    STAMP(5);
    BAR();
    if (a < 4) {
      const v2u mm = *(LAS v2u*)(L + L_MX + l8);
      const float m = *(LAS float*)(L + mrd);
      const v4f s0 = *(LAS v4f*)(L + L_S + l16), s1 = *(LAS v4f*)(L + L_S + 1024 + l16);
      const v4f p0 = s0 * ex2(uf(mm[0]) - m), p1 = s1 * ex2(uf(mm[1]) - m);
      const v4u pb = {pk(p0[0], p0[1]), pk(p0[2], p0[3]), pk(p1[0], p1[1]), pk(p1[2], p1[3])};
      const v4f z = {0.f, 0.f, 0.f, 0.f};
      v4f d0 = mfma(cat(vt[0], vt[1]), pb, z), d1 = mfma(cat(vt[8], vt[9]), pb, z);
      const v4u pb1 = dpp4<0x55>(pb), pb2 = dpp4<0xAA>(pb);
      SB();
      d0 = mfma(cat(vt[2], vt[3]), pb1, d0); d1 = mfma(cat(vt[10], vt[11]), pb1, d1);
      v4u pb3 = dpp4<0xFF>(pb);
      asm volatile("" : "+v"(pb3) : "v"(pb1), "v"(vt[0]), "v"(vt[1]), "v"(vt[8]), "v"(vt[9]));
      SB();
      if (WS == 1 || WS == 5) WDO();
      if (WS == 2) WD2();
      STAMP(7);
      BAR();
#pragma unroll
      for (int i = 0; i < 8; ++i) vt[4 + (i & 1) + ((i >> 2) & 1) * 2 + ((i >> 1) & 1) * 8] = trd(L + tadr + (4 + (i & 1) + ((i >> 2) & 1) * 2) * 16 * VSTR + ((i >> 1) & 1) * 32);
      d0 = mfma(cat(vt[4], vt[5]), pb2, d0); d1 = mfma(cat(vt[12], vt[13]), pb2, d1);
      d0 = mfma(cat(vt[6], vt[7]), pb3, d0); d1 = mfma(cat(vt[14], vt[15]), pb3, d1);
      o0[0] = pk(d0[0], d0[1]); o0[2] = pk(d0[2], d0[3]); o1[0] = pk(d1[0], d1[1]); o1[2] = pk(d1[2], d1[3]);
      ST16(o0, obo, obs);
      ST16(o1, obo + 64, obs);
      STAMP(6);
      if (WS == 3) { WDO(); asm volatile("s_waitcnt vmcnt(0)" ::: "memory"); } else if (WS == 4) { WD2(); asm volatile("s_waitcnt vmcnt(0)" ::: "memory"); }
      else if (WP == 3 || !wnow) { if (TW) while ((unsigned)__builtin_amdgcn_s_memrealtime() - t0 < TX) __builtin_amdgcn_s_sleep(1); WDMA(); asm volatile("s_waitcnt vmcnt(0)" ::: "memory"); }
      else asm volatile("s_waitcnt vmcnt(2)" ::: "memory");
      STAMP(1);
    }
    SB();
    if (a >= 4) {
#pragma unroll
      for (int i = 0; i < 4; ++i) *(LAS v4u*)(L + vwr + i * 4 * VSTR) = vv[i];
      STAMP(3);
      BAR();
      __builtin_amdgcn_s_sleep(PD);
    }
    SB();
    v4u pv = LD16(pvo, pso);
    BAR();
    if (a >= 4 && ANY(TBAD(pv))) pv = LD16(pvo, pso);
    SB();
    v2u wt[4];
    const int wrd = L_W + gq * 2 * WBLK + (n >> 2) * 256 + (a ^ (n >> 2)) * 32 + (n & 3) * 8;
#pragma unroll
    for (int i = 0; i < 4; ++i) wt[i] = trd(L + L_W + (gq * 4 + (i & 1) * 2 + (n >> 3)) * WBLK + ((n >> 2) & 1) * 512 + ((a * 2 + (i >> 1)) ^ ((n >> 2) & 1)) * 32 + (n & 3) * 8);
    SB();
    WAITV(0);
    if (a < 4) {
      const v4u hf = *(LAS v4u*)(L + L_H + gq * 16);
      LAS unsigned char* p = L + wrd + 16 * WBLK;
      const v4f z = {0.f, 0.f, 0.f, 0.f};
      const v4f f0 = mfma(hf, cat(trd(p), trd(p + WBLK)), z), f1 = mfma(hf, cat(trd(p + 128), trd(p + 128 + WBLK)), z);
      ST8((v2u{__float_as_uint(gq ? f1[0] : f0[0]), tag}), l < 32 ? gq * 32768 + (n >> 3) * 4096 + (n & 7) * 8 : OOB, fbs);
    }
    SB();
    if (a >= 4) {
#define OWR() (((LAS unsigned*)(L + owr))[0] = pv[0], ((LAS unsigned*)(L + owr))[1] = pv[2])
      OWR();
      UNTIL(TBAD(pv), (pv = LD16(pvo, pso), OWR()));
      STAMP(8);
    }
    BAR();
    {
      const v4u of0 = *(LAS v4u*)(L + oadr), of1 = *(LAS v4u*)(L + oadr + 16 * 80);
      const v4f w0 = *(LAS v4f*)(L + L_WG + gq * 16), w1_ = *(LAS v4f*)(L + L_WG + 64 + gq * 16);
      SB();
      const v4f z = {0.f, 0.f, 0.f, 0.f};
      const v4f d00 = mfma(of0, cat(wt[0], wt[1]), z), d01 = mfma(of0, cat(wt[2], wt[3]), z), d10 = mfma(of1, cat(wt[0], wt[1]), z), d11 = mfma(of1, cat(wt[2], wt[3]), z);
      SB();
      v2f c0 = d00.hi * w0.hi + d00.lo * w0.lo, c1 = d01.hi * w0.hi + d01.lo * w0.lo;
      c0 = d10.hi * w1_.hi + (d10.lo * w1_.lo + c0); c1 = d11.hi * w1_.hi + (d11.lo * w1_.lo + c1);
      float e = fold32(c0[0] + c0[1], c1[0] + c1[1]);
      e = fold16(e, e);
      ST8((v2u{__float_as_uint(e), tag}), sbo, sbs);
    }
    STAMP(10);
    if (b < 64) {
      __builtin_amdgcn_s_sleep(10); if (a == g) __builtin_amdgcn_s_sleep(10);
      v4u sv = LD16(l16, fso);
#define SWR() (((LAS unsigned*)(L + fadr))[0] = sv[0], ((LAS unsigned*)(L + fadr))[576] = sv[2])
      SWR();
      UNTIL(TBAD(sv), (sv = LD16(l16, fso), SWR()));
      BAR();
    }
    FLUSH();
  }
}

extern "C" void kernel_launch(void* const* d_in, const int* in_sizes, int n_in, void* d_out, int out_size, void* d_ws, size_t ws_size, hipStream_t stream) {
  fused_decode<<<256, NTHR, 0, stream>>>((const u16*)d_in[0], (const u16*)d_in[3], (const u16*)d_in[6], (const u16*)d_in[2], (unsigned char*)d_ws, (const u16*)d_in[1],
                                         (const u16*)d_in[4], (const u16*)d_in[8], (const u16*)d_in[7], (const u16*)d_in[9], (u16*)d_out);
}
```

```cpp
#define LK 1
#define KY 2
#define YT 2
#define PD 10
#define XQ 1
#define XO 1
#define WS 1
#define MLD 8
#include <hip/hip_runtime.h>
#include <hip/hip_bf16.h>

typedef unsigned short u16;
typedef unsigned long long u64;
typedef unsigned v4u __attribute__((ext_vector_type(4)));
typedef unsigned v2u __attribute__((ext_vector_type(2)));
typedef float v4f __attribute__((ext_vector_type(4)));
typedef short s16x4 __attribute__((ext_vector_type(4)));
typedef __bf16 bf16x2 __attribute__((ext_vector_type(2)));
typedef __bf16 bf16x8 __attribute__((ext_vector_type(8)));
typedef __amdgpu_buffer_rsrc_t rsrc_t;
#define GAS __attribute__((address_space(1)))
#define CAS __attribute__((address_space(4)))
#define LAS __attribute__((address_space(3)))

#define TQ 0
#define YV 0
#define OV 4
#define OT 0
#define EV 1
#define TW 0
#define TX TW
#define Q32 0
#define PFQ 1
#define SH 0
#define FORCE -1
#define VD 2
#define ASL 0
#define PFA 0
#define TLB 0
#define WP 1
#define STAMP(i)
#define FLUSH()
constexpr int SK = 32768, WIN = 4096, FF = 1792;
constexpr int NTHR = 576;
constexpr unsigned WS_QG = 4096;
constexpr unsigned WS_HG = WS_QG + 131072;
constexpr unsigned WS_ML = WS_HG + 57344;
constexpr unsigned WS_OB = WS_ML + 16384;
constexpr unsigned WS_SB = WS_OB + 524288;
constexpr unsigned WS_FB = WS_SB + 524288;
constexpr unsigned WS_END = WS_FB + 262144;
constexpr int VSTR = 288, L_V = 0, L_O = 128 * VSTR, L_Q = L_O + 4096, QSTR = 320, L_S = L_Q + 4 * QSTR, L_XN = (L_S + 2048 + 1023) / 1024 * 1024, L_MX = L_XN + 1024, L_M = L_MX + 512, L_H = L_M + 32,
              L_WG = L_H + 64, L_W = L_WG + 128, WBLK = 1088, L_END = L_W + 24 * WBLK;
static_assert(L_O % 128 == 0, "the o image is addressed with ^ 32 and ^ 64");
constexpr unsigned SPIN_CAP = 1u << 18;
constexpr float LOG2E = 1.4426950408889634f;
constexpr int OOB = 0x40000000;

__device__ __forceinline__ rsrc_t mk(const void* p, unsigned bytes) { return __builtin_amdgcn_make_buffer_rsrc((void*)p, 0, bytes, 0x00020000); }
__device__ __forceinline__ v4u ldw(rsrc_t r, int voff, int soff) { return __builtin_amdgcn_raw_buffer_load_b128(r, voff, soff, 2); }
__device__ __forceinline__ float uf(unsigned u) { return __uint_as_float(u); }
__device__ __forceinline__ float bflo(unsigned u) { return __uint_as_float(u << 16); }
__device__ __forceinline__ float bfhi(unsigned u) { return __uint_as_float(u & 0xFFFF0000u); }
__device__ __forceinline__ unsigned pk(float lo, float hi) { bf16x2 r = {(__bf16)lo, (__bf16)hi}; return __builtin_bit_cast(unsigned, r); }
typedef float v2f __attribute__((ext_vector_type(2)));
__device__ __forceinline__ float rb(float f) { return __builtin_bit_cast(float, __builtin_convertvector((v2f{0.f, f}), bf16x2)); }
__device__ __forceinline__ v4f mfma(v4u a, v4u b, v4f c) { return __builtin_amdgcn_mfma_f32_16x16x32_bf16(__builtin_bit_cast(bf16x8, a), __builtin_bit_cast(bf16x8, b), c, 0, 0, 0); }
template <int CTRL> __device__ __forceinline__ unsigned dppu(unsigned x) { return (unsigned)__builtin_amdgcn_mov_dpp((int)x, CTRL, 0xf, 0xf, true); }
template <int CTRL> __device__ __forceinline__ float dpp(float x) { return __uint_as_float(dppu<CTRL>(__float_as_uint(x))); }
template <int CTRL> __device__ __forceinline__ v4u dpp4(v4u x) { return v4u{dppu<CTRL>(x[0]), dppu<CTRL>(x[1]), dppu<CTRL>(x[2]), dppu<CTRL>(x[3])}; }
constexpr int X1 = 0xB1, X2 = 0x4E, HM = 0x141, R4 = 0x124, R8 = 0x128;
__device__ __forceinline__ float sw16(float a, float b, float& o) { auto r = __builtin_amdgcn_permlane16_swap(__float_as_uint(a), __float_as_uint(b), false, false); o = uf(r[1]); return uf(r[0]); }
__device__ __forceinline__ float sw32(float a, float b, float& o) { auto r = __builtin_amdgcn_permlane32_swap(__float_as_uint(a), __float_as_uint(b), false, false); o = uf(r[1]); return uf(r[0]); }
__device__ __forceinline__ float fold16(float a, float b) { float o; const float p = sw16(a, b, o); return p + o; }
__device__ __forceinline__ float fold32(float a, float b) { float o; const float p = sw32(a, b, o); return p + o; }
__device__ __forceinline__ float fmx(float a, float b) { return __builtin_elementwise_maximum(a, b); }
__device__ __forceinline__ float max16(float a) { float o; const float p = sw16(a, a, o); return fmx(p, o); }
#define MAXDPP(name, ctl) __device__ __forceinline__ float name(float m) { float r; asm("s_nop 1\n\tv_max_f32_dpp %0, %1, %1 " ctl " row_mask:0xf bank_mask:0xf" : "=v"(r) : "v"(m)); return r; }
MAXDPP(mx_r8, "row_ror:8") MAXDPP(mx_r4, "row_ror:4") MAXDPP(mx_x1, "quad_perm:[1,0,3,2]") MAXDPP(mx_x2, "quad_perm:[2,3,0,1]")
__device__ __forceinline__ float sum64(float v) { v += dpp<X1>(v); v += dpp<X2>(v); v += dpp<HM>(v); v += dpp<R8>(v); v = fold16(v, v); return fold32(v, v); }
__device__ __forceinline__ v2u trd(LAS unsigned char* p) { return __builtin_bit_cast(v2u, __builtin_amdgcn_ds_read_tr16_b64_v4i16((LAS s16x4*)p)); }
__device__ __forceinline__ v4u cat(v2u a, v2u b) { return v4u{a[0], a[1], b[0], b[1]}; }
__device__ __forceinline__ float ex2(float x) { return __builtin_amdgcn_exp2f(x); }
__device__ __forceinline__ float rcp(float x) { return __builtin_amdgcn_rcpf(x); }
__device__ __forceinline__ float gval(unsigned d) { return __uint_as_float(d & 0xFFFFFF00u); }
__device__ __forceinline__ unsigned d4(float f, unsigned tagbyte) { return ((__float_as_uint(f) + 0x80u) & 0xFFFFFF00u) | tagbyte; }
#define SB() __builtin_amdgcn_sched_barrier(0)
#define BAR() do { asm volatile("s_waitcnt lgkmcnt(0)" ::: "memory"); __builtin_amdgcn_s_barrier(); asm volatile("" ::: "memory"); } while (0)
#define FENCE() asm volatile("" ::: "memory")
#define WAITV(n) __builtin_amdgcn_s_waitcnt(0x0F70 | (n))
#define LD16(off, soff) __builtin_amdgcn_raw_buffer_load_b128(rs, (off), (soff), 16)
#define ST16(v, off, soff) __builtin_amdgcn_raw_buffer_store_b128((v), rs, (off), (soff), 16)
#define ST8(v, off, soff) __builtin_amdgcn_raw_buffer_store_b64((v), rs, (off), (soff), 16)
#define TBAD(v) __builtin_amdgcn_bitop3_b32((v)[1], (v)[3], tag, 0x7E)
#define ANY(t) (__builtin_amdgcn_ballot_w64((t) != 0) != 0)
#define DBAD(v) __builtin_amdgcn_bitop3_b32(__builtin_amdgcn_perm((v)[1], (v)[0], 0x0c0c0400u), __builtin_amdgcn_perm((v)[3], (v)[2], 0x04000c0cu), tag, 0x56)
#define EPOCH() asm volatile("" : "+s"(ep0)); const unsigned tag = ((ep0 & 0x7F7F7F7Fu) + 0x01010101u) ^ (ep0 & 0x80808080u)
#define UNTIL(BADEXPR, RELOAD) do { if (ANY(BADEXPR)) { int spins_ = SPIN_CAP; _Pragma("nounroll") do { FENCE(); RELOAD; if (!ANY(BADEXPR)) break; asm volatile("" : "+s"(spins_)); } while (--spins_); } } while (0)

__device__ __forceinline__ void mksel(unsigned (&sel)[8], int l) {
#pragma unroll
  for (int d = 0; d < 4; ++d) { const bool on = ((l & 7) >> 1) == d;
    sel[d] = on ? ((l & 1) ? 0x01000c0cu : 0x0c0c0100u) : 0x0c0c0c0cu; sel[4 + d] = on ? ((l & 1) ? 0x03020c0cu : 0x0c0c0302u) : 0x0c0c0c0cu; }
}
__device__ __forceinline__ v4u diag(unsigned pair, const unsigned (&sel)[8], int hi) {
  return v4u{__builtin_amdgcn_perm(0u, pair, sel[4 * hi]), __builtin_amdgcn_perm(0u, pair, sel[4 * hi + 1]), __builtin_amdgcn_perm(0u, pair, sel[4 * hi + 2]), __builtin_amdgcn_perm(0u, pair, sel[4 * hi + 3])};
}
__device__ __forceinline__ float upr8(float v) { return __builtin_bit_cast(float, __builtin_amdgcn_update_dpp(__builtin_bit_cast(int, v), __builtin_bit_cast(int, v), R8, 0xC, 0xF, false)); }
__device__ __forceinline__ v2u diag_out(v4f d, unsigned tb0, unsigned tb1) {
  const float t0 = upr8(d[0]), t1 = upr8(d[1]), t2 = upr8(d[2]), t3 = upr8(d[3]);
  return v2u{d4(fold32(t0, t2), tb0), d4(fold32(t1, t3), tb1)};
}

__global__ void __launch_bounds__(NTHR, 3) fused_decode(const u16* __restrict__ x, const u16* __restrict__ wq, const u16* __restrict__ w1, const u16* __restrict__ vc, unsigned char* ws,
                                                        const u16* __restrict__ kc, const u16* __restrict__ wo, const u16* __restrict__ w2, const u16* __restrict__ b1,
                                                        const u16* __restrict__ b2, u16* __restrict__ out) {
  __shared__ __attribute__((aligned(128))) unsigned char lds_[L_END];
  LAS unsigned char* L = (LAS unsigned char*)lds_;
  const int b = blockIdx.x, g = b & 7, j = b >> 3, l = threadIdx.x & 63;
  int w = __builtin_amdgcn_readfirstlane(threadIdx.x >> 6);
  const unsigned t0 = (TQ | TW) ? (unsigned)__builtin_amdgcn_s_memrealtime() : 0u;
  const unsigned c0_ = (unsigned)__builtin_amdgcn_s_memrealtime();
  { u64 pc; unsigned keep, t_;
    asm volatile("s_cmp_lg_u32 %3, 4\n\ts_cbranch_scc1 1f\n\ts_getpc_b64 %0\n\ts_mov_b32 %1, m0\n\ts_mov_b32 m0, %5\n\tv_lshlrev_b32 %2, 7, %4\n\t"
                 "global_load_lds_dword %2, %0\n\ts_mov_b32 m0, %1\n1:"
                 : "=&s"(pc), "=&s"(keep), "=&v"(t_) : "s"(w), "v"(PFA == 1 ? l + 1 : PFA == 2 ? ((l < 16) ? 8 + l : min((j * 5 + (l & 7) % 5) % 80, 64)) : min((j * 5 + (l & 7) % 5) % 80, SH ? 70 : 64)), "i"(L_O) : "scc", "memory"); }
  const int kq = j & 3, rb_ = j >> 2, gn = (g + XQ) & 7, gp = (g - XO) & 7;
  const int gq = l >> 4, n = l & 15;
  const bool hasw1 = rb_ != 3; const int jw = rb_ == 7 ? j - 16 : j;
  int kvo = (g * SK + (SK - WIN) + j * 128 + (((w - 1) * 2 + ((w - 1) >> 2) + j) & 7) * 16) * 256, l16 = l * 16, kof = (n >> 2) * 1024 + gq * 64 + (n & 3) * 16;
  int vo = (l >> 3) * (16 * 8192) + (l & 7) * 16, so = kq * (128 * 8192) + gn * 1024 + rb_ * 128;
  const bool early = !SH || (FORCE >= 0 ? FORCE == 1 : (__builtin_amdgcn_s_getreg(20 | (3 << 11)) & 4) == 0);
  int vo1 = (hasw1 && early) ? (l >> 3) * (8 * FF * 2) + (l & 7) * 16 : OOB, so1 = g * (64 * FF * 2) + jw * 128;
  asm volatile("" : "+s"(kvo), "+s"(so), "+s"(so1));
  asm volatile("s_setprio 3" : "+v"(l16), "+v"(vo), "+v"(vo1), "+v"(kof));
  asm volatile("" :: "s"(x), "s"(kc), "s"(vc), "s"(wq), "s"(wo), "s"(w1), "s"(b1), "s"(w2), "s"(b2), "s"(out), "s"(ws));
  unsigned ep0 = 0; if (!EV) ep0 = *(const CAS unsigned*)ws;
  const rsrc_t rs = mk(ws, WS_END);
  v4u kv[4];
  if (w != 0) {
    const rsrc_t rk = mk(kc, 8u * SK * 256);
    if (PFQ && w == 3) {
      unsigned keep;
      const unsigned po = PFQ == 1 ? (j * 16 + 8 + (l & 7)) * 8192 + gn * 1024 + 384 + ((l >> 3) & 1) * 512
                        : PFQ == 2 ? (j * 16 + (l & 15)) * 8192 + gn * 1024 + 384 + ((l >> 4) & 1) * 512
                        : PFQ == 3 ? (j * 16 + 4 + (l & 15) % 12) * 8192 + gn * 1024 + 384
                        : (j * 16 + 8 + (l & 7)) * 8192 + gn * 1024 + 128 + ((l >> 3) & 3) * 256;
      asm volatile("s_mov_b32 %0, m0\n\ts_mov_b32 m0, %3\n\ts_nop 0\n\tglobal_load_lds_dword %1, %2 nt\n\ts_mov_b32 m0, %0" : "=&s"(keep) : "v"(po), "s"(wq), "i"(L_O + 256) : "memory");
    }
    if (TLB && w == 2) {
      unsigned keep; const int k = l & 7;
      const u64 ad = k == 0 ? (u64)vc + (unsigned)kvo : k == 1 ? (u64)wo + (gp * 512 + (j >> 1) * 32) * 1024 : k == 2 ? (u64)w2 + (gp * 224 + rb_ * 28) * 1024 : k == 3 ? (u64)b1 : k == 4 ? (u64)b2
                   : k == 5 ? (u64)ws + WS_END : k == 6 ? (u64)kc + (unsigned)kvo : (u64)out;
      asm volatile("s_mov_b32 %0, m0\n\ts_mov_b32 m0, %2\n\ts_nop 0\n\tglobal_load_lds_dword %1, off nt\n\ts_mov_b32 m0, %0" : "=&s"(keep) : "v"(ad), "i"(L_O + 512) : "memory");
    }
    if (EV) ep0 = *(const CAS unsigned*)ws;
    __builtin_amdgcn_s_setprio(2);
    __builtin_amdgcn_s_barrier();
    SB();
#pragma unroll
    for (int k = 0; k < KY; ++k) kv[k] = ldw(rk, kof + k * 256, kvo);
    SB();
#define RESTK(RK) do { \
    if (KY < 4 && w > 4) __builtin_amdgcn_s_barrier(); \
    SB(); \
    _Pragma("unroll") \
    for (int k = KY; k < 4; ++k) kv[k] = ldw(RK, kof + k * 256, kvo); \
    SB(); \
    if (KY < 4 && w <= 4) __builtin_amdgcn_s_barrier(); \
 } while (0)
    if (ASL) __builtin_amdgcn_s_sleep(ASL);
    if (!LK) RESTK(rk);
    if (LK == 2 && w <= 4) {
#pragma unroll
      for (int k = KY; k < 4; ++k) kv[k] = ldw(rk, kof + k * 256, kvo); }
  }
  asm volatile("" : "+s"(w));
  if (__builtin_expect(w == 0, 1)) {
    v4u r16[16], f8[8];
    const v4u xv = *(const GAS v4u*)((const GAS unsigned char*)x + (unsigned)l16);
    const unsigned epv = EV ? __builtin_amdgcn_raw_buffer_load_b32(rs, 0, 0, 0) : 0u;
    { const rsrc_t r = mk(wq, 512 * 4096 * 2), r1 = mk(w1, 512 * FF * 2);
#pragma unroll
      for (int i = 0; i < 16; ++i) r16[i] = ldw(r, vo, so + i * 8192);
#pragma unroll
      for (int i = 0; i < 8; ++i) f8[i] = ldw(r1, vo1, so1 + i * (FF * 2)); }
    SB();
    __builtin_amdgcn_s_barrier();
    SB();
    unsigned sel[8]; mksel(sel, l);
    int gvo = (l & 8) ? OOB : (l & 7) * 32 + (l & 16) + (l >> 5) * 8, xra = L_XN + kq * 256 + (l >> 3) * 32, xrc = L_XN + g * 128 + (l >> 3) * 16;
    asm volatile("" : "+v"(sel[0]), "+v"(sel[1]), "+v"(sel[2]), "+v"(sel[3]), "+v"(sel[4]), "+v"(sel[5]), "+v"(sel[6]), "+v"(sel[7]), "+v"(gvo), "+v"(xra), "+v"(xrc));
    SB();
    float xf[8];
#pragma unroll
    for (int i = 0; i < 4; ++i) { xf[2 * i] = bflo(xv[i]); xf[2 * i + 1] = bfhi(xv[i]); }
    const float mean = sum64(((xf[0] + xf[1]) + (xf[2] + xf[3])) + ((xf[4] + xf[5]) + (xf[6] + xf[7]))) * (1.f / 512);
    float vs = 0.f;
#pragma unroll
    for (int i = 0; i < 8; ++i) { const float d = xf[i] - mean; vs = fmaf(d, d, vs); }
    const float var = sum64(vs) * (1.f / 512);
    const float mb = rb(mean), rsd = rcp(rb(__builtin_amdgcn_sqrtf(rb(rb(var) + 1e-5f))));
    v4u xn;
#pragma unroll
    for (int i = 0; i < 4; ++i) { const unsigned d = pk(xf[2 * i] - mb, xf[2 * i + 1] - mb); xn[i] = pk(bflo(d) * rsd, bfhi(d) * rsd); }
    *(LAS v4u*)(L + L_XN + l * 16) = xn;
    *(LAS float*)(L + L_M + (l & 3) * 4) = -__builtin_inff();
    const v4u xa = *(LAS v4u*)(L + xra), xb = *(LAS v4u*)(L + xra + 16), xc = *(LAS v4u*)(L + xrc);
    if (EV) ep0 = __builtin_amdgcn_readfirstlane(epv);
    EPOCH();
    const unsigned tb0 = (tag >> ((l >> 5) * 16)) & 0xFFu, tb1 = (tag >> ((l >> 5) * 16 + 8)) & 0xFFu;
    { v4u bx[8];
#pragma unroll
      for (int i = 0; i < 8; ++i) bx[i] = diag(xa[i >> 1], sel, i & 1);
      SB();
      v4f d = {0.f, 0.f, 0.f, 0.f};
#pragma unroll
      for (int i = 0; i < 8; ++i) { d = mfma(bx[i], r16[i], d); bx[i] = diag(xb[i >> 1], sel, i & 1); SB(); }
#pragma unroll
      for (int i = 0; i < 8; ++i) d = mfma(bx[i], r16[8 + i], d);
      ST8(diag_out(d, tb0, tb1), gvo, WS_QG + gn * 8192 + kq * 2048 + rb_ * 256);
      STAMP(3);
      SB();
      if (SH) { const rsrc_t r1 = mk(w1, 512 * FF * 2);
#pragma unroll
        for (int i = 0; i < 8; ++i) r16[i] = ldw(r1, vo1, so1 + ((g ^ 4) - g) * (64 * FF * 2) + i * (FF * 2)); }
      SB();
      if (KY < 4) __builtin_amdgcn_s_barrier();
      SB();
    }
    { v4f d = {0.f, 0.f, 0.f, 0.f};
#pragma unroll
      for (int i = 0; i < 8; ++i) d = mfma(diag(xc[i >> 1], sel, i & 1), f8[i], d);
      ST8(diag_out(d, tb0, tb1), (hasw1 && early) ? gvo : OOB, WS_HG + g * (FF * 4) + jw * 256); }
    const int hr0 = gp * 224 + rb_ * 28, hu = min(l & 7, 6), ocol = (b & 63) * 8 + (l & 3) * 2 + (l >> 5);
    int mlo = (l & 51) == 0 ? (n >> 2) * 512 : OOB, mls = WS_ML + g * 2048 + j * 16, hvo = (l >> 3) * (FF * 4) + hu * 16, hso = WS_HG + hr0 * 4;
    int pvo = (l & 31) * 16, pso = WS_ML + gp * 2048 + (j >> 3) * 512;
    asm volatile("" : "+v"(mlo), "+v"(hvo), "+v"(pvo));
    asm volatile("" : "+s"(mls), "+s"(hso), "+s"(pso));
    const float b1v = bflo(b1[hr0 + hu * 4 + gq]), b2v = bflo(b2[ocol]), xo = bflo(x[ocol]);
    if (SH) { const v4u xd = *(LAS v4u*)(L + (xrc ^ 512)); v4f d = {0.f, 0.f, 0.f, 0.f};
#pragma unroll
      for (int i = 0; i < 8; ++i) d = mfma(diag(xd[i >> 1], sel, i & 1), r16[i], d);
      ST8(diag_out(d, tb0, tb1), (hasw1 && early) ? gvo : OOB, WS_HG + (g ^ 4) * (FF * 4) + jw * 256); }
    BAR();
    BAR();
    v4u hg; if (!SH) hg = LD16(hvo, hso);
    SB();
    BAR();
    { const v2u mm = *(LAS v2u*)(L + L_MX + l * 8);
      const v4f s0 = *(LAS v4f*)(L + L_S + l * 16), s1 = *(LAS v4f*)(L + L_S + 1024 + l * 16);
      const float m = *(LAS float*)(L + L_M + (n >> 2) * 4);
      float ls = fmaf((s0[0] + s0[1]) + (s0[2] + s0[3]), ex2(uf(mm[0]) - m), ((s1[0] + s1[1]) + (s1[2] + s1[3])) * ex2(uf(mm[1]) - m));
      ls += dpp<X1>(ls); ls += dpp<X2>(ls); ls = fold16(ls, ls); ls = fold32(ls, ls);
      ST16((v4u{__float_as_uint(m), tag, __float_as_uint(ls), tag}), mlo, mls);
    }
    STAMP(6);
    v4u own;
    if (SH) { own = LD16(n * 16, WS_HG + g * (FF * 4) + jw * 256); hg = LD16(hvo, hso); SB(); }
    BAR();
    if (SH && __builtin_expect(hasw1 && !early && ANY(DBAD(own)), 0)) { int tries = 32;
      _Pragma("nounroll") do { FENCE(); own = LD16(n * 16, WS_HG + g * (FF * 4) + jw * 256); } while (ANY(DBAD(own)) && --tries);
      if (!tries) {
      const rsrc_t r1 = mk(w1, 512 * FF * 2); v4f d = {0.f, 0.f, 0.f, 0.f};
#pragma unroll
      for (int i = 0; i < 8; ++i) f8[i] = ldw(r1, (l >> 3) * (8 * FF * 2) + (l & 7) * 16, so1 + i * (FF * 2));
#pragma unroll
      for (int i = 0; i < 8; ++i) d = mfma(diag(xc[i >> 1], sel, i & 1), f8[i], d);
      ST8(diag_out(d, tb0, tb1), gvo, WS_HG + g * (FF * 4) + jw * 256);
    } }
    {
      if (__builtin_expect(ANY(DBAD(hg)), 0)) UNTIL(DBAD(hg), hg = LD16(hvo, hso));
      float hs = fold16(fold32(gval(hg[0]), gval(hg[2])), fold32(gval(hg[1]), gval(hg[3]))); hs += dpp<R8>(hs);
      const float hp = rb(rb(hs) + b1v);
      const float u2 = 1.5957691216057308f * fmaf(0.044715f * hp * hp, hp, hp);
      const float ge = hp * rcp(1.f + ex2(-u2 * LOG2E));
      *(LAS u16*)(L + L_H + (l & 7) * 8 + gq * 2) = ((l & 7) < 7) ? (u16)pk(ge, ge) : (u16)0;
    }
    __builtin_amdgcn_s_sleep(MLD);
    v4u pv = LD16(pvo, pso);
    SB();
    BAR();
    {
      UNTIL(TBAD(pv), pv = LD16(pvo, pso));
      const float m = uf(pv[0]);
      const float M = max16(mx_r4(mx_r8(mx_x2(mx_x1(m)))));
      const float e = ex2(m - M);
      float ls = e * uf(pv[2]);
      ls += dpp<X1>(ls); ls += dpp<X2>(ls); ls += dpp<HM>(ls); ls += dpp<R8>(ls); ls = fold16(ls, ls);
      *(LAS float*)(L + L_WG + (l & 31) * 4) = e * rcp(ls);
    }
    STAMP(8);
    BAR();
    if (b < 64) {
      const int fo = WS_FB + b * 4096;
      v4u f0 = LD16(l16, fo), f1 = LD16(l16 + 1024, fo), f2 = LD16(l16 + 2048, fo), f3 = LD16(l16 + 3072, fo);
      UNTIL(TBAD(f0) | TBAD(f1) | TBAD(f2) | TBAD(f3), (f0 = LD16(l16, fo), f1 = LD16(l16 + 1024, fo), f2 = LD16(l16 + 2048, fo), f3 = LD16(l16 + 3072, fo)));
      float t = fold32((uf(f0[0]) + uf(f1[0])) + (uf(f2[0]) + uf(f3[0])), (uf(f0[2]) + uf(f1[2])) + (uf(f2[2]) + uf(f3[2])));
      t = fold16(t, t); t += dpp<R8>(t); t += dpp<R4>(t);
      const float f = rb(rb(t) + b2v);
      float gt = rb(f * rcp(1.f + ex2(-f * LOG2E)));
      int frd = L_V + ((l >> 5) * 576 + (l & 3) * 136 + ((l >> 2) & 7) * 16) * 4;
      int ovo = (l & 28) ? OOB : ocol * 2; float xo_ = xo;
      asm volatile("" : "+v"(gt), "+v"(frd), "+v"(ovo), "+v"(xo_));
      BAR();
      const v4f u0 = *(LAS v4f*)(L + frd), u1 = *(LAS v4f*)(L + frd + 16);
      const v4f u = (u0 + u1) + (*(LAS v4f*)(L + frd + 32) + *(LAS v4f*)(L + frd + 48));
      float v = (u[0] + u[2]) + (u[1] + u[3]);
      v = fold16(v, v); v += dpp<R8>(v); v += dpp<R4>(v);
      const float o = xo_ + rb(rb(v) * gt);
      __builtin_amdgcn_raw_buffer_store_b16((u16)pk(o, o), mk(out, 1024), ovo, 0, 16);
      if (b == 0 && l == 0) __hip_atomic_store((GAS unsigned*)ws, tag, __ATOMIC_RELAXED, __HIP_MEMORY_SCOPE_AGENT);
    }
    STAMP(10);
    FLUSH();
  } else {
    const int a = w - 1, a3 = a & 3;
    int obo = (n & 3) == 0 ? (n >> 2) * 16384 + gq * 16 : OOB;
    int sbo = (l & 16) ? OOB : (l >> 5) * 16384 + (n >> 3) * 8192 + (n & 7) * 8;
    int obs = WS_OB + g * 65536 + a3 * 4096 + j * 128, sbs = WS_SB + ((j & 1) * 32 + a * 4) * 8192 + (g * 16 + (j >> 1)) * 64;
    int fbs = WS_FB + (kq * 16 + a * 2) * 4096 + (g * 8 + rb_) * 64;
    int sadr = L_S + (a & 1) * 1024 + gq * 256 + (n & 3) * 64 + (a >> 1) * 16 + (n >> 2) * 4, qadr = L_Q + (n & 3) * QSTR + gq * 64 + (n >> 2) * 16;
    int pso = WS_OB + gp * 65536 + (j >> 1) * 4096 + a3 * 1024, pvo = a >= 4 ? l * 16 : OOB;
    int oadr = L_O + n * 80 + gq * 16, owr = L_O + (a3 * 8 + (l >> 3)) * 80 + (l & 7) * 8;
    int mad = L_M + (n & 3) * 4, l8 = l * 8, mrd = L_M + (n >> 2) * 4;
    asm volatile("" : "+v"(mad), "+v"(l8), "+v"(mrd));
    int fso = WS_SB + b * 8192 + a * 1024, fadr = L_V + ((l & 3) * 136 + a * 16 + (l >> 2)) * 4;
    int vwr = L_V + (a * 16 + gq) * VSTR + n * 16;
    int qso = Q32 ? WS_QG + g * 16384 + a3 * 1024 : WS_QG + g * 8192 + a3 * 512, qvo = a >= 4 ? (Q32 ? l * 16 : (l >> 5) * 4096 + (l & 31) * 16) : OOB;
    int mxw = L_MX + gq * 128 + (n & 3) * 32 + a * 4, qwr = L_Q + a3 * QSTR + (Q32 ? l * 4 : (l & 31) * 8 + (l >> 5) * 4);
    int wov = (l >> 5) * 1024 + ((l & 31) ^ ((l >> 5) * 2)) * 16;
    asm volatile("" : "+v"(wov));
    int wos = (gp * 512 + (j >> 1) * 32 + a3 * 8) * 1024 + (j & 1) * 512, w2s = (gp * 224 + rb_ * 28 + a3 * 8) * 1024 + kq * 256, wvo = gq * 1024 + (n ^ (gq * 2)) * 16;
    int w2t = w2s + (a3 == 3 ? 0 : 4096);
    int wl0 = L_W + a3 * 4 * WBLK, wl1 = L_W + (16 + a3 * 2) * WBLK;
    v4u so_ = {(unsigned)(u64)wo, (unsigned)((u64)wo >> 32) & 0xFFFFu, 4096u * 512 * 2, 0x20000u}, s2_ = {(unsigned)(u64)w2, (unsigned)((u64)w2 >> 32) & 0xFFFFu, FF * 512u * 2, 0x20000u};
    asm volatile("" : "+v"(obo), "+v"(sbo), "+v"(sadr), "+v"(qadr), "+v"(oadr), "+v"(owr), "+v"(vwr), "+v"(fadr), "+v"(qvo), "+v"(mxw), "+v"(qwr), "+v"(pvo), "+v"(wvo));
    asm volatile("" : "+s"(obs), "+s"(sbs), "+s"(pso), "+s"(qso), "+s"(fso), "+s"(wos), "+s"(w2s), "+s"(so_), "+s"(s2_), "+s"(wl0), "+s"(wl1), "+s"(w2t));
    const rsrc_t rv = mk(vc, 8u * SK * 256);
    int tadr = L_V + (gq * 4 + (n >> 2)) * VSTR + a3 * 64 + (n & 3) * 8;
    asm volatile("" : "+v"(tadr));
    EPOCH();
    v4u o0 = {0u, tag, 0u, tag}, o1 = o0; u64 lead = 0x000F000F000F000Full;
    asm volatile("" : "+v"(o0), "+v"(o1)); asm volatile("" : "+s"(lead));
    SB();
#define DMA1(VO, SRD, SO) "buffer_load_dwordx4 " VO ", " SRD ", " SO " offen nt lds\n\ts_add_u32 m0, m0, %8\n\ts_add_u32 " SO ", " SO ", 0x800\n\t"
#define WDMA() do { unsigned keep; asm volatile("s_mov_b32 %0, m0\n\ts_mov_b32 m0, %6\n\ts_nop 0\n\t" DMA1("%10", "%4", "%1") DMA1("%10", "%4", "%1") DMA1("%10", "%4", "%1") DMA1("%10", "%4", "%1") \
                   "s_mov_b32 m0, %7\n\ts_nop 0\n\t" DMA1("%3", "%5", "%2") "buffer_load_dwordx4 %3, %5, %9 offen nt lds\n\ts_mov_b32 m0, %0" \
                   : "=&s"(keep), "+s"(wos), "+s"(w2s) : "v"(wvo), "s"(so_), "s"(s2_), "s"(wl0), "s"(wl1), "i"(WBLK), "s"(w2t), "v"(wov) : "memory", "scc"); } while (0)
#define WDO() do { unsigned keep; asm volatile("s_mov_b32 %0, m0\n\ts_mov_b32 m0, %4\n\ts_nop 0\n\t" \
      "buffer_load_dwordx4 %2, %3, %1 offen nt lds\n\ts_add_u32 m0, m0, %5\n\ts_add_u32 %1, %1, 0x800\n\tbuffer_load_dwordx4 %2, %3, %1 offen nt lds\n\ts_add_u32 m0, m0, %5\n\ts_add_u32 %1, %1, 0x800\n\t" \
      "buffer_load_dwordx4 %2, %3, %1 offen nt lds\n\ts_add_u32 m0, m0, %5\n\ts_add_u32 %1, %1, 0x800\n\tbuffer_load_dwordx4 %2, %3, %1 offen nt lds\n\ts_mov_b32 m0, %0" \
      : "=&s"(keep), "+s"(wos) : "v"(wov), "s"(so_), "s"(wl0), "i"(WBLK) : "memory", "scc"); } while (0)
#define WD2() do { unsigned keep; asm volatile("s_mov_b32 %0, m0\n\ts_mov_b32 m0, %4\n\ts_nop 0\n\t" \
      "buffer_load_dwordx4 %2, %3, %1 offen nt lds\n\ts_add_u32 m0, m0, %5\n\ts_nop 0\n\tbuffer_load_dwordx4 %2, %3, %6 offen nt lds\n\ts_mov_b32 m0, %0" \
      : "=&s"(keep) : "s"(w2s), "v"(wvo), "s"(s2_), "s"(wl1), "i"(WBLK), "s"(w2t) : "memory", "scc"); } while (0)
    if (LK == 1) { const rsrc_t rk2 = mk(kc, 8u * SK * 256); RESTK(rk2); }
    if (LK == 2) { const rsrc_t rk2 = mk(kc, 8u * SK * 256); __builtin_amdgcn_s_barrier(); SB();
      if (a >= 4) {
#pragma unroll
        for (int k = KY; k < 4; ++k) kv[k] = ldw(rk2, kof + k * 256, kvo); } else if (VD) __builtin_amdgcn_s_sleep(VD);
      SB(); }
    v4u vv[4];
    WAITV(3 - OT);
    SB();
#pragma unroll
    for (int i = 0; i < YV; ++i) vv[i] = ldw(rv, l16 + i * 1024, kvo);
    SB();
    if (a >= 4) {
      WAITV(3 - YT + YV);
      if (TQ) { while ((unsigned)__builtin_amdgcn_s_memrealtime() - t0 < TQ) __builtin_amdgcn_s_sleep(1); }
      __builtin_amdgcn_s_barrier();
    }
    SB();
    v4u q0, q1, q2, q3;
#define QLD() (q0 = LD16(qvo, qso), q1 = LD16(qvo, qso + 2048))
#define QBAD() (DBAD(q0) | DBAD(q1))
    QLD();
    SB();
    STAMP(9);
    if (a >= 4) __builtin_amdgcn_s_sleep(1);
#pragma unroll
    for (int i = YV; i < OV; ++i) vv[i] = ldw(rv, l16 + i * 1024, kvo);
    SB();
    if (a < 4) { __builtin_amdgcn_s_barrier(); if (OV < 4) __builtin_amdgcn_s_sleep(1); }
    SB();
#pragma unroll
    for (int i = OV; i < 4; ++i) vv[i] = ldw(rv, l16 + i * 1024, kvo);
    SB();
    WAITV(4 - YV);
    STAMP(2);
#define QSUM() do { const v4f u_ = __builtin_bit_cast(v4f, q0 & 0xFFFFFF00u) + __builtin_bit_cast(v4f, q1 & 0xFFFFFF00u); float c_, d_; const float a_ = sw32(u_[0], u_[2], c_), b_ = sw32(u_[1], u_[3], d_); \
        *(LAS unsigned*)(L + qwr) = pk(a_ + c_, b_ + d_); } while (0)
    if (a >= 4) {
      QSUM();
      SB();
      if (__builtin_expect(ANY(QBAD()), 0)) {
        UNTIL(QBAD(), QLD());
        QSUM();
      }
    }
    SB();
    if (a < 4) {
#pragma unroll
      for (int i = 0; i < 4; ++i) *(LAS v4u*)(L + vwr + i * 4 * VSTR) = vv[i];
      if (WP == 0) WDMA();
      if (WS == 5 || WS == 6) WD2();
    }
    const bool wnow = !TW || (unsigned)__builtin_amdgcn_s_memrealtime() - t0 >= TW;
    STAMP(4);
    BAR();
    {
      const v4u qf = *(LAS v4u*)(L + qadr);
      if (WS == 0 && WP == 1 && a < 4 && wnow) WDMA();
      if ((WS == 1 || WS == 3 || WS == 7) && a < 4) WD2();
      if ((WS == 2 || WS == 4) && a < 4) WDO();
      v4f sc[4];
#pragma unroll
      for (int i = 0; i < 4; ++i) { sc[i] = mfma(kv[i], qf, v4f{0.f, 0.f, 0.f, 0.f}); SB(); }
      float t, m;
#define DADD(src, acc, rot, bank) "v_add_f32_dpp %0, " src ", " acc " row_ror:" rot " row_mask:0xf bank_mask:" bank "\n\t"
      asm volatile("s_nop 4\n\t"
                   DADD("%3", "%2", "12", "0x1") DADD("%6", "%7", "4", "0x2") DADD("%10", "%12", "8", "0x4") DADD("%14", "%17", "12", "0x8")
                   DADD("%4", "%0", "8", "0x1") DADD("%8", "%0", "12", "0x2") DADD("%11", "%0", "4", "0x4") DADD("%15", "%0", "8", "0x8")
                   DADD("%5", "%0", "4", "0x1") DADD("%9", "%0", "8", "0x2") DADD("%13", "%0", "12", "0x4") DADD("%16", "%0", "4", "0x8")
                   "v_cvt_pk_bf16_f32 %0, 0, %0\n\tv_mul_f32 %0, 0x3db504f3, %0\n\tv_cvt_pk_bf16_f32 %0, 0, %0\n\tv_mul_f32 %0, 0x3fb8aa3b, %0\n\ts_nop 1\n\t"
                   "v_max_f32_dpp %1, %0, %0 row_ror:8 row_mask:0xf bank_mask:0xf\n\ts_nop 1\n\t"
                   "v_max_f32_dpp %1, %1, %1 row_ror:4 row_mask:0xf bank_mask:0xf\n\t"
                   "v_sub_f32 %0, %0, %1\n\tv_exp_f32 %0, %0\n\t"
                   "s_mov_b64 exec, %21\n\tds_max_f32 %18, %1\n\tds_write_b32 %19, %1\n\ts_mov_b64 exec, -1\n\tds_write_b32 %20, %0"
                   : "=&v"(t), "=&v"(m)
                   : "v"(sc[0][0]), "v"(sc[0][1]), "v"(sc[0][2]), "v"(sc[0][3]), "v"(sc[1][0]), "v"(sc[1][1]), "v"(sc[1][2]), "v"(sc[1][3]),
                     "v"(sc[2][0]), "v"(sc[2][1]), "v"(sc[2][2]), "v"(sc[2][3]), "v"(sc[3][0]), "v"(sc[3][1]), "v"(sc[3][2]), "v"(sc[3][3]),
                     "v"(mad), "v"(mxw), "v"(sadr), "s"(lead)
                   : "memory");
#pragma unroll
      for (int i = 0; i < 5; ++i) __builtin_amdgcn_s_setprio(2);
    }
    v2u vt[16];
    if (a < 4) {
#pragma unroll
      for (int i = 0; i < 8; ++i) vt[(i & 3) + (i >> 2) * 8] = trd(L + tadr + (i & 3) * 16 * VSTR + (i >> 2) * 32);
      if (WP == 2) WDMA();
      if (WS == 6 || WS == 7) WDO();
    }
    STAMP(5);
    BAR();
    if (a < 4) {
      const v2u mm = *(LAS v2u*)(L + L_MX + l8);
      const float m = *(LAS float*)(L + mrd);
      const v4f s0 = *(LAS v4f*)(L + L_S + l16), s1 = *(LAS v4f*)(L + L_S + 1024 + l16);
      const v4f p0 = s0 * ex2(uf(mm[0]) - m), p1 = s1 * ex2(uf(mm[1]) - m);
      const v4u pb = {pk(p0[0], p0[1]), pk(p0[2], p0[3]), pk(p1[0], p1[1]), pk(p1[2], p1[3])};
      const v4f z = {0.f, 0.f, 0.f, 0.f};
      v4f d0 = mfma(cat(vt[0], vt[1]), pb, z), d1 = mfma(cat(vt[8], vt[9]), pb, z);
      const v4u pb1 = dpp4<0x55>(pb), pb2 = dpp4<0xAA>(pb);
      SB();
      d0 = mfma(cat(vt[2], vt[3]), pb1, d0); d1 = mfma(cat(vt[10], vt[11]), pb1, d1);
      v4u pb3 = dpp4<0xFF>(pb);
      asm volatile("" : "+v"(pb3) : "v"(pb1), "v"(vt[0]), "v"(vt[1]), "v"(vt[8]), "v"(vt[9]));
      SB();
      if (WS == 1 || WS == 5) WDO();
      if (WS == 2) WD2();
      STAMP(7);
      BAR();
#pragma unroll
      for (int i = 0; i < 8; ++i) vt[4 + (i & 1) + ((i >> 2) & 1) * 2 + ((i >> 1) & 1) * 8] = trd(L + tadr + (4 + (i & 1) + ((i >> 2) & 1) * 2) * 16 * VSTR + ((i >> 1) & 1) * 32);
      d0 = mfma(cat(vt[4], vt[5]), pb2, d0); d1 = mfma(cat(vt[12], vt[13]), pb2, d1);
      d0 = mfma(cat(vt[6], vt[7]), pb3, d0); d1 = mfma(cat(vt[14], vt[15]), pb3, d1);
      o0[0] = pk(d0[0], d0[1]); o0[2] = pk(d0[2], d0[3]); o1[0] = pk(d1[0], d1[1]); o1[2] = pk(d1[2], d1[3]);
      ST16(o0, obo, obs);
      ST16(o1, obo + 64, obs);
      STAMP(6);
      if (WS == 3) { WDO(); asm volatile("s_waitcnt vmcnt(0)" ::: "memory"); } else if (WS == 4) { WD2(); asm volatile("s_waitcnt vmcnt(0)" ::: "memory"); }
      else if (WP == 3 || !wnow) { if (TW) while ((unsigned)__builtin_amdgcn_s_memrealtime() - t0 < TX) __builtin_amdgcn_s_sleep(1); WDMA(); asm volatile("s_waitcnt vmcnt(0)" ::: "memory"); }
      else asm volatile("s_waitcnt vmcnt(2)" ::: "memory");
      STAMP(1);
    }
    SB();
    if (a >= 4) {
#pragma unroll
      for (int i = 0; i < 4; ++i) *(LAS v4u*)(L + vwr + i * 4 * VSTR) = vv[i];
      STAMP(3);
      BAR();
      __builtin_amdgcn_s_sleep(PD);
    }
    SB();
    v4u pv = LD16(pvo, pso);
    BAR();
    if (a >= 4 && ANY(TBAD(pv))) pv = LD16(pvo, pso);
    SB();
    v2u wt[4];
    const int wrd = L_W + gq * 2 * WBLK + (n >> 2) * 256 + (a ^ (n >> 2)) * 32 + (n & 3) * 8;
#pragma unroll
    for (int i = 0; i < 4; ++i) wt[i] = trd(L + L_W + (gq * 4 + (i & 1) * 2 + (n >> 3)) * WBLK + ((n >> 2) & 1) * 512 + ((a * 2 + (i >> 1)) ^ ((n >> 2) & 1)) * 32 + (n & 3) * 8);
    SB();
    WAITV(0);
    if (a < 4) {
      const v4u hf = *(LAS v4u*)(L + L_H + gq * 16);
      LAS unsigned char* p = L + wrd + 16 * WBLK;
      const v4f z = {0.f, 0.f, 0.f, 0.f};
      const v4f f0 = mfma(hf, cat(trd(p), trd(p + WBLK)), z), f1 = mfma(hf, cat(trd(p + 128), trd(p + 128 + WBLK)), z);
      ST8((v2u{__float_as_uint(gq ? f1[0] : f0[0]), tag}), l < 32 ? gq * 32768 + (n >> 3) * 4096 + (n & 7) * 8 : OOB, fbs);
    }
    SB();
    if (a >= 4) {
#define OWR() (((LAS unsigned*)(L + owr))[0] = pv[0], ((LAS unsigned*)(L + owr))[1] = pv[2])
      OWR();
      UNTIL(TBAD(pv), (pv = LD16(pvo, pso), OWR()));
      STAMP(8);
    }
    BAR();
    {
      const v4u of0 = *(LAS v4u*)(L + oadr), of1 = *(LAS v4u*)(L + oadr + 16 * 80);
      const v4f w0 = *(LAS v4f*)(L + L_WG + gq * 16), w1_ = *(LAS v4f*)(L + L_WG + 64 + gq * 16);
      SB();
      const v4f z = {0.f, 0.f, 0.f, 0.f};
      const v4f d00 = mfma(of0, cat(wt[0], wt[1]), z), d01 = mfma(of0, cat(wt[2], wt[3]), z), d10 = mfma(of1, cat(wt[0], wt[1]), z), d11 = mfma(of1, cat(wt[2], wt[3]), z);
      SB();
      v2f c0 = d00.hi * w0.hi + d00.lo * w0.lo, c1 = d01.hi * w0.hi + d01.lo * w0.lo;
      c0 = d10.hi * w1_.hi + (d10.lo * w1_.lo + c0); c1 = d11.hi * w1_.hi + (d11.lo * w1_.lo + c1);
      float e = fold32(c0[0] + c0[1], c1[0] + c1[1]);
      e = fold16(e, e);
      ST8((v2u{__float_as_uint(e), tag}), sbo, sbs);
    }
    STAMP(10);
    if (b < 64) {
      __builtin_amdgcn_s_sleep(10); if (a == g) __builtin_amdgcn_s_sleep(10);
      v4u sv = LD16(l16, fso);
#define SWR() (((LAS unsigned*)(L + fadr))[0] = sv[0], ((LAS unsigned*)(L + fadr))[576] = sv[2])
      SWR();
      UNTIL(TBAD(sv), (sv = LD16(l16, fso), SWR()));
      BAR();
      if (a == 0) { const unsigned e_ = (unsigned)__builtin_amdgcn_s_memrealtime(), d_ = 4 * (e_ - c0_); while ((unsigned)__builtin_amdgcn_s_memrealtime() - e_ < d_ && d_ < 100000u) {} }
    }
  }
}

extern "C" void kernel_launch(void* const* d_in, const int* in_sizes, int n_in, void* d_out, int out_size, void* d_ws, size_t ws_size, hipStream_t stream) {
  fused_decode<<<256, NTHR, 0, stream>>>((const u16*)d_in[0], (const u16*)d_in[3], (const u16*)d_in[6], (const u16*)d_in[2], (unsigned char*)d_ws, (const u16*)d_in[1],
                                         (const u16*)d_in[4], (const u16*)d_in[8], (const u16*)d_in[7], (const u16*)d_in[9], (u16*)d_out);
}
```

```cpp
#define LK 1
#define KY 2
#define YT 2
#define PD 10
#define XQ 1
#define XO 1
#define WS 1
#define MLD 8
#define XS 1
#include <hip/hip_runtime.h>
#include <hip/hip_bf16.h>

typedef unsigned short u16;
typedef unsigned long long u64;
typedef unsigned v4u __attribute__((ext_vector_type(4)));
typedef unsigned v16u __attribute__((ext_vector_type(16)));
typedef unsigned v2u __attribute__((ext_vector_type(2)));
typedef float v4f __attribute__((ext_vector_type(4)));
typedef short s16x4 __attribute__((ext_vector_type(4)));
typedef __bf16 bf16x2 __attribute__((ext_vector_type(2)));
typedef __bf16 bf16x8 __attribute__((ext_vector_type(8)));
typedef __amdgpu_buffer_rsrc_t rsrc_t;
#define GAS __attribute__((address_space(1)))
#define CAS __attribute__((address_space(4)))
#define LAS __attribute__((address_space(3)))

#define TQ 0
#define YV 0
#define OV 4
#define OT 0
#define EV 1
#define TW 0
#define TX TW
#define Q32 0
#define PFQ 1
#define SH 0
#define FORCE -1
#define VD 2
#define ASL 0
#define PFA 0
#define TLB 0
#define WP 1
#define STAMP(i)
#define FLUSH()
constexpr int SK = 32768, WIN = 4096, FF = 1792;
constexpr int NTHR = 576;
constexpr unsigned WS_QG = 4096;
constexpr unsigned WS_HG = WS_QG + 131072;
constexpr unsigned WS_ML = WS_HG + 57344;
constexpr unsigned WS_OB = WS_ML + 16384;
constexpr unsigned WS_SB = WS_OB + 524288;
constexpr unsigned WS_FB = WS_SB + 524288;
constexpr unsigned WS_END = WS_FB + 262144;
constexpr int VSTR = 288, L_V = 0, L_O = 128 * VSTR, L_Q = L_O + 4096, QSTR = 320, L_S = L_Q + 4 * QSTR, L_XN = (L_S + 2048 + 1023) / 1024 * 1024, L_MX = L_XN + 1024, L_M = L_MX + 512, L_H = L_M + 32,
              L_WG = L_H + 64, L_W = L_WG + 128, WBLK = 1088, L_END = L_W + 24 * WBLK;
static_assert(L_O % 128 == 0, "the o image is addressed with ^ 32 and ^ 64");
constexpr unsigned SPIN_CAP = 1u << 18;
constexpr float LOG2E = 1.4426950408889634f;
constexpr int OOB = 0x40000000;

__device__ __forceinline__ rsrc_t mk(const void* p, unsigned bytes) { return __builtin_amdgcn_make_buffer_rsrc((void*)p, 0, bytes, 0x00020000); }
__device__ __forceinline__ v4u ldw(rsrc_t r, int voff, int soff) { return __builtin_amdgcn_raw_buffer_load_b128(r, voff, soff, 2); }
__device__ __forceinline__ float uf(unsigned u) { return __uint_as_float(u); }
__device__ __forceinline__ float bflo(unsigned u) { return __uint_as_float(u << 16); }
__device__ __forceinline__ float bfhi(unsigned u) { return __uint_as_float(u & 0xFFFF0000u); }
__device__ __forceinline__ unsigned pk(float lo, float hi) { bf16x2 r = {(__bf16)lo, (__bf16)hi}; return __builtin_bit_cast(unsigned, r); }
typedef float v2f __attribute__((ext_vector_type(2)));
__device__ __forceinline__ float rb(float f) { return __builtin_bit_cast(float, __builtin_convertvector((v2f{0.f, f}), bf16x2)); }
__device__ __forceinline__ v4f mfma(v4u a, v4u b, v4f c) { return __builtin_amdgcn_mfma_f32_16x16x32_bf16(__builtin_bit_cast(bf16x8, a), __builtin_bit_cast(bf16x8, b), c, 0, 0, 0); }
template <int CTRL> __device__ __forceinline__ unsigned dppu(unsigned x) { return (unsigned)__builtin_amdgcn_mov_dpp((int)x, CTRL, 0xf, 0xf, true); }
template <int CTRL> __device__ __forceinline__ float dpp(float x) { return __uint_as_float(dppu<CTRL>(__float_as_uint(x))); }
template <int CTRL> __device__ __forceinline__ v4u dpp4(v4u x) { return v4u{dppu<CTRL>(x[0]), dppu<CTRL>(x[1]), dppu<CTRL>(x[2]), dppu<CTRL>(x[3])}; }
constexpr int X1 = 0xB1, X2 = 0x4E, HM = 0x141, R4 = 0x124, R8 = 0x128;
__device__ __forceinline__ float sw16(float a, float b, float& o) { auto r = __builtin_amdgcn_permlane16_swap(__float_as_uint(a), __float_as_uint(b), false, false); o = uf(r[1]); return uf(r[0]); }
__device__ __forceinline__ float sw32(float a, float b, float& o) { auto r = __builtin_amdgcn_permlane32_swap(__float_as_uint(a), __float_as_uint(b), false, false); o = uf(r[1]); return uf(r[0]); }
__device__ __forceinline__ float fold16(float a, float b) { float o; const float p = sw16(a, b, o); return p + o; }
__device__ __forceinline__ float fold32(float a, float b) { float o; const float p = sw32(a, b, o); return p + o; }
__device__ __forceinline__ float fmx(float a, float b) { return __builtin_elementwise_maximum(a, b); }
__device__ __forceinline__ float max16(float a) { float o; const float p = sw16(a, a, o); return fmx(p, o); }
#define MAXDPP(name, ctl) __device__ __forceinline__ float name(float m) { float r; asm("s_nop 1\n\tv_max_f32_dpp %0, %1, %1 " ctl " row_mask:0xf bank_mask:0xf" : "=v"(r) : "v"(m)); return r; }
MAXDPP(mx_r8, "row_ror:8") MAXDPP(mx_r4, "row_ror:4") MAXDPP(mx_x1, "quad_perm:[1,0,3,2]") MAXDPP(mx_x2, "quad_perm:[2,3,0,1]")
__device__ __forceinline__ float sum64(float v) { v += dpp<X1>(v); v += dpp<X2>(v); v += dpp<HM>(v); v += dpp<R8>(v); v = fold16(v, v); return fold32(v, v); }
__device__ __forceinline__ v2u trd(LAS unsigned char* p) { return __builtin_bit_cast(v2u, __builtin_amdgcn_ds_read_tr16_b64_v4i16((LAS s16x4*)p)); }
__device__ __forceinline__ v4u cat(v2u a, v2u b) { return v4u{a[0], a[1], b[0], b[1]}; }
__device__ __forceinline__ float ex2(float x) { return __builtin_amdgcn_exp2f(x); }
__device__ __forceinline__ float rcp(float x) { return __builtin_amdgcn_rcpf(x); }
__device__ __forceinline__ float gval(unsigned d) { return __uint_as_float(d & 0xFFFFFF00u); }
__device__ __forceinline__ unsigned d4(float f, unsigned tagbyte) { return ((__float_as_uint(f) + 0x80u) & 0xFFFFFF00u) | tagbyte; }
#define SB() __builtin_amdgcn_sched_barrier(0)
#define BAR() do { asm volatile("s_waitcnt lgkmcnt(0)" ::: "memory"); __builtin_amdgcn_s_barrier(); asm volatile("" ::: "memory"); } while (0)
#define FENCE() asm volatile("" ::: "memory")
#define WAITV(n) __builtin_amdgcn_s_waitcnt(0x0F70 | (n))
#define LD16(off, soff) __builtin_amdgcn_raw_buffer_load_b128(rs, (off), (soff), 16)
#define ST16(v, off, soff) __builtin_amdgcn_raw_buffer_store_b128((v), rs, (off), (soff), 16)
#define ST8(v, off, soff) __builtin_amdgcn_raw_buffer_store_b64((v), rs, (off), (soff), 16)
#define TBAD(v) __builtin_amdgcn_bitop3_b32((v)[1], (v)[3], tag, 0x7E)
#define ANY(t) (__builtin_amdgcn_ballot_w64((t) != 0) != 0)
#define DBAD(v) __builtin_amdgcn_bitop3_b32(__builtin_amdgcn_perm((v)[1], (v)[0], 0x0c0c0400u), __builtin_amdgcn_perm((v)[3], (v)[2], 0x04000c0cu), tag, 0x56)
#define EPOCH() asm volatile("" : "+s"(ep0)); const unsigned tag = ((ep0 & 0x7F7F7F7Fu) + 0x01010101u) ^ (ep0 & 0x80808080u)
#define UNTIL(BADEXPR, RELOAD) do { if (ANY(BADEXPR)) { int spins_ = SPIN_CAP; _Pragma("nounroll") do { FENCE(); RELOAD; if (!ANY(BADEXPR)) break; asm volatile("" : "+s"(spins_)); } while (--spins_); } } while (0)

__device__ __forceinline__ void mksel(unsigned (&sel)[8], int l) {
#pragma unroll
  for (int d = 0; d < 4; ++d) { const bool on = ((l & 7) >> 1) == d;
    sel[d] = on ? ((l & 1) ? 0x01000c0cu : 0x0c0c0100u) : 0x0c0c0c0cu; sel[4 + d] = on ? ((l & 1) ? 0x03020c0cu : 0x0c0c0302u) : 0x0c0c0c0cu; }
}
__device__ __forceinline__ v4u diag(unsigned pair, const unsigned (&sel)[8], int hi) {
  return v4u{__builtin_amdgcn_perm(0u, pair, sel[4 * hi]), __builtin_amdgcn_perm(0u, pair, sel[4 * hi + 1]), __builtin_amdgcn_perm(0u, pair, sel[4 * hi + 2]), __builtin_amdgcn_perm(0u, pair, sel[4 * hi + 3])};
}
__device__ __forceinline__ float upr8(float v) { return __builtin_bit_cast(float, __builtin_amdgcn_update_dpp(__builtin_bit_cast(int, v), __builtin_bit_cast(int, v), R8, 0xC, 0xF, false)); }
__device__ __forceinline__ v2u diag_out(v4f d, unsigned tb0, unsigned tb1) {
  const float t0 = upr8(d[0]), t1 = upr8(d[1]), t2 = upr8(d[2]), t3 = upr8(d[3]);
  return v2u{d4(fold32(t0, t2), tb0), d4(fold32(t1, t3), tb1)};
}

__global__ void __launch_bounds__(NTHR, 3) fused_decode(const u16* __restrict__ x, const u16* __restrict__ wq, const u16* __restrict__ w1, const u16* __restrict__ vc, unsigned char* ws,
                                                        const u16* __restrict__ kc, const u16* __restrict__ wo, const u16* __restrict__ w2, const u16* __restrict__ b1,
                                                        const u16* __restrict__ b2, u16* __restrict__ out) {
  __shared__ __attribute__((aligned(128))) unsigned char lds_[L_END];
  LAS unsigned char* L = (LAS unsigned char*)lds_;
  const int b = blockIdx.x, g = b & 7, j = b >> 3, l = threadIdx.x & 63;
  int w = __builtin_amdgcn_readfirstlane(threadIdx.x >> 6);
  const unsigned t0 = (TQ | TW) ? (unsigned)__builtin_amdgcn_s_memrealtime() : 0u;
  const unsigned c0_ = (unsigned)__builtin_amdgcn_s_memrealtime();
  { u64 pc; unsigned keep, t_;
    asm volatile("s_cmp_lg_u32 %3, 4\n\ts_cbranch_scc1 1f\n\ts_getpc_b64 %0\n\ts_mov_b32 %1, m0\n\ts_mov_b32 m0, %5\n\tv_lshlrev_b32 %2, 7, %4\n\t"
                 "global_load_lds_dword %2, %0\n\ts_mov_b32 m0, %1\n1:"
                 : "=&s"(pc), "=&s"(keep), "=&v"(t_) : "s"(w), "v"(PFA == 1 ? l + 1 : PFA == 2 ? ((l < 16) ? 8 + l : min((j * 5 + (l & 7) % 5) % 80, 64)) : min((j * 5 + (l & 7) % 5) % 80, SH ? 70 : 64)), "i"(L_O) : "scc", "memory"); }
  const int kq = j & 3, rb_ = j >> 2, gn = (g + XQ) & 7, gp = (g - XO) & 7;
  const int gq = l >> 4, n = l & 15;
  const bool hasw1 = rb_ != 3; const int jw = rb_ == 7 ? j - 16 : j;
  int kvo = (g * SK + (SK - WIN) + j * 128 + (((w - 1) * 2 + ((w - 1) >> 2) + j) & 7) * 16) * 256, l16 = l * 16, kof = (n >> 2) * 1024 + gq * 64 + (n & 3) * 16;
  int vo = (l >> 3) * (16 * 8192) + (l & 7) * 16, so = kq * (128 * 8192) + gn * 1024 + rb_ * 128;
  const bool early = !SH || (FORCE >= 0 ? FORCE == 1 : (__builtin_amdgcn_s_getreg(20 | (3 << 11)) & 4) == 0);
  int vo1 = (hasw1 && early) ? (l >> 3) * (8 * FF * 2) + (l & 7) * 16 : OOB, so1 = g * (64 * FF * 2) + jw * 128;
  asm volatile("" : "+s"(kvo), "+s"(so), "+s"(so1));
  asm volatile("s_setprio 3" : "+v"(l16), "+v"(vo), "+v"(vo1), "+v"(kof));
  asm volatile("" :: "s"(x), "s"(kc), "s"(vc), "s"(wq), "s"(wo), "s"(w1), "s"(b1), "s"(w2), "s"(b2), "s"(out), "s"(ws));
  unsigned ep0 = 0; if (!EV) ep0 = *(const CAS unsigned*)ws;
  const rsrc_t rs = mk(ws, WS_END);
  v4u kv[4]; v16u xs0, xs1;
  if (w != 0) {
    const rsrc_t rk = mk(kc, 8u * SK * 256);
    if (PFQ && w == 3) {
      unsigned keep;
      const unsigned po = PFQ == 1 ? (j * 16 + 8 + (l & 7)) * 8192 + gn * 1024 + 384 + ((l >> 3) & 1) * 512
                        : PFQ == 2 ? (j * 16 + (l & 15)) * 8192 + gn * 1024 + 384 + ((l >> 4) & 1) * 512
                        : PFQ == 3 ? (j * 16 + 4 + (l & 15) % 12) * 8192 + gn * 1024 + 384
                        : (j * 16 + 8 + (l & 7)) * 8192 + gn * 1024 + 128 + ((l >> 3) & 3) * 256;
      asm volatile("s_mov_b32 %0, m0\n\ts_mov_b32 m0, %3\n\ts_nop 0\n\tglobal_load_lds_dword %1, %2 nt\n\ts_mov_b32 m0, %0" : "=&s"(keep) : "v"(po), "s"(wq), "i"(L_O + 256) : "memory");
    }
    if (TLB && w == 2) {
      unsigned keep; const int k = l & 7;
      const u64 ad = k == 0 ? (u64)vc + (unsigned)kvo : k == 1 ? (u64)wo + (gp * 512 + (j >> 1) * 32) * 1024 : k == 2 ? (u64)w2 + (gp * 224 + rb_ * 28) * 1024 : k == 3 ? (u64)b1 : k == 4 ? (u64)b2
                   : k == 5 ? (u64)ws + WS_END : k == 6 ? (u64)kc + (unsigned)kvo : (u64)out;
      asm volatile("s_mov_b32 %0, m0\n\ts_mov_b32 m0, %2\n\ts_nop 0\n\tglobal_load_lds_dword %1, off nt\n\ts_mov_b32 m0, %0" : "=&s"(keep) : "v"(ad), "i"(L_O + 512) : "memory");
    }
    if (XS) { const CAS v16u* xp = (const CAS v16u*)x + (w - 1) * 2; xs0 = xp[0]; xs1 = xp[1]; }
    if (EV && XS != 2) ep0 = *(const CAS unsigned*)ws;
    __builtin_amdgcn_s_setprio(2);
    __builtin_amdgcn_s_barrier();
    SB();
#pragma unroll
    for (int k = 0; k < KY; ++k) kv[k] = ldw(rk, kof + k * 256, kvo);
    SB();
#define RESTK(RK) do { \
    if (KY < 4 && w > 4) __builtin_amdgcn_s_barrier(); \
    SB(); \
    _Pragma("unroll") \
    for (int k = KY; k < 4; ++k) kv[k] = ldw(RK, kof + k * 256, kvo); \
    SB(); \
    if (KY < 4 && w <= 4) __builtin_amdgcn_s_barrier(); \
 } while (0)
    if (ASL) __builtin_amdgcn_s_sleep(ASL);
    if (!LK) RESTK(rk);
    if (LK == 2 && w <= 4) {
#pragma unroll
      for (int k = KY; k < 4; ++k) kv[k] = ldw(rk, kof + k * 256, kvo); }
  }
  asm volatile("" : "+s"(w));
  if (__builtin_expect(w == 0, 1)) {
    v4u r16[16], f8[8];
    v4u xv; unsigned epv = 0;
    if (!XS) { xv = *(const GAS v4u*)((const GAS unsigned char*)x + (unsigned)l16); epv = EV ? __builtin_amdgcn_raw_buffer_load_b32(rs, 0, 0, 0) : 0u; }
    { const rsrc_t r = mk(wq, 512 * 4096 * 2), r1 = mk(w1, 512 * FF * 2);
#pragma unroll
      for (int i = 0; i < 16; ++i) r16[i] = ldw(r, vo, so + i * 8192);
      if (XS == 2) { SB(); epv = __builtin_amdgcn_raw_buffer_load_b32(rs, 0, 0, 2); SB(); }
#pragma unroll
      for (int i = 0; i < 8; ++i) f8[i] = ldw(r1, vo1, so1 + i * (FF * 2)); }
    SB();
    __builtin_amdgcn_s_barrier();
    SB();
    unsigned sel[8]; mksel(sel, l);
    int gvo = (l & 8) ? OOB : (l & 7) * 32 + (l & 16) + (l >> 5) * 8, xra = L_XN + kq * 256 + (l >> 3) * 32, xrc = L_XN + g * 128 + (l >> 3) * 16;
    asm volatile("" : "+v"(sel[0]), "+v"(sel[1]), "+v"(sel[2]), "+v"(sel[3]), "+v"(sel[4]), "+v"(sel[5]), "+v"(sel[6]), "+v"(sel[7]), "+v"(gvo), "+v"(xra), "+v"(xrc));
    SB();
    if (XS) { BAR(); xv = *(LAS v4u*)(L + L_S + l16); if (XS == 1) epv = *(LAS unsigned*)(L + L_S + 1024); STAMP(5); }
    float xf[8];
#pragma unroll
    for (int i = 0; i < 4; ++i) { xf[2 * i] = bflo(xv[i]); xf[2 * i + 1] = bfhi(xv[i]); }
    const float mean = sum64(((xf[0] + xf[1]) + (xf[2] + xf[3])) + ((xf[4] + xf[5]) + (xf[6] + xf[7]))) * (1.f / 512);
    float vs = 0.f;
#pragma unroll
    for (int i = 0; i < 8; ++i) { const float d = xf[i] - mean; vs = fmaf(d, d, vs); }
    const float var = sum64(vs) * (1.f / 512);
    const float mb = rb(mean), rsd = rcp(rb(__builtin_amdgcn_sqrtf(rb(rb(var) + 1e-5f))));
    v4u xn;
#pragma unroll
    for (int i = 0; i < 4; ++i) { const unsigned d = pk(xf[2 * i] - mb, xf[2 * i + 1] - mb); xn[i] = pk(bflo(d) * rsd, bfhi(d) * rsd); }
    *(LAS v4u*)(L + L_XN + l * 16) = xn;
    *(LAS float*)(L + L_M + (l & 3) * 4) = -__builtin_inff();
    const v4u xa = *(LAS v4u*)(L + xra), xb = *(LAS v4u*)(L + xra + 16), xc = *(LAS v4u*)(L + xrc);
    unsigned tag, tb0, tb1;
#define MKTAG() do { if (EV) ep0 = __builtin_amdgcn_readfirstlane(epv); asm volatile("" : "+s"(ep0)); tag = ((ep0 & 0x7F7F7F7Fu) + 0x01010101u) ^ (ep0 & 0x80808080u); tb0 = (tag >> ((l >> 5) * 16)) & 0xFFu; tb1 = (tag >> ((l >> 5) * 16 + 8)) & 0xFFu; } while (0)
    if (XS != 2) MKTAG();
    { v4u bx[8];
#pragma unroll
      for (int i = 0; i < 8; ++i) bx[i] = diag(xa[i >> 1], sel, i & 1);
      SB();
      v4f d = {0.f, 0.f, 0.f, 0.f};
#pragma unroll
      for (int i = 0; i < 8; ++i) { d = mfma(bx[i], r16[i], d); bx[i] = diag(xb[i >> 1], sel, i & 1); SB(); }
#pragma unroll
      for (int i = 0; i < 8; ++i) d = mfma(bx[i], r16[8 + i], d);
      SB();
      if (XS == 2) MKTAG();
      ST8(diag_out(d, tb0, tb1), gvo, WS_QG + gn * 8192 + kq * 2048 + rb_ * 256);
      STAMP(3);
      SB();
      if (SH) { const rsrc_t r1 = mk(w1, 512 * FF * 2);
#pragma unroll
        for (int i = 0; i < 8; ++i) r16[i] = ldw(r1, vo1, so1 + ((g ^ 4) - g) * (64 * FF * 2) + i * (FF * 2)); }
      SB();
      if (KY < 4) __builtin_amdgcn_s_barrier();
      SB();
    }
    { v4f d = {0.f, 0.f, 0.f, 0.f};
#pragma unroll
      for (int i = 0; i < 8; ++i) d = mfma(diag(xc[i >> 1], sel, i & 1), f8[i], d);
      ST8(diag_out(d, tb0, tb1), (hasw1 && early) ? gvo : OOB, WS_HG + g * (FF * 4) + jw * 256); }
    const int hr0 = gp * 224 + rb_ * 28, hu = min(l & 7, 6), ocol = (b & 63) * 8 + (l & 3) * 2 + (l >> 5);
    int mlo = (l & 51) == 0 ? (n >> 2) * 512 : OOB, mls = WS_ML + g * 2048 + j * 16, hvo = (l >> 3) * (FF * 4) + hu * 16, hso = WS_HG + hr0 * 4;
    int pvo = (l & 31) * 16, pso = WS_ML + gp * 2048 + (j >> 3) * 512;
    asm volatile("" : "+v"(mlo), "+v"(hvo), "+v"(pvo));
    asm volatile("" : "+s"(mls), "+s"(hso), "+s"(pso));
    const float b1v = bflo(b1[hr0 + hu * 4 + gq]), b2v = bflo(b2[ocol]), xo = bflo(x[ocol]);
    if (SH) { const v4u xd = *(LAS v4u*)(L + (xrc ^ 512)); v4f d = {0.f, 0.f, 0.f, 0.f};
#pragma unroll
      for (int i = 0; i < 8; ++i) d = mfma(diag(xd[i >> 1], sel, i & 1), r16[i], d);
      ST8(diag_out(d, tb0, tb1), (hasw1 && early) ? gvo : OOB, WS_HG + (g ^ 4) * (FF * 4) + jw * 256); }
    BAR();
    BAR();
    v4u hg; if (!SH) hg = LD16(hvo, hso);
    SB();
    BAR();
    { const v2u mm = *(LAS v2u*)(L + L_MX + l * 8);
      const v4f s0 = *(LAS v4f*)(L + L_S + l * 16), s1 = *(LAS v4f*)(L + L_S + 1024 + l * 16);
      const float m = *(LAS float*)(L + L_M + (n >> 2) * 4);
      float ls = fmaf((s0[0] + s0[1]) + (s0[2] + s0[3]), ex2(uf(mm[0]) - m), ((s1[0] + s1[1]) + (s1[2] + s1[3])) * ex2(uf(mm[1]) - m));
      ls += dpp<X1>(ls); ls += dpp<X2>(ls); ls = fold16(ls, ls); ls = fold32(ls, ls);
      ST16((v4u{__float_as_uint(m), tag, __float_as_uint(ls), tag}), mlo, mls);
    }
    STAMP(6);
    v4u own;
    if (SH) { own = LD16(n * 16, WS_HG + g * (FF * 4) + jw * 256); hg = LD16(hvo, hso); SB(); }
    BAR();
    if (SH && __builtin_expect(hasw1 && !early && ANY(DBAD(own)), 0)) { int tries = 32;
      _Pragma("nounroll") do { FENCE(); own = LD16(n * 16, WS_HG + g * (FF * 4) + jw * 256); } while (ANY(DBAD(own)) && --tries);
      if (!tries) {
      const rsrc_t r1 = mk(w1, 512 * FF * 2); v4f d = {0.f, 0.f, 0.f, 0.f};
#pragma unroll
      for (int i = 0; i < 8; ++i) f8[i] = ldw(r1, (l >> 3) * (8 * FF * 2) + (l & 7) * 16, so1 + i * (FF * 2));
#pragma unroll
      for (int i = 0; i < 8; ++i) d = mfma(diag(xc[i >> 1], sel, i & 1), f8[i], d);
      ST8(diag_out(d, tb0, tb1), gvo, WS_HG + g * (FF * 4) + jw * 256);
    } }
    {
      if (__builtin_expect(ANY(DBAD(hg)), 0)) UNTIL(DBAD(hg), hg = LD16(hvo, hso));
      float hs = fold16(fold32(gval(hg[0]), gval(hg[2])), fold32(gval(hg[1]), gval(hg[3]))); hs += dpp<R8>(hs);
      const float hp = rb(rb(hs) + b1v);
      const float u2 = 1.5957691216057308f * fmaf(0.044715f * hp * hp, hp, hp);
      const float ge = hp * rcp(1.f + ex2(-u2 * LOG2E));
      *(LAS u16*)(L + L_H + (l & 7) * 8 + gq * 2) = ((l & 7) < 7) ? (u16)pk(ge, ge) : (u16)0;
    }
    __builtin_amdgcn_s_sleep(MLD);
    v4u pv = LD16(pvo, pso);
    SB();
    BAR();
    {
      UNTIL(TBAD(pv), pv = LD16(pvo, pso));
      const float m = uf(pv[0]);
      const float M = max16(mx_r4(mx_r8(mx_x2(mx_x1(m)))));
      const float e = ex2(m - M);
      float ls = e * uf(pv[2]);
      ls += dpp<X1>(ls); ls += dpp<X2>(ls); ls += dpp<HM>(ls); ls += dpp<R8>(ls); ls = fold16(ls, ls);
      *(LAS float*)(L + L_WG + (l & 31) * 4) = e * rcp(ls);
    }
    STAMP(8);
    BAR();
    if (b < 64) {
      const int fo = WS_FB + b * 4096;
      v4u f0 = LD16(l16, fo), f1 = LD16(l16 + 1024, fo), f2 = LD16(l16 + 2048, fo), f3 = LD16(l16 + 3072, fo);
      UNTIL(TBAD(f0) | TBAD(f1) | TBAD(f2) | TBAD(f3), (f0 = LD16(l16, fo), f1 = LD16(l16 + 1024, fo), f2 = LD16(l16 + 2048, fo), f3 = LD16(l16 + 3072, fo)));
      float t = fold32((uf(f0[0]) + uf(f1[0])) + (uf(f2[0]) + uf(f3[0])), (uf(f0[2]) + uf(f1[2])) + (uf(f2[2]) + uf(f3[2])));
      t = fold16(t, t); t += dpp<R8>(t); t += dpp<R4>(t);
      const float f = rb(rb(t) + b2v);
      float gt = rb(f * rcp(1.f + ex2(-f * LOG2E)));
      int frd = L_V + ((l >> 5) * 576 + (l & 3) * 136 + ((l >> 2) & 7) * 16) * 4;
      int ovo = (l & 28) ? OOB : ocol * 2; float xo_ = xo;
      asm volatile("" : "+v"(gt), "+v"(frd), "+v"(ovo), "+v"(xo_));
      BAR();
      const v4f u0 = *(LAS v4f*)(L + frd), u1 = *(LAS v4f*)(L + frd + 16);
      const v4f u = (u0 + u1) + (*(LAS v4f*)(L + frd + 32) + *(LAS v4f*)(L + frd + 48));
      float v = (u[0] + u[2]) + (u[1] + u[3]);
      v = fold16(v, v); v += dpp<R8>(v); v += dpp<R4>(v);
      const float o = xo_ + rb(rb(v) * gt);
      __builtin_amdgcn_raw_buffer_store_b16((u16)pk(o, o), mk(out, 1024), ovo, 0, 16);
      if (b == 0 && l == 0) __hip_atomic_store((GAS unsigned*)ws, tag, __ATOMIC_RELAXED, __HIP_MEMORY_SCOPE_AGENT);
    }
    STAMP(10);
    FLUSH();
  } else {
    const int a = w - 1, a3 = a & 3;
    if (XS) {
      unsigned t = ep0;
      asm("v_writelane_b32 %0, %1, 0\n\t" "v_writelane_b32 %0, %2, 1\n\t" "v_writelane_b32 %0, %3, 2\n\t" "v_writelane_b32 %0, %4, 3\n\t" "v_writelane_b32 %0, %5, 4\n\t" "v_writelane_b32 %0, %6, 5\n\t" "v_writelane_b32 %0, %7, 6\n\t" "v_writelane_b32 %0, %8, 7\n\t" "v_writelane_b32 %0, %9, 8\n\t" "v_writelane_b32 %0, %10, 9\n\t" "v_writelane_b32 %0, %11, 10\n\t" "v_writelane_b32 %0, %12, 11\n\t" "v_writelane_b32 %0, %13, 12\n\t" "v_writelane_b32 %0, %14, 13\n\t" "v_writelane_b32 %0, %15, 14\n\t" "v_writelane_b32 %0, %16, 15\n\t" : "+v"(t) : "s"(xs0[0]), "s"(xs0[1]), "s"(xs0[2]), "s"(xs0[3]), "s"(xs0[4]), "s"(xs0[5]), "s"(xs0[6]), "s"(xs0[7]), "s"(xs0[8]), "s"(xs0[9]), "s"(xs0[10]), "s"(xs0[11]), "s"(xs0[12]), "s"(xs0[13]), "s"(xs0[14]), "s"(xs0[15]));
      asm("v_writelane_b32 %0, %1, 16\n\t" "v_writelane_b32 %0, %2, 17\n\t" "v_writelane_b32 %0, %3, 18\n\t" "v_writelane_b32 %0, %4, 19\n\t" "v_writelane_b32 %0, %5, 20\n\t" "v_writelane_b32 %0, %6, 21\n\t" "v_writelane_b32 %0, %7, 22\n\t" "v_writelane_b32 %0, %8, 23\n\t" "v_writelane_b32 %0, %9, 24\n\t" "v_writelane_b32 %0, %10, 25\n\t" "v_writelane_b32 %0, %11, 26\n\t" "v_writelane_b32 %0, %12, 27\n\t" "v_writelane_b32 %0, %13, 28\n\t" "v_writelane_b32 %0, %14, 29\n\t" "v_writelane_b32 %0, %15, 30\n\t" "v_writelane_b32 %0, %16, 31\n\t" : "+v"(t) : "s"(xs1[0]), "s"(xs1[1]), "s"(xs1[2]), "s"(xs1[3]), "s"(xs1[4]), "s"(xs1[5]), "s"(xs1[6]), "s"(xs1[7]), "s"(xs1[8]), "s"(xs1[9]), "s"(xs1[10]), "s"(xs1[11]), "s"(xs1[12]), "s"(xs1[13]), "s"(xs1[14]), "s"(xs1[15]));
      *(LAS unsigned*)(L + L_S + (l < 32 ? a * 128 + l * 4 : 1024)) = t;
      BAR();
      if (XS == 2) ep0 = *(const CAS unsigned*)ws;
    }
    int obo = (n & 3) == 0 ? (n >> 2) * 16384 + gq * 16 : OOB;
    int sbo = (l & 16) ? OOB : (l >> 5) * 16384 + (n >> 3) * 8192 + (n & 7) * 8;
    int obs = WS_OB + g * 65536 + a3 * 4096 + j * 128, sbs = WS_SB + ((j & 1) * 32 + a * 4) * 8192 + (g * 16 + (j >> 1)) * 64;
    int fbs = WS_FB + (kq * 16 + a * 2) * 4096 + (g * 8 + rb_) * 64;
    int sadr = L_S + (a & 1) * 1024 + gq * 256 + (n & 3) * 64 + (a >> 1) * 16 + (n >> 2) * 4, qadr = L_Q + (n & 3) * QSTR + gq * 64 + (n >> 2) * 16;
    int pso = WS_OB + gp * 65536 + (j >> 1) * 4096 + a3 * 1024, pvo = a >= 4 ? l * 16 : OOB;
    int oadr = L_O + n * 80 + gq * 16, owr = L_O + (a3 * 8 + (l >> 3)) * 80 + (l & 7) * 8;
    int mad = L_M + (n & 3) * 4, l8 = l * 8, mrd = L_M + (n >> 2) * 4;
    asm volatile("" : "+v"(mad), "+v"(l8), "+v"(mrd));
    int fso = WS_SB + b * 8192 + a * 1024, fadr = L_V + ((l & 3) * 136 + a * 16 + (l >> 2)) * 4;
    int vwr = L_V + (a * 16 + gq) * VSTR + n * 16;
    int qso = Q32 ? WS_QG + g * 16384 + a3 * 1024 : WS_QG + g * 8192 + a3 * 512, qvo = a >= 4 ? (Q32 ? l * 16 : (l >> 5) * 4096 + (l & 31) * 16) : OOB;
    int mxw = L_MX + gq * 128 + (n & 3) * 32 + a * 4, qwr = L_Q + a3 * QSTR + (Q32 ? l * 4 : (l & 31) * 8 + (l >> 5) * 4);
    int wov = (l >> 5) * 1024 + ((l & 31) ^ ((l >> 5) * 2)) * 16;
    asm volatile("" : "+v"(wov));
    int wos = (gp * 512 + (j >> 1) * 32 + a3 * 8) * 1024 + (j & 1) * 512, w2s = (gp * 224 + rb_ * 28 + a3 * 8) * 1024 + kq * 256, wvo = gq * 1024 + (n ^ (gq * 2)) * 16;
    int w2t = w2s + (a3 == 3 ? 0 : 4096);
    int wl0 = L_W + a3 * 4 * WBLK, wl1 = L_W + (16 + a3 * 2) * WBLK;
    v4u so_ = {(unsigned)(u64)wo, (unsigned)((u64)wo >> 32) & 0xFFFFu, 4096u * 512 * 2, 0x20000u}, s2_ = {(unsigned)(u64)w2, (unsigned)((u64)w2 >> 32) & 0xFFFFu, FF * 512u * 2, 0x20000u};
    asm volatile("" : "+v"(obo), "+v"(sbo), "+v"(sadr), "+v"(qadr), "+v"(oadr), "+v"(owr), "+v"(vwr), "+v"(fadr), "+v"(qvo), "+v"(mxw), "+v"(qwr), "+v"(pvo), "+v"(wvo));
    asm volatile("" : "+s"(obs), "+s"(sbs), "+s"(pso), "+s"(qso), "+s"(fso), "+s"(wos), "+s"(w2s), "+s"(so_), "+s"(s2_), "+s"(wl0), "+s"(wl1), "+s"(w2t));
    const rsrc_t rv = mk(vc, 8u * SK * 256);
    int tadr = L_V + (gq * 4 + (n >> 2)) * VSTR + a3 * 64 + (n & 3) * 8;
    asm volatile("" : "+v"(tadr));
    SB();
#define DMA1(VO, SRD, SO) "buffer_load_dwordx4 " VO ", " SRD ", " SO " offen nt lds\n\ts_add_u32 m0, m0, %8\n\ts_add_u32 " SO ", " SO ", 0x800\n\t"
#define WDMA() do { unsigned keep; asm volatile("s_mov_b32 %0, m0\n\ts_mov_b32 m0, %6\n\ts_nop 0\n\t" DMA1("%10", "%4", "%1") DMA1("%10", "%4", "%1") DMA1("%10", "%4", "%1") DMA1("%10", "%4", "%1") \
                   "s_mov_b32 m0, %7\n\ts_nop 0\n\t" DMA1("%3", "%5", "%2") "buffer_load_dwordx4 %3, %5, %9 offen nt lds\n\ts_mov_b32 m0, %0" \
                   : "=&s"(keep), "+s"(wos), "+s"(w2s) : "v"(wvo), "s"(so_), "s"(s2_), "s"(wl0), "s"(wl1), "i"(WBLK), "s"(w2t), "v"(wov) : "memory", "scc"); } while (0)
#define WDO() do { unsigned keep; asm volatile("s_mov_b32 %0, m0\n\ts_mov_b32 m0, %4\n\ts_nop 0\n\t" \
      "buffer_load_dwordx4 %2, %3, %1 offen nt lds\n\ts_add_u32 m0, m0, %5\n\ts_add_u32 %1, %1, 0x800\n\tbuffer_load_dwordx4 %2, %3, %1 offen nt lds\n\ts_add_u32 m0, m0, %5\n\ts_add_u32 %1, %1, 0x800\n\t" \
      "buffer_load_dwordx4 %2, %3, %1 offen nt lds\n\ts_add_u32 m0, m0, %5\n\ts_add_u32 %1, %1, 0x800\n\tbuffer_load_dwordx4 %2, %3, %1 offen nt lds\n\ts_mov_b32 m0, %0" \
      : "=&s"(keep), "+s"(wos) : "v"(wov), "s"(so_), "s"(wl0), "i"(WBLK) : "memory", "scc"); } while (0)
#define WD2() do { unsigned keep; asm volatile("s_mov_b32 %0, m0\n\ts_mov_b32 m0, %4\n\ts_nop 0\n\t" \
      "buffer_load_dwordx4 %2, %3, %1 offen nt lds\n\ts_add_u32 m0, m0, %5\n\ts_nop 0\n\tbuffer_load_dwordx4 %2, %3, %6 offen nt lds\n\ts_mov_b32 m0, %0" \
      : "=&s"(keep) : "s"(w2s), "v"(wvo), "s"(s2_), "s"(wl1), "i"(WBLK), "s"(w2t) : "memory", "scc"); } while (0)
    if (LK == 1) { const rsrc_t rk2 = mk(kc, 8u * SK * 256); RESTK(rk2); }
    if (LK == 2) { const rsrc_t rk2 = mk(kc, 8u * SK * 256); __builtin_amdgcn_s_barrier(); SB();
      if (a >= 4) {
#pragma unroll
        for (int k = KY; k < 4; ++k) kv[k] = ldw(rk2, kof + k * 256, kvo); } else if (VD) __builtin_amdgcn_s_sleep(VD);
      SB(); }
    EPOCH();
    v4u o0 = {0u, tag, 0u, tag}, o1 = o0; u64 lead = 0x000F000F000F000Full;
    asm volatile("" : "+v"(o0), "+v"(o1)); asm volatile("" : "+s"(lead));
    v4u vv[4];
    WAITV(3 - OT);
    SB();
#pragma unroll
    for (int i = 0; i < YV; ++i) vv[i] = ldw(rv, l16 + i * 1024, kvo);
    SB();
    if (a >= 4) {
      WAITV(3 - YT + YV);
      if (TQ) { while ((unsigned)__builtin_amdgcn_s_memrealtime() - t0 < TQ) __builtin_amdgcn_s_sleep(1); }
      __builtin_amdgcn_s_barrier();
    }
    SB();
    v4u q0, q1, q2, q3;
#define QLD() (q0 = LD16(qvo, qso), q1 = LD16(qvo, qso + 2048))
#define QBAD() (DBAD(q0) | DBAD(q1))
    QLD();
    SB();
    STAMP(9);
    if (a >= 4) __builtin_amdgcn_s_sleep(1);
#pragma unroll
    for (int i = YV; i < OV; ++i) vv[i] = ldw(rv, l16 + i * 1024, kvo);
    SB();
    if (a < 4) { __builtin_amdgcn_s_barrier(); if (OV < 4) __builtin_amdgcn_s_sleep(1); }
    SB();
#pragma unroll
    for (int i = OV; i < 4; ++i) vv[i] = ldw(rv, l16 + i * 1024, kvo);
    SB();
    WAITV(4 - YV);
    STAMP(2);
#define QSUM() do { const v4f u_ = __builtin_bit_cast(v4f, q0 & 0xFFFFFF00u) + __builtin_bit_cast(v4f, q1 & 0xFFFFFF00u); float c_, d_; const float a_ = sw32(u_[0], u_[2], c_), b_ = sw32(u_[1], u_[3], d_); \
        *(LAS unsigned*)(L + qwr) = pk(a_ + c_, b_ + d_); } while (0)
    if (a >= 4) {
      QSUM();
      SB();
      if (__builtin_expect(ANY(QBAD()), 0)) {
        UNTIL(QBAD(), QLD());
        QSUM();
      }
    }
    SB();
    if (a < 4) {
#pragma unroll
      for (int i = 0; i < 4; ++i) *(LAS v4u*)(L + vwr + i * 4 * VSTR) = vv[i];
      if (WP == 0) WDMA();
      if (WS == 5 || WS == 6) WD2();
    }
    const bool wnow = !TW || (unsigned)__builtin_amdgcn_s_memrealtime() - t0 >= TW;
    STAMP(4);
    BAR();
    {
      const v4u qf = *(LAS v4u*)(L + qadr);
      if (WS == 0 && WP == 1 && a < 4 && wnow) WDMA();
      if ((WS == 1 || WS == 3 || WS == 7) && a < 4) WD2();
      if ((WS == 2 || WS == 4) && a < 4) WDO();
      v4f sc[4];
#pragma unroll
      for (int i = 0; i < 4; ++i) { sc[i] = mfma(kv[i], qf, v4f{0.f, 0.f, 0.f, 0.f}); SB(); }
      float t, m;
#define DADD(src, acc, rot, bank) "v_add_f32_dpp %0, " src ", " acc " row_ror:" rot " row_mask:0xf bank_mask:" bank "\n\t"
      asm volatile("s_nop 4\n\t"
                   DADD("%3", "%2", "12", "0x1") DADD("%6", "%7", "4", "0x2") DADD("%10", "%12", "8", "0x4") DADD("%14", "%17", "12", "0x8")
                   DADD("%4", "%0", "8", "0x1") DADD("%8", "%0", "12", "0x2") DADD("%11", "%0", "4", "0x4") DADD("%15", "%0", "8", "0x8")
                   DADD("%5", "%0", "4", "0x1") DADD("%9", "%0", "8", "0x2") DADD("%13", "%0", "12", "0x4") DADD("%16", "%0", "4", "0x8")
                   "v_cvt_pk_bf16_f32 %0, 0, %0\n\tv_mul_f32 %0, 0x3db504f3, %0\n\tv_cvt_pk_bf16_f32 %0, 0, %0\n\tv_mul_f32 %0, 0x3fb8aa3b, %0\n\ts_nop 1\n\t"
                   "v_max_f32_dpp %1, %0, %0 row_ror:8 row_mask:0xf bank_mask:0xf\n\ts_nop 1\n\t"
                   "v_max_f32_dpp %1, %1, %1 row_ror:4 row_mask:0xf bank_mask:0xf\n\t"
                   "v_sub_f32 %0, %0, %1\n\tv_exp_f32 %0, %0\n\t"
                   "s_mov_b64 exec, %21\n\tds_max_f32 %18, %1\n\tds_write_b32 %19, %1\n\ts_mov_b64 exec, -1\n\tds_write_b32 %20, %0"
                   : "=&v"(t), "=&v"(m)
                   : "v"(sc[0][0]), "v"(sc[0][1]), "v"(sc[0][2]), "v"(sc[0][3]), "v"(sc[1][0]), "v"(sc[1][1]), "v"(sc[1][2]), "v"(sc[1][3]),
                     "v"(sc[2][0]), "v"(sc[2][1]), "v"(sc[2][2]), "v"(sc[2][3]), "v"(sc[3][0]), "v"(sc[3][1]), "v"(sc[3][2]), "v"(sc[3][3]),
                     "v"(mad), "v"(mxw), "v"(sadr), "s"(lead)
                   : "memory");
#pragma unroll
      for (int i = 0; i < 5; ++i) __builtin_amdgcn_s_setprio(2);
    }
    v2u vt[16];
    if (a < 4) {
#pragma unroll
      for (int i = 0; i < 8; ++i) vt[(i & 3) + (i >> 2) * 8] = trd(L + tadr + (i & 3) * 16 * VSTR + (i >> 2) * 32);
      if (WP == 2) WDMA();
      if (WS == 6 || WS == 7) WDO();
    }
    STAMP(5);
    BAR();
    if (a < 4) {
      const v2u mm = *(LAS v2u*)(L + L_MX + l8);
      const float m = *(LAS float*)(L + mrd);
      const v4f s0 = *(LAS v4f*)(L + L_S + l16), s1 = *(LAS v4f*)(L + L_S + 1024 + l16);
      const v4f p0 = s0 * ex2(uf(mm[0]) - m), p1 = s1 * ex2(uf(mm[1]) - m);
      const v4u pb = {pk(p0[0], p0[1]), pk(p0[2], p0[3]), pk(p1[0], p1[1]), pk(p1[2], p1[3])};
      const v4f z = {0.f, 0.f, 0.f, 0.f};
      v4f d0 = mfma(cat(vt[0], vt[1]), pb, z), d1 = mfma(cat(vt[8], vt[9]), pb, z);
      const v4u pb1 = dpp4<0x55>(pb), pb2 = dpp4<0xAA>(pb);
      SB();
      d0 = mfma(cat(vt[2], vt[3]), pb1, d0); d1 = mfma(cat(vt[10], vt[11]), pb1, d1);
      v4u pb3 = dpp4<0xFF>(pb);
      asm volatile("" : "+v"(pb3) : "v"(pb1), "v"(vt[0]), "v"(vt[1]), "v"(vt[8]), "v"(vt[9]));
      SB();
      if (WS == 1 || WS == 5) WDO();
      if (WS == 2) WD2();
      STAMP(7);
      BAR();
#pragma unroll
      for (int i = 0; i < 8; ++i) vt[4 + (i & 1) + ((i >> 2) & 1) * 2 + ((i >> 1) & 1) * 8] = trd(L + tadr + (4 + (i & 1) + ((i >> 2) & 1) * 2) * 16 * VSTR + ((i >> 1) & 1) * 32);
      d0 = mfma(cat(vt[4], vt[5]), pb2, d0); d1 = mfma(cat(vt[12], vt[13]), pb2, d1);
      d0 = mfma(cat(vt[6], vt[7]), pb3, d0); d1 = mfma(cat(vt[14], vt[15]), pb3, d1);
      o0[0] = pk(d0[0], d0[1]); o0[2] = pk(d0[2], d0[3]); o1[0] = pk(d1[0], d1[1]); o1[2] = pk(d1[2], d1[3]);
      ST16(o0, obo, obs);
      ST16(o1, obo + 64, obs);
      STAMP(6);
      if (WS == 3) { WDO(); asm volatile("s_waitcnt vmcnt(0)" ::: "memory"); } else if (WS == 4) { WD2(); asm volatile("s_waitcnt vmcnt(0)" ::: "memory"); }
      else if (WP == 3 || !wnow) { if (TW) while ((unsigned)__builtin_amdgcn_s_memrealtime() - t0 < TX) __builtin_amdgcn_s_sleep(1); WDMA(); asm volatile("s_waitcnt vmcnt(0)" ::: "memory"); }
      else asm volatile("s_waitcnt vmcnt(2)" ::: "memory");
      STAMP(1);
    }
    SB();
    if (a >= 4) {
#pragma unroll
      for (int i = 0; i < 4; ++i) *(LAS v4u*)(L + vwr + i * 4 * VSTR) = vv[i];
      STAMP(3);
      BAR();
      __builtin_amdgcn_s_sleep(PD);
    }
    SB();
    v4u pv = LD16(pvo, pso);
    BAR();
    if (a >= 4 && ANY(TBAD(pv))) pv = LD16(pvo, pso);
    SB();
    v2u wt[4];
    const int wrd = L_W + gq * 2 * WBLK + (n >> 2) * 256 + (a ^ (n >> 2)) * 32 + (n & 3) * 8;
#pragma unroll
    for (int i = 0; i < 4; ++i) wt[i] = trd(L + L_W + (gq * 4 + (i & 1) * 2 + (n >> 3)) * WBLK + ((n >> 2) & 1) * 512 + ((a * 2 + (i >> 1)) ^ ((n >> 2) & 1)) * 32 + (n & 3) * 8);
    SB();
    WAITV(0);
    if (a < 4) {
      const v4u hf = *(LAS v4u*)(L + L_H + gq * 16);
      LAS unsigned char* p = L + wrd + 16 * WBLK;
      const v4f z = {0.f, 0.f, 0.f, 0.f};
      const v4f f0 = mfma(hf, cat(trd(p), trd(p + WBLK)), z), f1 = mfma(hf, cat(trd(p + 128), trd(p + 128 + WBLK)), z);
      ST8((v2u{__float_as_uint(gq ? f1[0] : f0[0]), tag}), l < 32 ? gq * 32768 + (n >> 3) * 4096 + (n & 7) * 8 : OOB, fbs);
    }
    SB();
    if (a >= 4) {
#define OWR() (((LAS unsigned*)(L + owr))[0] = pv[0], ((LAS unsigned*)(L + owr))[1] = pv[2])
      OWR();
      UNTIL(TBAD(pv), (pv = LD16(pvo, pso), OWR()));
      STAMP(8);
    }
    BAR();
    {
      const v4u of0 = *(LAS v4u*)(L + oadr), of1 = *(LAS v4u*)(L + oadr + 16 * 80);
      const v4f w0 = *(LAS v4f*)(L + L_WG + gq * 16), w1_ = *(LAS v4f*)(L + L_WG + 64 + gq * 16);
      SB();
      const v4f z = {0.f, 0.f, 0.f, 0.f};
      const v4f d00 = mfma(of0, cat(wt[0], wt[1]), z), d01 = mfma(of0, cat(wt[2], wt[3]), z), d10 = mfma(of1, cat(wt[0], wt[1]), z), d11 = mfma(of1, cat(wt[2], wt[3]), z);
      SB();
      v2f c0 = d00.hi * w0.hi + d00.lo * w0.lo, c1 = d01.hi * w0.hi + d01.lo * w0.lo;
      c0 = d10.hi * w1_.hi + (d10.lo * w1_.lo + c0); c1 = d11.hi * w1_.hi + (d11.lo * w1_.lo + c1);
      float e = fold32(c0[0] + c0[1], c1[0] + c1[1]);
      e = fold16(e, e);
      ST8((v2u{__float_as_uint(e), tag}), sbo, sbs);
    }
    STAMP(10);
    if (b < 64) {
      __builtin_amdgcn_s_sleep(10); if (a == g) __builtin_amdgcn_s_sleep(10);
      v4u sv = LD16(l16, fso);
#define SWR() (((LAS unsigned*)(L + fadr))[0] = sv[0], ((LAS unsigned*)(L + fadr))[576] = sv[2])
      SWR();
      UNTIL(TBAD(sv), (sv = LD16(l16, fso), SWR()));
      BAR();
      if (a == 0) { const unsigned e_ = (unsigned)__builtin_amdgcn_s_memrealtime(), d_ = 4 * (e_ - c0_); while ((unsigned)__builtin_amdgcn_s_memrealtime() - e_ < d_ && d_ < 100000u) {} }
    }
  }
}

extern "C" void kernel_launch(void* const* d_in, const int* in_sizes, int n_in, void* d_out, int out_size, void* d_ws, size_t ws_size, hipStream_t stream) {
  fused_decode<<<256, NTHR, 0, stream>>>((const u16*)d_in[0], (const u16*)d_in[3], (const u16*)d_in[6], (const u16*)d_in[2], (unsigned char*)d_ws, (const u16*)d_in[1],
                                         (const u16*)d_in[4], (const u16*)d_in[8], (const u16*)d_in[7], (const u16*)d_in[9], (u16*)d_out);
}
```

```cpp
#define LK 1
#define KY 2
#define YT 2
#define PD 10
#define XQ 0
#define XO 4
#define WS 1
#define MLD 8
#define DUMMYb 1
#include <hip/hip_runtime.h>
#include <hip/hip_bf16.h>

typedef unsigned short u16;
typedef unsigned long long u64;
typedef unsigned v4u __attribute__((ext_vector_type(4)));
typedef unsigned v16u __attribute__((ext_vector_type(16)));
typedef unsigned v2u __attribute__((ext_vector_type(2)));
typedef float v4f __attribute__((ext_vector_type(4)));
typedef short s16x4 __attribute__((ext_vector_type(4)));
typedef __bf16 bf16x2 __attribute__((ext_vector_type(2)));
typedef __bf16 bf16x8 __attribute__((ext_vector_type(8)));
typedef __amdgpu_buffer_rsrc_t rsrc_t;
#define GAS __attribute__((address_space(1)))
#define CAS __attribute__((address_space(4)))
#define LAS __attribute__((address_space(3)))

#define TQ 0
#define YV 0
#define OV 4
#define OT 0
#define EV 1
#define TW 0
#define TX TW
#define Q32 0
#define PFQ 1
#define SH 0
#define FORCE -1
#define VD 2
#define ASL 0
#define PFA 0
#define TLB 0
#define XS 0
#define WP 1
#define STAMP(i)
#define FLUSH()
constexpr int SK = 32768, WIN = 4096, FF = 1792;
constexpr int NTHR = 576;
constexpr unsigned WS_QG = 4096;
constexpr unsigned WS_HG = WS_QG + 131072;
constexpr unsigned WS_ML = WS_HG + 57344;
constexpr unsigned WS_OB = WS_ML + 16384;
constexpr unsigned WS_SB = WS_OB + 524288;
constexpr unsigned WS_FB = WS_SB + 524288;
constexpr unsigned WS_END = WS_FB + 262144;
constexpr int VSTR = 288, L_V = 0, L_O = 128 * VSTR, L_Q = L_O + 4096, QSTR = 320, L_S = L_Q + 4 * QSTR, L_XN = (L_S + 2048 + 1023) / 1024 * 1024, L_MX = L_XN + 1024, L_M = L_MX + 512, L_H = L_M + 32,
              L_WG = L_H + 64, L_W = L_WG + 128, WBLK = 1088, L_END = L_W + 24 * WBLK;
static_assert(L_O % 128 == 0, "the o image is addressed with ^ 32 and ^ 64");
constexpr unsigned SPIN_CAP = 1u << 18;
constexpr float LOG2E = 1.4426950408889634f;
constexpr int OOB = 0x40000000;

__device__ __forceinline__ rsrc_t mk(const void* p, unsigned bytes) { return __builtin_amdgcn_make_buffer_rsrc((void*)p, 0, bytes, 0x00020000); }
__device__ __forceinline__ v4u ldw(rsrc_t r, int voff, int soff) { return __builtin_amdgcn_raw_buffer_load_b128(r, voff, soff, 2); }
__device__ __forceinline__ float uf(unsigned u) { return __uint_as_float(u); }
__device__ __forceinline__ float bflo(unsigned u) { return __uint_as_float(u << 16); }
__device__ __forceinline__ float bfhi(unsigned u) { return __uint_as_float(u & 0xFFFF0000u); }
__device__ __forceinline__ unsigned pk(float lo, float hi) { bf16x2 r = {(__bf16)lo, (__bf16)hi}; return __builtin_bit_cast(unsigned, r); }
typedef float v2f __attribute__((ext_vector_type(2)));
__device__ __forceinline__ float rb(float f) { return __builtin_bit_cast(float, __builtin_convertvector((v2f{0.f, f}), bf16x2)); }
__device__ __forceinline__ v4f mfma(v4u a, v4u b, v4f c) { return __builtin_amdgcn_mfma_f32_16x16x32_bf16(__builtin_bit_cast(bf16x8, a), __builtin_bit_cast(bf16x8, b), c, 0, 0, 0); }
template <int CTRL> __device__ __forceinline__ unsigned dppu(unsigned x) { return (unsigned)__builtin_amdgcn_mov_dpp((int)x, CTRL, 0xf, 0xf, true); }
template <int CTRL> __device__ __forceinline__ float dpp(float x) { return __uint_as_float(dppu<CTRL>(__float_as_uint(x))); }
template <int CTRL> __device__ __forceinline__ v4u dpp4(v4u x) { return v4u{dppu<CTRL>(x[0]), dppu<CTRL>(x[1]), dppu<CTRL>(x[2]), dppu<CTRL>(x[3])}; }
constexpr int X1 = 0xB1, X2 = 0x4E, HM = 0x141, R4 = 0x124, R8 = 0x128;
__device__ __forceinline__ float sw16(float a, float b, float& o) { auto r = __builtin_amdgcn_permlane16_swap(__float_as_uint(a), __float_as_uint(b), false, false); o = uf(r[1]); return uf(r[0]); }
__device__ __forceinline__ float sw32(float a, float b, float& o) { auto r = __builtin_amdgcn_permlane32_swap(__float_as_uint(a), __float_as_uint(b), false, false); o = uf(r[1]); return uf(r[0]); }
__device__ __forceinline__ float fold16(float a, float b) { float o; const float p = sw16(a, b, o); return p + o; }
__device__ __forceinline__ float fold32(float a, float b) { float o; const float p = sw32(a, b, o); return p + o; }
__device__ __forceinline__ float fmx(float a, float b) { return __builtin_elementwise_maximum(a, b); }
__device__ __forceinline__ float max16(float a) { float o; const float p = sw16(a, a, o); return fmx(p, o); }
#define MAXDPP(name, ctl) __device__ __forceinline__ float name(float m) { float r; asm("s_nop 1\n\tv_max_f32_dpp %0, %1, %1 " ctl " row_mask:0xf bank_mask:0xf" : "=v"(r) : "v"(m)); return r; }
MAXDPP(mx_r8, "row_ror:8") MAXDPP(mx_r4, "row_ror:4") MAXDPP(mx_x1, "quad_perm:[1,0,3,2]") MAXDPP(mx_x2, "quad_perm:[2,3,0,1]")
__device__ __forceinline__ float sum64(float v) { v += dpp<X1>(v); v += dpp<X2>(v); v += dpp<HM>(v); v += dpp<R8>(v); v = fold16(v, v); return fold32(v, v); }
__device__ __forceinline__ v2u trd(LAS unsigned char* p) { return __builtin_bit_cast(v2u, __builtin_amdgcn_ds_read_tr16_b64_v4i16((LAS s16x4*)p)); }
__device__ __forceinline__ v4u cat(v2u a, v2u b) { return v4u{a[0], a[1], b[0], b[1]}; }
__device__ __forceinline__ float ex2(float x) { return __builtin_amdgcn_exp2f(x); }
__device__ __forceinline__ float rcp(float x) { return __builtin_amdgcn_rcpf(x); }
__device__ __forceinline__ float gval(unsigned d) { return __uint_as_float(d & 0xFFFFFF00u); }
__device__ __forceinline__ unsigned d4(float f, unsigned tagbyte) { return ((__float_as_uint(f) + 0x80u) & 0xFFFFFF00u) | tagbyte; }
#define SB() __builtin_amdgcn_sched_barrier(0)
#define BAR() do { asm volatile("s_waitcnt lgkmcnt(0)" ::: "memory"); __builtin_amdgcn_s_barrier(); asm volatile("" ::: "memory"); } while (0)
#define FENCE() asm volatile("" ::: "memory")
#define WAITV(n) __builtin_amdgcn_s_waitcnt(0x0F70 | (n))
#define LD16(off, soff) __builtin_amdgcn_raw_buffer_load_b128(rs, (off), (soff), 16)
#define ST16(v, off, soff) __builtin_amdgcn_raw_buffer_store_b128((v), rs, (off), (soff), 16)
#define ST8(v, off, soff) __builtin_amdgcn_raw_buffer_store_b64((v), rs, (off), (soff), 16)
#define TBAD(v) __builtin_amdgcn_bitop3_b32((v)[1], (v)[3], tag, 0x7E)
#define ANY(t) (__builtin_amdgcn_ballot_w64((t) != 0) != 0)
#define DBAD(v) __builtin_amdgcn_bitop3_b32(__builtin_amdgcn_perm((v)[1], (v)[0], 0x0c0c0400u), __builtin_amdgcn_perm((v)[3], (v)[2], 0x04000c0cu), tag, 0x56)
#define EPOCH() asm volatile("" : "+s"(ep0)); const unsigned tag = ((ep0 & 0x7F7F7F7Fu) + 0x01010101u) ^ (ep0 & 0x80808080u)
#define UNTIL(BADEXPR, RELOAD) do { if (ANY(BADEXPR)) { int spins_ = SPIN_CAP; _Pragma("nounroll") do { FENCE(); RELOAD; if (!ANY(BADEXPR)) break; asm volatile("" : "+s"(spins_)); } while (--spins_); } } while (0)

__device__ __forceinline__ void mksel(unsigned (&sel)[8], int l) {
#pragma unroll
  for (int d = 0; d < 4; ++d) { const bool on = ((l & 7) >> 1) == d;
    sel[d] = on ? ((l & 1) ? 0x01000c0cu : 0x0c0c0100u) : 0x0c0c0c0cu; sel[4 + d] = on ? ((l & 1) ? 0x03020c0cu : 0x0c0c0302u) : 0x0c0c0c0cu; }
}
__device__ __forceinline__ v4u diag(unsigned pair, const unsigned (&sel)[8], int hi) {
  return v4u{__builtin_amdgcn_perm(0u, pair, sel[4 * hi]), __builtin_amdgcn_perm(0u, pair, sel[4 * hi + 1]), __builtin_amdgcn_perm(0u, pair, sel[4 * hi + 2]), __builtin_amdgcn_perm(0u, pair, sel[4 * hi + 3])};
}
__device__ __forceinline__ float upr8(float v) { return __builtin_bit_cast(float, __builtin_amdgcn_update_dpp(__builtin_bit_cast(int, v), __builtin_bit_cast(int, v), R8, 0xC, 0xF, false)); }
__device__ __forceinline__ v2u diag_out(v4f d, unsigned tb0, unsigned tb1) {
  const float t0 = upr8(d[0]), t1 = upr8(d[1]), t2 = upr8(d[2]), t3 = upr8(d[3]);
  return v2u{d4(fold32(t0, t2), tb0), d4(fold32(t1, t3), tb1)};
}

__global__ void __launch_bounds__(NTHR, 3) fused_decode(const u16* __restrict__ x, const u16* __restrict__ wq, const u16* __restrict__ w1, const u16* __restrict__ vc, unsigned char* ws,
                                                        const u16* __restrict__ kc, const u16* __restrict__ wo, const u16* __restrict__ w2, const u16* __restrict__ b1,
                                                        const u16* __restrict__ b2, u16* __restrict__ out) {
  __shared__ __attribute__((aligned(128))) unsigned char lds_[L_END];
  LAS unsigned char* L = (LAS unsigned char*)lds_;
  const int b = blockIdx.x, g = b & 7, j = b >> 3, l = threadIdx.x & 63;
  int w = __builtin_amdgcn_readfirstlane(threadIdx.x >> 6);
  const unsigned t0 = (TQ | TW) ? (unsigned)__builtin_amdgcn_s_memrealtime() : 0u;
  const unsigned c0_ = (unsigned)__builtin_amdgcn_s_memrealtime();
  { u64 pc; unsigned keep, t_;
    asm volatile("s_cmp_lg_u32 %3, 4\n\ts_cbranch_scc1 1f\n\ts_getpc_b64 %0\n\ts_mov_b32 %1, m0\n\ts_mov_b32 m0, %5\n\tv_lshlrev_b32 %2, 7, %4\n\t"
                 "global_load_lds_dword %2, %0\n\ts_mov_b32 m0, %1\n1:"
                 : "=&s"(pc), "=&s"(keep), "=&v"(t_) : "s"(w), "v"(PFA == 1 ? l + 1 : PFA == 2 ? ((l < 16) ? 8 + l : min((j * 5 + (l & 7) % 5) % 80, 64)) : min((j * 5 + (l & 7) % 5) % 80, SH ? 70 : 64)), "i"(L_O) : "scc", "memory"); }
  const int kq = j & 3, rb_ = j >> 2, gn = (g + XQ) & 7, gp = (g - XO) & 7;
  const int gq = l >> 4, n = l & 15;
  const bool hasw1 = rb_ != 3; const int jw = rb_ == 7 ? j - 16 : j;
  int kvo = (g * SK + (SK - WIN) + j * 128 + (((w - 1) * 2 + ((w - 1) >> 2) + j) & 7) * 16) * 256, l16 = l * 16, kof = (n >> 2) * 1024 + gq * 64 + (n & 3) * 16;
  int vo = (l >> 3) * (16 * 8192) + (l & 7) * 16, so = kq * (128 * 8192) + gn * 1024 + rb_ * 128;
  const bool early = !SH || (FORCE >= 0 ? FORCE == 1 : (__builtin_amdgcn_s_getreg(20 | (3 << 11)) & 4) == 0);
  int vo1 = (hasw1 && early) ? (l >> 3) * (8 * FF * 2) + (l & 7) * 16 : OOB, so1 = g * (64 * FF * 2) + jw * 128;
  asm volatile("" : "+s"(kvo), "+s"(so), "+s"(so1));
  asm volatile("s_setprio 3" : "+v"(l16), "+v"(vo), "+v"(vo1), "+v"(kof));
  asm volatile("" :: "s"(x), "s"(kc), "s"(vc), "s"(wq), "s"(wo), "s"(w1), "s"(b1), "s"(w2), "s"(b2), "s"(out), "s"(ws));
  unsigned ep0 = 0; if (!EV) ep0 = *(const CAS unsigned*)ws;
  const rsrc_t rs = mk(ws, WS_END);
  v4u kv[4]; v16u xs0, xs1;
  if (w != 0) {
    const rsrc_t rk = mk(kc, 8u * SK * 256);
    if (PFQ && w == 3) {
      unsigned keep;
      const unsigned po = PFQ == 1 ? (j * 16 + 8 + (l & 7)) * 8192 + gn * 1024 + 384 + ((l >> 3) & 1) * 512
                        : PFQ == 2 ? (j * 16 + (l & 15)) * 8192 + gn * 1024 + 384 + ((l >> 4) & 1) * 512
                        : PFQ == 3 ? (j * 16 + 4 + (l & 15) % 12) * 8192 + gn * 1024 + 384
                        : (j * 16 + 8 + (l & 7)) * 8192 + gn * 1024 + 128 + ((l >> 3) & 3) * 256;
      asm volatile("s_mov_b32 %0, m0\n\ts_mov_b32 m0, %3\n\ts_nop 0\n\tglobal_load_lds_dword %1, %2 nt\n\ts_mov_b32 m0, %0" : "=&s"(keep) : "v"(po), "s"(wq), "i"(L_O + 256) : "memory");
    }
    if (TLB && w == 2) {
      unsigned keep; const int k = l & 7;
      const u64 ad = k == 0 ? (u64)vc + (unsigned)kvo : k == 1 ? (u64)wo + (gp * 512 + (j >> 1) * 32) * 1024 : k == 2 ? (u64)w2 + (gp * 224 + rb_ * 28) * 1024 : k == 3 ? (u64)b1 : k == 4 ? (u64)b2
                   : k == 5 ? (u64)ws + WS_END : k == 6 ? (u64)kc + (unsigned)kvo : (u64)out;
      asm volatile("s_mov_b32 %0, m0\n\ts_mov_b32 m0, %2\n\ts_nop 0\n\tglobal_load_lds_dword %1, off nt\n\ts_mov_b32 m0, %0" : "=&s"(keep) : "v"(ad), "i"(L_O + 512) : "memory");
    }
    if (XS) { const CAS v16u* xp = (const CAS v16u*)x + (w - 1) * 2; xs0 = xp[0]; xs1 = xp[1]; }
    if (EV && XS != 2) ep0 = *(const CAS unsigned*)ws;
    __builtin_amdgcn_s_setprio(2);
    __builtin_amdgcn_s_barrier();
    SB();
#pragma unroll
    for (int k = 0; k < KY; ++k) kv[k] = ldw(rk, kof + k * 256, kvo);
    SB();
#define RESTK(RK) do { \
    if (KY < 4 && w > 4) __builtin_amdgcn_s_barrier(); \
    SB(); \
    _Pragma("unroll") \
    for (int k = KY; k < 4; ++k) kv[k] = ldw(RK, kof + k * 256, kvo); \
    SB(); \
    if (KY < 4 && w <= 4) __builtin_amdgcn_s_barrier(); \
 } while (0)
    if (ASL) __builtin_amdgcn_s_sleep(ASL);
    if (!LK) RESTK(rk);
    if (LK == 2 && w <= 4) {
#pragma unroll
      for (int k = KY; k < 4; ++k) kv[k] = ldw(rk, kof + k * 256, kvo); }
  }
  asm volatile("" : "+s"(w));
  if (__builtin_expect(w == 0, 1)) {
    v4u r16[16], f8[8];
    v4u xv; unsigned epv = 0;
    if (!XS) { xv = *(const GAS v4u*)((const GAS unsigned char*)x + (unsigned)l16); epv = EV ? __builtin_amdgcn_raw_buffer_load_b32(rs, 0, 0, 0) : 0u; }
    { const rsrc_t r = mk(wq, 512 * 4096 * 2), r1 = mk(w1, 512 * FF * 2);
#pragma unroll
      for (int i = 0; i < 16; ++i) r16[i] = ldw(r, vo, so + i * 8192);
      if (XS == 2) { SB(); epv = __builtin_amdgcn_raw_buffer_load_b32(rs, 0, 0, 2); SB(); }
#pragma unroll
      for (int i = 0; i < 8; ++i) f8[i] = ldw(r1, vo1, so1 + i * (FF * 2)); }
    SB();
    __builtin_amdgcn_s_barrier();
    SB();
    unsigned sel[8]; mksel(sel, l);
    int gvo = (l & 8) ? OOB : (l & 7) * 32 + (l & 16) + (l >> 5) * 8, xra = L_XN + kq * 256 + (l >> 3) * 32, xrc = L_XN + g * 128 + (l >> 3) * 16;
    asm volatile("" : "+v"(sel[0]), "+v"(sel[1]), "+v"(sel[2]), "+v"(sel[3]), "+v"(sel[4]), "+v"(sel[5]), "+v"(sel[6]), "+v"(sel[7]), "+v"(gvo), "+v"(xra), "+v"(xrc));
    SB();
    if (XS) { BAR(); xv = *(LAS v4u*)(L + L_S + l16); if (XS == 1) epv = *(LAS unsigned*)(L + L_S + 1024); STAMP(5); }
    float xf[8];
#pragma unroll
    for (int i = 0; i < 4; ++i) { xf[2 * i] = bflo(xv[i]); xf[2 * i + 1] = bfhi(xv[i]); }
    const float mean = sum64(((xf[0] + xf[1]) + (xf[2] + xf[3])) + ((xf[4] + xf[5]) + (xf[6] + xf[7]))) * (1.f / 512);
    float vs = 0.f;
#pragma unroll
    for (int i = 0; i < 8; ++i) { const float d = xf[i] - mean; vs = fmaf(d, d, vs); }
    const float var = sum64(vs) * (1.f / 512);
    const float mb = rb(mean), rsd = rcp(rb(__builtin_amdgcn_sqrtf(rb(rb(var) + 1e-5f))));
    v4u xn;
#pragma unroll
    for (int i = 0; i < 4; ++i) { const unsigned d = pk(xf[2 * i] - mb, xf[2 * i + 1] - mb); xn[i] = pk(bflo(d) * rsd, bfhi(d) * rsd); }
    *(LAS v4u*)(L + L_XN + l * 16) = xn;
    *(LAS float*)(L + L_M + (l & 3) * 4) = -__builtin_inff();
    const v4u xa = *(LAS v4u*)(L + xra), xb = *(LAS v4u*)(L + xra + 16), xc = *(LAS v4u*)(L + xrc);
    unsigned tag, tb0, tb1;
#define MKTAG() do { if (EV) ep0 = __builtin_amdgcn_readfirstlane(epv); asm volatile("" : "+s"(ep0)); tag = ((ep0 & 0x7F7F7F7Fu) + 0x01010101u) ^ (ep0 & 0x80808080u); tb0 = (tag >> ((l >> 5) * 16)) & 0xFFu; tb1 = (tag >> ((l >> 5) * 16 + 8)) & 0xFFu; } while (0)
    if (XS != 2) MKTAG();
    { v4u bx[8];
#pragma unroll
      for (int i = 0; i < 8; ++i) bx[i] = diag(xa[i >> 1], sel, i & 1);
      SB();
      v4f d = {0.f, 0.f, 0.f, 0.f};
#pragma unroll
      for (int i = 0; i < 8; ++i) { d = mfma(bx[i], r16[i], d); bx[i] = diag(xb[i >> 1], sel, i & 1); SB(); }
#pragma unroll
      for (int i = 0; i < 8; ++i) d = mfma(bx[i], r16[8 + i], d);
      SB();
      if (XS == 2) MKTAG();
      ST8(diag_out(d, tb0, tb1), gvo, WS_QG + gn * 8192 + kq * 2048 + rb_ * 256);
      STAMP(3);
      SB();
      if (SH) { const rsrc_t r1 = mk(w1, 512 * FF * 2);
#pragma unroll
        for (int i = 0; i < 8; ++i) r16[i] = ldw(r1, vo1, so1 + ((g ^ 4) - g) * (64 * FF * 2) + i * (FF * 2)); }
      SB();
      if (KY < 4) __builtin_amdgcn_s_barrier();
      SB();
    }
    { v4f d = {0.f, 0.f, 0.f, 0.f};
#pragma unroll
      for (int i = 0; i < 8; ++i) d = mfma(diag(xc[i >> 1], sel, i & 1), f8[i], d);
      ST8(diag_out(d, tb0, tb1), (hasw1 && early) ? gvo : OOB, WS_HG + g * (FF * 4) + jw * 256); }
    const int hr0 = gp * 224 + rb_ * 28, hu = min(l & 7, 6), ocol = (b & 63) * 8 + (l & 3) * 2 + (l >> 5);
    int mlo = (l & 51) == 0 ? (n >> 2) * 512 : OOB, mls = WS_ML + g * 2048 + j * 16, hvo = (l >> 3) * (FF * 4) + hu * 16, hso = WS_HG + hr0 * 4;
    int pvo = (l & 31) * 16, pso = WS_ML + gp * 2048 + (j >> 3) * 512;
    asm volatile("" : "+v"(mlo), "+v"(hvo), "+v"(pvo));
    asm volatile("" : "+s"(mls), "+s"(hso), "+s"(pso));
    const float b1v = bflo(b1[hr0 + hu * 4 + gq]), b2v = bflo(b2[ocol]), xo = bflo(x[ocol]);
    if (SH) { const v4u xd = *(LAS v4u*)(L + (xrc ^ 512)); v4f d = {0.f, 0.f, 0.f, 0.f};
#pragma unroll
      for (int i = 0; i < 8; ++i) d = mfma(diag(xd[i >> 1], sel, i & 1), r16[i], d);
      ST8(diag_out(d, tb0, tb1), (hasw1 && early) ? gvo : OOB, WS_HG + (g ^ 4) * (FF * 4) + jw * 256); }
    BAR();
    BAR();
    v4u hg; if (!SH) hg = LD16(hvo, hso);
    SB();
    BAR();
    { const v2u mm = *(LAS v2u*)(L + L_MX + l * 8);
      const v4f s0 = *(LAS v4f*)(L + L_S + l * 16), s1 = *(LAS v4f*)(L + L_S + 1024 + l * 16);
      const float m = *(LAS float*)(L + L_M + (n >> 2) * 4);
      float ls = fmaf((s0[0] + s0[1]) + (s0[2] + s0[3]), ex2(uf(mm[0]) - m), ((s1[0] + s1[1]) + (s1[2] + s1[3])) * ex2(uf(mm[1]) - m));
      ls += dpp<X1>(ls); ls += dpp<X2>(ls); ls = fold16(ls, ls); ls = fold32(ls, ls);
      ST16((v4u{__float_as_uint(m), tag, __float_as_uint(ls), tag}), mlo, mls);
    }
    STAMP(6);
    v4u own;
    if (SH) { own = LD16(n * 16, WS_HG + g * (FF * 4) + jw * 256); hg = LD16(hvo, hso); SB(); }
    BAR();
    if (SH && __builtin_expect(hasw1 && !early && ANY(DBAD(own)), 0)) { int tries = 32;
      _Pragma("nounroll") do { FENCE(); own = LD16(n * 16, WS_HG + g * (FF * 4) + jw * 256); } while (ANY(DBAD(own)) && --tries);
      if (!tries) {
      const rsrc_t r1 = mk(w1, 512 * FF * 2); v4f d = {0.f, 0.f, 0.f, 0.f};
#pragma unroll
      for (int i = 0; i < 8; ++i) f8[i] = ldw(r1, (l >> 3) * (8 * FF * 2) + (l & 7) * 16, so1 + i * (FF * 2));
#pragma unroll
      for (int i = 0; i < 8; ++i) d = mfma(diag(xc[i >> 1], sel, i & 1), f8[i], d);
      ST8(diag_out(d, tb0, tb1), gvo, WS_HG + g * (FF * 4) + jw * 256);
    } }
    {
      if (__builtin_expect(ANY(DBAD(hg)), 0)) UNTIL(DBAD(hg), hg = LD16(hvo, hso));
      float hs = fold16(fold32(gval(hg[0]), gval(hg[2])), fold32(gval(hg[1]), gval(hg[3]))); hs += dpp<R8>(hs);
      const float hp = rb(rb(hs) + b1v);
      const float u2 = 1.5957691216057308f * fmaf(0.044715f * hp * hp, hp, hp);
      const float ge = hp * rcp(1.f + ex2(-u2 * LOG2E));
      *(LAS u16*)(L + L_H + (l & 7) * 8 + gq * 2) = ((l & 7) < 7) ? (u16)pk(ge, ge) : (u16)0;
    }
    __builtin_amdgcn_s_sleep(MLD);
    v4u pv = LD16(pvo, pso);
    SB();
    BAR();
    {
      UNTIL(TBAD(pv), pv = LD16(pvo, pso));
      const float m = uf(pv[0]);
      const float M = max16(mx_r4(mx_r8(mx_x2(mx_x1(m)))));
      const float e = ex2(m - M);
      float ls = e * uf(pv[2]);
      ls += dpp<X1>(ls); ls += dpp<X2>(ls); ls += dpp<HM>(ls); ls += dpp<R8>(ls); ls = fold16(ls, ls);
      *(LAS float*)(L + L_WG + (l & 31) * 4) = e * rcp(ls);
    }
    STAMP(8);
    BAR();
    if (b < 64) {
      const int fo = WS_FB + b * 4096;
      v4u f0 = LD16(l16, fo), f1 = LD16(l16 + 1024, fo), f2 = LD16(l16 + 2048, fo), f3 = LD16(l16 + 3072, fo);
      UNTIL(TBAD(f0) | TBAD(f1) | TBAD(f2) | TBAD(f3), (f0 = LD16(l16, fo), f1 = LD16(l16 + 1024, fo), f2 = LD16(l16 + 2048, fo), f3 = LD16(l16 + 3072, fo)));
      float t = fold32((uf(f0[0]) + uf(f1[0])) + (uf(f2[0]) + uf(f3[0])), (uf(f0[2]) + uf(f1[2])) + (uf(f2[2]) + uf(f3[2])));
      t = fold16(t, t); t += dpp<R8>(t); t += dpp<R4>(t);
      const float f = rb(rb(t) + b2v);
      float gt = rb(f * rcp(1.f + ex2(-f * LOG2E)));
      int frd = L_V + ((l >> 5) * 576 + (l & 3) * 136 + ((l >> 2) & 7) * 16) * 4;
      int ovo = (l & 28) ? OOB : ocol * 2; float xo_ = xo;
      asm volatile("" : "+v"(gt), "+v"(frd), "+v"(ovo), "+v"(xo_));
      BAR();
      const v4f u0 = *(LAS v4f*)(L + frd), u1 = *(LAS v4f*)(L + frd + 16);
      const v4f u = (u0 + u1) + (*(LAS v4f*)(L + frd + 32) + *(LAS v4f*)(L + frd + 48));
      float v = (u[0] + u[2]) + (u[1] + u[3]);
      v = fold16(v, v); v += dpp<R8>(v); v += dpp<R4>(v);
      const float o = xo_ + rb(rb(v) * gt);
      __builtin_amdgcn_raw_buffer_store_b16((u16)pk(o, o), mk(out, 1024), ovo, 0, 16);
      if (b == 0 && l == 0) __hip_atomic_store((GAS unsigned*)ws, tag, __ATOMIC_RELAXED, __HIP_MEMORY_SCOPE_AGENT);
    }
    STAMP(10);
    FLUSH();
  } else {
    const int a = w - 1, a3 = a & 3;
    if (XS) {
      unsigned t = ep0;
      asm("v_writelane_b32 %0, %1, 0\n\t" "v_writelane_b32 %0, %2, 1\n\t" "v_writelane_b32 %0, %3, 2\n\t" "v_writelane_b32 %0, %4, 3\n\t" "v_writelane_b32 %0, %5, 4\n\t" "v_writelane_b32 %0, %6, 5\n\t" "v_writelane_b32 %0, %7, 6\n\t" "v_writelane_b32 %0, %8, 7\n\t" "v_writelane_b32 %0, %9, 8\n\t" "v_writelane_b32 %0, %10, 9\n\t" "v_writelane_b32 %0, %11, 10\n\t" "v_writelane_b32 %0, %12, 11\n\t" "v_writelane_b32 %0, %13, 12\n\t" "v_writelane_b32 %0, %14, 13\n\t" "v_writelane_b32 %0, %15, 14\n\t" "v_writelane_b32 %0, %16, 15\n\t" : "+v"(t) : "s"(xs0[0]), "s"(xs0[1]), "s"(xs0[2]), "s"(xs0[3]), "s"(xs0[4]), "s"(xs0[5]), "s"(xs0[6]), "s"(xs0[7]), "s"(xs0[8]), "s"(xs0[9]), "s"(xs0[10]), "s"(xs0[11]), "s"(xs0[12]), "s"(xs0[13]), "s"(xs0[14]), "s"(xs0[15]));
      asm("v_writelane_b32 %0, %1, 16\n\t" "v_writelane_b32 %0, %2, 17\n\t" "v_writelane_b32 %0, %3, 18\n\t" "v_writelane_b32 %0, %4, 19\n\t" "v_writelane_b32 %0, %5, 20\n\t" "v_writelane_b32 %0, %6, 21\n\t" "v_writelane_b32 %0, %7, 22\n\t" "v_writelane_b32 %0, %8, 23\n\t" "v_writelane_b32 %0, %9, 24\n\t" "v_writelane_b32 %0, %10, 25\n\t" "v_writelane_b32 %0, %11, 26\n\t" "v_writelane_b32 %0, %12, 27\n\t" "v_writelane_b32 %0, %13, 28\n\t" "v_writelane_b32 %0, %14, 29\n\t" "v_writelane_b32 %0, %15, 30\n\t" "v_writelane_b32 %0, %16, 31\n\t" : "+v"(t) : "s"(xs1[0]), "s"(xs1[1]), "s"(xs1[2]), "s"(xs1[3]), "s"(xs1[4]), "s"(xs1[5]), "s"(xs1[6]), "s"(xs1[7]), "s"(xs1[8]), "s"(xs1[9]), "s"(xs1[10]), "s"(xs1[11]), "s"(xs1[12]), "s"(xs1[13]), "s"(xs1[14]), "s"(xs1[15]));
      *(LAS unsigned*)(L + L_S + (l < 32 ? a * 128 + l * 4 : 1024)) = t;
      BAR();
      if (XS == 2) ep0 = *(const CAS unsigned*)ws;
    }
    int obo = (n & 3) == 0 ? (n >> 2) * 16384 + gq * 16 : OOB;
    int sbo = (l & 16) ? OOB : (l >> 5) * 16384 + (n >> 3) * 8192 + (n & 7) * 8;
    int obs = WS_OB + g * 65536 + a3 * 4096 + j * 128, sbs = WS_SB + ((j & 1) * 32 + a * 4) * 8192 + (g * 16 + (j >> 1)) * 64;
    int fbs = WS_FB + (kq * 16 + a * 2) * 4096 + (g * 8 + rb_) * 64;
    int sadr = L_S + (a & 1) * 1024 + gq * 256 + (n & 3) * 64 + (a >> 1) * 16 + (n >> 2) * 4, qadr = L_Q + (n & 3) * QSTR + gq * 64 + (n >> 2) * 16;
    int pso = WS_OB + gp * 65536 + (j >> 1) * 4096 + a3 * 1024, pvo = a >= 4 ? l * 16 : OOB;
    int oadr = L_O + n * 80 + gq * 16, owr = L_O + (a3 * 8 + (l >> 3)) * 80 + (l & 7) * 8;
    int mad = L_M + (n & 3) * 4, l8 = l * 8, mrd = L_M + (n >> 2) * 4;
    asm volatile("" : "+v"(mad), "+v"(l8), "+v"(mrd));
    int fso = WS_SB + b * 8192 + a * 1024, fadr = L_V + ((l & 3) * 136 + a * 16 + (l >> 2)) * 4;
    int vwr = L_V + (a * 16 + gq) * VSTR + n * 16;
    int qso = Q32 ? WS_QG + g * 16384 + a3 * 1024 : WS_QG + g * 8192 + a3 * 512, qvo = a >= 4 ? (Q32 ? l * 16 : (l >> 5) * 4096 + (l & 31) * 16) : OOB;
    int mxw = L_MX + gq * 128 + (n & 3) * 32 + a * 4, qwr = L_Q + a3 * QSTR + (Q32 ? l * 4 : (l & 31) * 8 + (l >> 5) * 4);
    int wov = (l >> 5) * 1024 + ((l & 31) ^ ((l >> 5) * 2)) * 16;
    asm volatile("" : "+v"(wov));
    int wos = (gp * 512 + (j >> 1) * 32 + a3 * 8) * 1024 + (j & 1) * 512, w2s = (gp * 224 + rb_ * 28 + a3 * 8) * 1024 + kq * 256, wvo = gq * 1024 + (n ^ (gq * 2)) * 16;
    int w2t = w2s + (a3 == 3 ? 0 : 4096);
    int wl0 = L_W + a3 * 4 * WBLK, wl1 = L_W + (16 + a3 * 2) * WBLK;
    v4u so_ = {(unsigned)(u64)wo, (unsigned)((u64)wo >> 32) & 0xFFFFu, 4096u * 512 * 2, 0x20000u}, s2_ = {(unsigned)(u64)w2, (unsigned)((u64)w2 >> 32) & 0xFFFFu, FF * 512u * 2, 0x20000u};
    asm volatile("" : "+v"(obo), "+v"(sbo), "+v"(sadr), "+v"(qadr), "+v"(oadr), "+v"(owr), "+v"(vwr), "+v"(fadr), "+v"(qvo), "+v"(mxw), "+v"(qwr), "+v"(pvo), "+v"(wvo));
    asm volatile("" : "+s"(obs), "+s"(sbs), "+s"(pso), "+s"(qso), "+s"(fso), "+s"(wos), "+s"(w2s), "+s"(so_), "+s"(s2_), "+s"(wl0), "+s"(wl1), "+s"(w2t));
    const rsrc_t rv = mk(vc, 8u * SK * 256);
    int tadr = L_V + (gq * 4 + (n >> 2)) * VSTR + a3 * 64 + (n & 3) * 8;
    asm volatile("" : "+v"(tadr));
    SB();
#define DMA1(VO, SRD, SO) "buffer_load_dwordx4 " VO ", " SRD ", " SO " offen nt lds\n\ts_add_u32 m0, m0, %8\n\ts_add_u32 " SO ", " SO ", 0x800\n\t"
#define WDMA() do { unsigned keep; asm volatile("s_mov_b32 %0, m0\n\ts_mov_b32 m0, %6\n\ts_nop 0\n\t" DMA1("%10", "%4", "%1") DMA1("%10", "%4", "%1") DMA1("%10", "%4", "%1") DMA1("%10", "%4", "%1") \
                   "s_mov_b32 m0, %7\n\ts_nop 0\n\t" DMA1("%3", "%5", "%2") "buffer_load_dwordx4 %3, %5, %9 offen nt lds\n\ts_mov_b32 m0, %0" \
                   : "=&s"(keep), "+s"(wos), "+s"(w2s) : "v"(wvo), "s"(so_), "s"(s2_), "s"(wl0), "s"(wl1), "i"(WBLK), "s"(w2t), "v"(wov) : "memory", "scc"); } while (0)
#define WDO() do { unsigned keep; asm volatile("s_mov_b32 %0, m0\n\ts_mov_b32 m0, %4\n\ts_nop 0\n\t" \
      "buffer_load_dwordx4 %2, %3, %1 offen nt lds\n\ts_add_u32 m0, m0, %5\n\ts_add_u32 %1, %1, 0x800\n\tbuffer_load_dwordx4 %2, %3, %1 offen nt lds\n\ts_add_u32 m0, m0, %5\n\ts_add_u32 %1, %1, 0x800\n\t" \
      "buffer_load_dwordx4 %2, %3, %1 offen nt lds\n\ts_add_u32 m0, m0, %5\n\ts_add_u32 %1, %1, 0x800\n\tbuffer_load_dwordx4 %2, %3, %1 offen nt lds\n\ts_mov_b32 m0, %0" \
      : "=&s"(keep), "+s"(wos) : "v"(wov), "s"(so_), "s"(wl0), "i"(WBLK) : "memory", "scc"); } while (0)
#define WD2() do { unsigned keep; asm volatile("s_mov_b32 %0, m0\n\ts_mov_b32 m0, %4\n\ts_nop 0\n\t" \
      "buffer_load_dwordx4 %2, %3, %1 offen nt lds\n\ts_add_u32 m0, m0, %5\n\ts_nop 0\n\tbuffer_load_dwordx4 %2, %3, %6 offen nt lds\n\ts_mov_b32 m0, %0" \
      : "=&s"(keep) : "s"(w2s), "v"(wvo), "s"(s2_), "s"(wl1), "i"(WBLK), "s"(w2t) : "memory", "scc"); } while (0)
    if (LK == 1) { const rsrc_t rk2 = mk(kc, 8u * SK * 256); RESTK(rk2); }
    if (LK == 2) { const rsrc_t rk2 = mk(kc, 8u * SK * 256); __builtin_amdgcn_s_barrier(); SB();
      if (a >= 4) {
#pragma unroll
        for (int k = KY; k < 4; ++k) kv[k] = ldw(rk2, kof + k * 256, kvo); } else if (VD) __builtin_amdgcn_s_sleep(VD);
      SB(); }
    EPOCH();
    v4u o0 = {0u, tag, 0u, tag}, o1 = o0; u64 lead = 0x000F000F000F000Full;
    asm volatile("" : "+v"(o0), "+v"(o1)); asm volatile("" : "+s"(lead));
    v4u vv[4];
    WAITV(3 - OT);
    SB();
#pragma unroll
    for (int i = 0; i < YV; ++i) vv[i] = ldw(rv, l16 + i * 1024, kvo);
    SB();
    if (a >= 4) {
      WAITV(3 - YT + YV);
      if (TQ) { while ((unsigned)__builtin_amdgcn_s_memrealtime() - t0 < TQ) __builtin_amdgcn_s_sleep(1); }
      __builtin_amdgcn_s_barrier();
    }
    SB();
    v4u q0, q1, q2, q3;
#define QLD() (q0 = LD16(qvo, qso), q1 = LD16(qvo, qso + 2048))
#define QBAD() (DBAD(q0) | DBAD(q1))
    QLD();
    SB();
    STAMP(9);
    if (a >= 4) __builtin_amdgcn_s_sleep(1);
#pragma unroll
    for (int i = YV; i < OV; ++i) vv[i] = ldw(rv, l16 + i * 1024, kvo);
    SB();
    if (a < 4) { __builtin_amdgcn_s_barrier(); if (OV < 4) __builtin_amdgcn_s_sleep(1); }
    SB();
#pragma unroll
    for (int i = OV; i < 4; ++i) vv[i] = ldw(rv, l16 + i * 1024, kvo);
    SB();
    WAITV(4 - YV);
    STAMP(2);
#define QSUM() do { const v4f u_ = __builtin_bit_cast(v4f, q0 & 0xFFFFFF00u) + __builtin_bit_cast(v4f, q1 & 0xFFFFFF00u); float c_, d_; const float a_ = sw32(u_[0], u_[2], c_), b_ = sw32(u_[1], u_[3], d_); \
        *(LAS unsigned*)(L + qwr) = pk(a_ + c_, b_ + d_); } while (0)
    if (a >= 4) {
      QSUM();
      SB();
      if (__builtin_expect(ANY(QBAD()), 0)) {
        UNTIL(QBAD(), QLD());
        QSUM();
      }
    }
    SB();
    if (a < 4) {
#pragma unroll
      for (int i = 0; i < 4; ++i) *(LAS v4u*)(L + vwr + i * 4 * VSTR) = vv[i];
      if (WP == 0) WDMA();
      if (WS == 5 || WS == 6) WD2();
    }
    const bool wnow = !TW || (unsigned)__builtin_amdgcn_s_memrealtime() - t0 >= TW;
    STAMP(4);
    BAR();
    {
      const v4u qf = *(LAS v4u*)(L + qadr);
      if (WS == 0 && WP == 1 && a < 4 && wnow) WDMA();
      if ((WS == 1 || WS == 3 || WS == 7) && a < 4) WD2();
      if ((WS == 2 || WS == 4) && a < 4) WDO();
      v4f sc[4];
#pragma unroll
      for (int i = 0; i < 4; ++i) { sc[i] = mfma(kv[i], qf, v4f{0.f, 0.f, 0.f, 0.f}); SB(); }
      float t, m;
#define DADD(src, acc, rot, bank) "v_add_f32_dpp %0, " src ", " acc " row_ror:" rot " row_mask:0xf bank_mask:" bank "\n\t"
      asm volatile("s_nop 4\n\t"
                   DADD("%3", "%2", "12", "0x1") DADD("%6", "%7", "4", "0x2") DADD("%10", "%12", "8", "0x4") DADD("%14", "%17", "12", "0x8")
                   DADD("%4", "%0", "8", "0x1") DADD("%8", "%0", "12", "0x2") DADD("%11", "%0", "4", "0x4") DADD("%15", "%0", "8", "0x8")
                   DADD("%5", "%0", "4", "0x1") DADD("%9", "%0", "8", "0x2") DADD("%13", "%0", "12", "0x4") DADD("%16", "%0", "4", "0x8")
                   "v_cvt_pk_bf16_f32 %0, 0, %0\n\tv_mul_f32 %0, 0x3db504f3, %0\n\tv_cvt_pk_bf16_f32 %0, 0, %0\n\tv_mul_f32 %0, 0x3fb8aa3b, %0\n\ts_nop 1\n\t"
                   "v_max_f32_dpp %1, %0, %0 row_ror:8 row_mask:0xf bank_mask:0xf\n\ts_nop 1\n\t"
                   "v_max_f32_dpp %1, %1, %1 row_ror:4 row_mask:0xf bank_mask:0xf\n\t"
                   "v_sub_f32 %0, %0, %1\n\tv_exp_f32 %0, %0\n\t"
                   "s_mov_b64 exec, %21\n\tds_max_f32 %18, %1\n\tds_write_b32 %19, %1\n\ts_mov_b64 exec, -1\n\tds_write_b32 %20, %0"
                   : "=&v"(t), "=&v"(m)
                   : "v"(sc[0][0]), "v"(sc[0][1]), "v"(sc[0][2]), "v"(sc[0][3]), "v"(sc[1][0]), "v"(sc[1][1]), "v"(sc[1][2]), "v"(sc[1][3]),
                     "v"(sc[2][0]), "v"(sc[2][1]), "v"(sc[2][2]), "v"(sc[2][3]), "v"(sc[3][0]), "v"(sc[3][1]), "v"(sc[3][2]), "v"(sc[3][3]),
                     "v"(mad), "v"(mxw), "v"(sadr), "s"(lead)
                   : "memory");
#pragma unroll
      for (int i = 0; i < 5; ++i) __builtin_amdgcn_s_setprio(2);
    }
    v2u vt[16];
    if (a < 4) {
#pragma unroll
      for (int i = 0; i < 8; ++i) vt[(i & 3) + (i >> 2) * 8] = trd(L + tadr + (i & 3) * 16 * VSTR + (i >> 2) * 32);
      if (WP == 2) WDMA();
      if (WS == 6 || WS == 7) WDO();
    }
    STAMP(5);
    BAR();
    if (a < 4) {
      const v2u mm = *(LAS v2u*)(L + L_MX + l8);
      const float m = *(LAS float*)(L + mrd);
      const v4f s0 = *(LAS v4f*)(L + L_S + l16), s1 = *(LAS v4f*)(L + L_S + 1024 + l16);
      const v4f p0 = s0 * ex2(uf(mm[0]) - m), p1 = s1 * ex2(uf(mm[1]) - m);
      const v4u pb = {pk(p0[0], p0[1]), pk(p0[2], p0[3]), pk(p1[0], p1[1]), pk(p1[2], p1[3])};
      const v4f z = {0.f, 0.f, 0.f, 0.f};
      v4f d0 = mfma(cat(vt[0], vt[1]), pb, z), d1 = mfma(cat(vt[8], vt[9]), pb, z);
      const v4u pb1 = dpp4<0x55>(pb), pb2 = dpp4<0xAA>(pb);
      SB();
      d0 = mfma(cat(vt[2], vt[3]), pb1, d0); d1 = mfma(cat(vt[10], vt[11]), pb1, d1);
      v4u pb3 = dpp4<0xFF>(pb);
      asm volatile("" : "+v"(pb3) : "v"(pb1), "v"(vt[0]), "v"(vt[1]), "v"(vt[8]), "v"(vt[9]));
      SB();
      if (WS == 1 || WS == 5) WDO();
      if (WS == 2) WD2();
      STAMP(7);
      BAR();
#pragma unroll
      for (int i = 0; i < 8; ++i) vt[4 + (i & 1) + ((i >> 2) & 1) * 2 + ((i >> 1) & 1) * 8] = trd(L + tadr + (4 + (i & 1) + ((i >> 2) & 1) * 2) * 16 * VSTR + ((i >> 1) & 1) * 32);
      d0 = mfma(cat(vt[4], vt[5]), pb2, d0); d1 = mfma(cat(vt[12], vt[13]), pb2, d1);
      d0 = mfma(cat(vt[6], vt[7]), pb3, d0); d1 = mfma(cat(vt[14], vt[15]), pb3, d1);
      o0[0] = pk(d0[0], d0[1]); o0[2] = pk(d0[2], d0[3]); o1[0] = pk(d1[0], d1[1]); o1[2] = pk(d1[2], d1[3]);
      ST16(o0, obo, obs);
      ST16(o1, obo + 64, obs);
      STAMP(6);
      if (WS == 3) { WDO(); asm volatile("s_waitcnt vmcnt(0)" ::: "memory"); } else if (WS == 4) { WD2(); asm volatile("s_waitcnt vmcnt(0)" ::: "memory"); }
      else if (WP == 3 || !wnow) { if (TW) while ((unsigned)__builtin_amdgcn_s_memrealtime() - t0 < TX) __builtin_amdgcn_s_sleep(1); WDMA(); asm volatile("s_waitcnt vmcnt(0)" ::: "memory"); }
      else asm volatile("s_waitcnt vmcnt(2)" ::: "memory");
      STAMP(1);
    }
    SB();
    if (a >= 4) {
#pragma unroll
      for (int i = 0; i < 4; ++i) *(LAS v4u*)(L + vwr + i * 4 * VSTR) = vv[i];
      STAMP(3);
      BAR();
      __builtin_amdgcn_s_sleep(PD);
    }
    SB();
    v4u pv = LD16(pvo, pso);
    BAR();
    if (a >= 4 && ANY(TBAD(pv))) pv = LD16(pvo, pso);
    SB();
    v2u wt[4];
    const int wrd = L_W + gq * 2 * WBLK + (n >> 2) * 256 + (a ^ (n >> 2)) * 32 + (n & 3) * 8;
#pragma unroll
    for (int i = 0; i < 4; ++i) wt[i] = trd(L + L_W + (gq * 4 + (i & 1) * 2 + (n >> 3)) * WBLK + ((n >> 2) & 1) * 512 + ((a * 2 + (i >> 1)) ^ ((n >> 2) & 1)) * 32 + (n & 3) * 8);
    SB();
    WAITV(0);
    if (a < 4) {
      const v4u hf = *(LAS v4u*)(L + L_H + gq * 16);
      LAS unsigned char* p = L + wrd + 16 * WBLK;
      const v4f z = {0.f, 0.f, 0.f, 0.f};
      const v4f f0 = mfma(hf, cat(trd(p), trd(p + WBLK)), z), f1 = mfma(hf, cat(trd(p + 128), trd(p + 128 + WBLK)), z);
      ST8((v2u{__float_as_uint(gq ? f1[0] : f0[0]), tag}), l < 32 ? gq * 32768 + (n >> 3) * 4096 + (n & 7) * 8 : OOB, fbs);
    }
    SB();
    if (a >= 4) {
#define OWR() (((LAS unsigned*)(L + owr))[0] = pv[0], ((LAS unsigned*)(L + owr))[1] = pv[2])
      OWR();
      UNTIL(TBAD(pv), (pv = LD16(pvo, pso), OWR()));
      STAMP(8);
    }
    BAR();
    {
      const v4u of0 = *(LAS v4u*)(L + oadr), of1 = *(LAS v4u*)(L + oadr + 16 * 80);
      const v4f w0 = *(LAS v4f*)(L + L_WG + gq * 16), w1_ = *(LAS v4f*)(L + L_WG + 64 + gq * 16);
      SB();
      const v4f z = {0.f, 0.f, 0.f, 0.f};
      const v4f d00 = mfma(of0, cat(wt[0], wt[1]), z), d01 = mfma(of0, cat(wt[2], wt[3]), z), d10 = mfma(of1, cat(wt[0], wt[1]), z), d11 = mfma(of1, cat(wt[2], wt[3]), z);
      SB();
      v2f c0 = d00.hi * w0.hi + d00.lo * w0.lo, c1 = d01.hi * w0.hi + d01.lo * w0.lo;
      c0 = d10.hi * w1_.hi + (d10.lo * w1_.lo + c0); c1 = d11.hi * w1_.hi + (d11.lo * w1_.lo + c1);
      float e = fold32(c0[0] + c0[1], c1[0] + c1[1]);
      e = fold16(e, e);
      ST8((v2u{__float_as_uint(e), tag}), sbo, sbs);
    }
    STAMP(10);
    if (b < 64) {
      __builtin_amdgcn_s_sleep(10); if (a == g) __builtin_amdgcn_s_sleep(10);
      v4u sv = LD16(l16, fso);
#define SWR() (((LAS unsigned*)(L + fadr))[0] = sv[0], ((LAS unsigned*)(L + fadr))[576] = sv[2])
      SWR();
      UNTIL(TBAD(sv), (sv = LD16(l16, fso), SWR()));
      BAR();
      if (a == 0) { const unsigned e_ = (unsigned)__builtin_amdgcn_s_memrealtime(), d_ = 4 * (e_ - c0_); while ((unsigned)__builtin_amdgcn_s_memrealtime() - e_ < d_ && d_ < 100000u) {} }
    }
  }
}

extern "C" void kernel_launch(void* const* d_in, const int* in_sizes, int n_in, void* d_out, int out_size, void* d_ws, size_t ws_size, hipStream_t stream) {
  fused_decode<<<256, NTHR, 0, stream>>>((const u16*)d_in[0], (const u16*)d_in[3], (const u16*)d_in[6], (const u16*)d_in[2], (unsigned char*)d_ws, (const u16*)d_in[1],
                                         (const u16*)d_in[4], (const u16*)d_in[8], (const u16*)d_in[7], (const u16*)d_in[9], (u16*)d_out);
}
```

```cpp
#include <hip/hip_runtime.h>
#include <hip/hip_bf16.h>

typedef unsigned short u16;
typedef unsigned long long u64;
typedef unsigned v4u __attribute__((ext_vector_type(4)));
typedef unsigned v2u __attribute__((ext_vector_type(2)));
typedef float v4f __attribute__((ext_vector_type(4)));
typedef short s16x4 __attribute__((ext_vector_type(4)));
typedef __bf16 bf16x2 __attribute__((ext_vector_type(2)));
typedef __bf16 bf16x8 __attribute__((ext_vector_type(8)));
typedef __amdgpu_buffer_rsrc_t rsrc_t;
#define GAS __attribute__((address_space(1)))
#define CAS __attribute__((address_space(4)))
#define LAS __attribute__((address_space(3)))

constexpr int SK = 32768, WIN = 4096, FF = 1792;
constexpr int NTHR = 576;
constexpr unsigned WS_QG = 4096;
constexpr unsigned WS_HG = WS_QG + 65536;
constexpr unsigned WS_ML = WS_HG + 57344;
constexpr unsigned WS_OB = WS_ML + 16384;
constexpr unsigned WS_SB = WS_OB + 524288;
constexpr unsigned WS_FB = WS_SB + 524288;
constexpr unsigned WS_END = WS_FB + 262144;
constexpr int VSTR = 288, L_V = 0, L_O = 128 * VSTR, L_Q = L_O + 4096, QSTR = 320, L_S = L_Q + 4 * QSTR, L_XN = L_S + 2048, L_MX = L_XN + 1024, L_M = L_MX + 512, L_H = L_M + 32,
              L_WG = L_H + 64, L_W = L_WG + 128, WBLK = 1088, L_END = L_W + 24 * WBLK;
constexpr unsigned SPIN_CAP = 1u << 18;
constexpr float LOG2E = 1.4426950408889634f;
constexpr int OOB = 0x40000000;

__device__ __forceinline__ rsrc_t mk(const void* p, unsigned bytes) { return __builtin_amdgcn_make_buffer_rsrc((void*)p, 0, bytes, 0x00020000); }
__device__ __forceinline__ v4u ldw(rsrc_t r, int voff, int soff) { return __builtin_amdgcn_raw_buffer_load_b128(r, voff, soff, 2); }
__device__ __forceinline__ float uf(unsigned u) { return __uint_as_float(u); }
__device__ __forceinline__ float bflo(unsigned u) { return __uint_as_float(u << 16); }
__device__ __forceinline__ float bfhi(unsigned u) { return __uint_as_float(u & 0xFFFF0000u); }
__device__ __forceinline__ unsigned pk(float lo, float hi) { bf16x2 r = {(__bf16)lo, (__bf16)hi}; return __builtin_bit_cast(unsigned, r); }
typedef float v2f __attribute__((ext_vector_type(2)));
__device__ __forceinline__ float rb(float f) { return __builtin_bit_cast(float, __builtin_convertvector((v2f{0.f, f}), bf16x2)); }
__device__ __forceinline__ v4f mfma(v4u a, v4u b, v4f c) { return __builtin_amdgcn_mfma_f32_16x16x32_bf16(__builtin_bit_cast(bf16x8, a), __builtin_bit_cast(bf16x8, b), c, 0, 0, 0); }
template <int CTRL> __device__ __forceinline__ unsigned dppu(unsigned x) { return (unsigned)__builtin_amdgcn_mov_dpp((int)x, CTRL, 0xf, 0xf, true); }
template <int CTRL> __device__ __forceinline__ float dpp(float x) { return __uint_as_float(dppu<CTRL>(__float_as_uint(x))); }
template <int CTRL> __device__ __forceinline__ v4u dpp4(v4u x) { return v4u{dppu<CTRL>(x[0]), dppu<CTRL>(x[1]), dppu<CTRL>(x[2]), dppu<CTRL>(x[3])}; }
constexpr int X1 = 0xB1, X2 = 0x4E, HM = 0x141, R4 = 0x124, R8 = 0x128;
__device__ __forceinline__ float sw16(float a, float b, float& o) { auto r = __builtin_amdgcn_permlane16_swap(__float_as_uint(a), __float_as_uint(b), false, false); o = uf(r[1]); return uf(r[0]); }
__device__ __forceinline__ float sw32(float a, float b, float& o) { auto r = __builtin_amdgcn_permlane32_swap(__float_as_uint(a), __float_as_uint(b), false, false); o = uf(r[1]); return uf(r[0]); }
__device__ __forceinline__ float fold16(float a, float b) { float o; const float p = sw16(a, b, o); return p + o; }
__device__ __forceinline__ float fold32(float a, float b) { float o; const float p = sw32(a, b, o); return p + o; }
__device__ __forceinline__ float fmx(float a, float b) { return __builtin_elementwise_maximum(a, b); }
__device__ __forceinline__ float max16(float a) { float o; const float p = sw16(a, a, o); return fmx(p, o); }
#define MAXDPP(name, ctl) __device__ __forceinline__ float name(float m) { float r; asm("s_nop 1\n\tv_max_f32_dpp %0, %1, %1 " ctl " row_mask:0xf bank_mask:0xf" : "=v"(r) : "v"(m)); return r; }
MAXDPP(mx_r8, "row_ror:8") MAXDPP(mx_r4, "row_ror:4") MAXDPP(mx_x1, "quad_perm:[1,0,3,2]") MAXDPP(mx_x2, "quad_perm:[2,3,0,1]")
__device__ __forceinline__ float sum64(float v) { v += dpp<X1>(v); v += dpp<X2>(v); v += dpp<HM>(v); v += dpp<R8>(v); v = fold16(v, v); return fold32(v, v); }
__device__ __forceinline__ v2u trd(LAS unsigned char* p) { return __builtin_bit_cast(v2u, __builtin_amdgcn_ds_read_tr16_b64_v4i16((LAS s16x4*)p)); }
__device__ __forceinline__ v4u cat(v2u a, v2u b) { return v4u{a[0], a[1], b[0], b[1]}; }
__device__ __forceinline__ float ex2(float x) { return __builtin_amdgcn_exp2f(x); }
__device__ __forceinline__ float rcp(float x) { return __builtin_amdgcn_rcpf(x); }
__device__ __forceinline__ float gval(unsigned d) { return __uint_as_float(d & 0xFFFFFF00u); }
__device__ __forceinline__ unsigned d4(float f, unsigned tagbyte) { return ((__float_as_uint(f) + 0x80u) & 0xFFFFFF00u) | tagbyte; }
#define SB() __builtin_amdgcn_sched_barrier(0)
#define BAR() do { asm volatile("s_waitcnt lgkmcnt(0)" ::: "memory"); __builtin_amdgcn_s_barrier(); asm volatile("" ::: "memory"); } while (0)
#define FENCE() asm volatile("" ::: "memory")
#define WAITV(n) __builtin_amdgcn_s_waitcnt(0x0F70 | (n))
#define LD16(off, soff) __builtin_amdgcn_raw_buffer_load_b128(rs, (off), (soff), 16)
#define ST16(v, off, soff) __builtin_amdgcn_raw_buffer_store_b128((v), rs, (off), (soff), 16)
#define ST8(v, off, soff) __builtin_amdgcn_raw_buffer_store_b64((v), rs, (off), (soff), 16)
#define TBAD(v) __builtin_amdgcn_bitop3_b32((v)[1], (v)[3], tag, 0x7E)
#define ANY(t) (__builtin_amdgcn_ballot_w64((t) != 0) != 0)
#define DBAD(v) __builtin_amdgcn_bitop3_b32(__builtin_amdgcn_perm((v)[1], (v)[0], 0x0c0c0400u), __builtin_amdgcn_perm((v)[3], (v)[2], 0x04000c0cu), tag, 0x56)
#define EPOCH() asm volatile("" : "+s"(ep0)); const unsigned tag = ((ep0 & 0x7F7F7F7Fu) + 0x01010101u) ^ (ep0 & 0x80808080u)
#define UNTIL(BADEXPR, RELOAD) do { if (ANY(BADEXPR)) { int spins_ = SPIN_CAP; _Pragma("nounroll") do { FENCE(); RELOAD; if (!ANY(BADEXPR)) break; asm volatile("" : "+s"(spins_)); } while (--spins_); } } while (0)

__device__ __forceinline__ void mksel(unsigned (&sel)[8], int l) {
#pragma unroll
  for (int d = 0; d < 4; ++d) { const bool on = ((l & 7) >> 1) == d;
    sel[d] = on ? ((l & 1) ? 0x01000c0cu : 0x0c0c0100u) : 0x0c0c0c0cu; sel[4 + d] = on ? ((l & 1) ? 0x03020c0cu : 0x0c0c0302u) : 0x0c0c0c0cu; }
}
__device__ __forceinline__ v4u diag(unsigned pair, const unsigned (&sel)[8], int hi) {
  return v4u{__builtin_amdgcn_perm(0u, pair, sel[4 * hi]), __builtin_amdgcn_perm(0u, pair, sel[4 * hi + 1]), __builtin_amdgcn_perm(0u, pair, sel[4 * hi + 2]), __builtin_amdgcn_perm(0u, pair, sel[4 * hi + 3])};
}
__device__ __forceinline__ float upr8(float v) { return __builtin_bit_cast(float, __builtin_amdgcn_update_dpp(__builtin_bit_cast(int, v), __builtin_bit_cast(int, v), R8, 0xC, 0xF, false)); }
__device__ __forceinline__ v2u diag_out(v4f d, unsigned tb0, unsigned tb1) {
  const float t0 = upr8(d[0]), t1 = upr8(d[1]), t2 = upr8(d[2]), t3 = upr8(d[3]);
  return v2u{d4(fold32(t0, t2), tb0), d4(fold32(t1, t3), tb1)};
}

__global__ void __launch_bounds__(NTHR, 3) fused_decode(const u16* __restrict__ x, const u16* __restrict__ wq, const u16* __restrict__ w1, const u16* __restrict__ vc, unsigned char* ws,
                                                        const u16* __restrict__ kc, const u16* __restrict__ wo, const u16* __restrict__ w2, const u16* __restrict__ b1,
                                                        const u16* __restrict__ b2, u16* __restrict__ out) {
  __shared__ __attribute__((aligned(128))) unsigned char lds_[L_END];
  LAS unsigned char* L = (LAS unsigned char*)lds_;
  const int b = blockIdx.x, g = b & 7, j = b >> 3, l = threadIdx.x & 63;
  int w = __builtin_amdgcn_readfirstlane(threadIdx.x >> 6);
  { u64 pc; unsigned keep, t_;
    asm volatile("s_cmp_lg_u32 %3, 4\n\ts_cbranch_scc1 1f\n\ts_getpc_b64 %0\n\ts_mov_b32 %1, m0\n\ts_mov_b32 m0, %5\n\tv_lshlrev_b32 %2, 7, %4\n\t"
                 "global_load_lds_dword %2, %0\n\ts_mov_b32 m0, %1\n1:"
                 : "=&s"(pc), "=&s"(keep), "=&v"(t_) : "s"(w), "v"((j * 4 + (l & 3)) & 63), "i"(L_O) : "scc", "memory"); }
  const int kq = j & 3, rb_ = j >> 2, gp = g ^ 4;
  const int gq = l >> 4, n = l & 15;
  const bool hasw1 = rb_ != 3; const int jw = rb_ == 7 ? j - 16 : j;
  int kvo = (g * SK + (SK - WIN) + j * 128 + (((w - 1) * 2 + ((w - 1) >> 2) + j) & 7) * 16) * 256, l16 = l * 16, kof = (n >> 2) * 1024 + gq * 64 + (n & 3) * 16;
  int vo = (l >> 3) * (16 * 8192) + (l & 7) * 16, so = kq * (128 * 8192) + g * 1024 + rb_ * 128;
  int vo1 = hasw1 ? (l >> 3) * (8 * FF * 2) + (l & 7) * 16 : OOB, so1 = g * (64 * FF * 2) + jw * 128;
  asm volatile("" : "+s"(kvo), "+s"(so), "+s"(so1));
  asm volatile("s_setprio 3" : "+v"(l16), "+v"(vo), "+v"(vo1), "+v"(kof));
  asm volatile("" :: "s"(x), "s"(kc), "s"(vc), "s"(wq), "s"(wo), "s"(w1), "s"(b1), "s"(w2), "s"(b2), "s"(out), "s"(ws));
  unsigned ep0 = 0;
  const rsrc_t rs = mk(ws, WS_END);
  v4u kv[4];
  if (w != 0) {
    const rsrc_t rk = mk(kc, 8u * SK * 256);
    if (w == 3) {
      unsigned keep;
      const unsigned po = (j * 16 + 8 + (l & 7)) * 8192 + g * 1024 + 384 + ((l >> 3) & 1) * 512;
      asm volatile("s_mov_b32 %0, m0\n\ts_mov_b32 m0, %3\n\ts_nop 0\n\tglobal_load_lds_dword %1, %2 nt\n\ts_mov_b32 m0, %0" : "=&s"(keep) : "v"(po), "s"(wq), "i"(L_O + 256) : "memory");
    }
    ep0 = *(const CAS unsigned*)ws;
    __builtin_amdgcn_s_setprio(2);
    __builtin_amdgcn_s_barrier();
    SB();
    kv[0] = ldw(rk, kof, kvo); kv[1] = ldw(rk, kof + 256, kvo);
    SB();
  }
  asm volatile("" : "+s"(w));
  if (__builtin_expect(w == 0, 1)) {
    v4u r16[16], f8[8];
    const v4u xv = *(const GAS v4u*)((const GAS unsigned char*)x + (unsigned)l16);
    const unsigned epv = __builtin_amdgcn_raw_buffer_load_b32(rs, 0, 0, 0);
    { const rsrc_t r = mk(wq, 512 * 4096 * 2), r1 = mk(w1, 512 * FF * 2);
#pragma unroll
      for (int i = 0; i < 16; ++i) r16[i] = ldw(r, vo, so + i * 8192);
#pragma unroll
      for (int i = 0; i < 8; ++i) f8[i] = ldw(r1, vo1, so1 + i * (FF * 2)); }
    SB();
    __builtin_amdgcn_s_barrier();
    SB();
    unsigned sel[8]; mksel(sel, l);
    int gvo = (l & 8) ? OOB : (l & 7) * 32 + (l & 16) + (l >> 5) * 8, xra = L_XN + kq * 256 + (l >> 3) * 32, xrc = L_XN + g * 128 + (l >> 3) * 16;
    asm volatile("" : "+v"(sel[0]), "+v"(sel[1]), "+v"(sel[2]), "+v"(sel[3]), "+v"(sel[4]), "+v"(sel[5]), "+v"(sel[6]), "+v"(sel[7]), "+v"(gvo), "+v"(xra), "+v"(xrc));
    SB();
    float xf[8];
#pragma unroll
    for (int i = 0; i < 4; ++i) { xf[2 * i] = bflo(xv[i]); xf[2 * i + 1] = bfhi(xv[i]); }
    const float mean = sum64(((xf[0] + xf[1]) + (xf[2] + xf[3])) + ((xf[4] + xf[5]) + (xf[6] + xf[7]))) * (1.f / 512);
    float vs = 0.f;
#pragma unroll
    for (int i = 0; i < 8; ++i) { const float d = xf[i] - mean; vs = fmaf(d, d, vs); }
    const float var = sum64(vs) * (1.f / 512);
    const float mb = rb(mean), rsd = rcp(rb(__builtin_amdgcn_sqrtf(rb(rb(var) + 1e-5f))));
    v4u xn;
#pragma unroll
    for (int i = 0; i < 4; ++i) { const unsigned d = pk(xf[2 * i] - mb, xf[2 * i + 1] - mb); xn[i] = pk(bflo(d) * rsd, bfhi(d) * rsd); }
    *(LAS v4u*)(L + L_XN + l * 16) = xn;
    *(LAS float*)(L + L_M + (l & 3) * 4) = -__builtin_inff();
    const v4u xa = *(LAS v4u*)(L + xra), xb = *(LAS v4u*)(L + xra + 16), xc = *(LAS v4u*)(L + xrc);
    ep0 = __builtin_amdgcn_readfirstlane(epv);
    EPOCH();
    const unsigned tb0 = (tag >> ((l >> 5) * 16)) & 0xFFu, tb1 = (tag >> ((l >> 5) * 16 + 8)) & 0xFFu;
    { v4u bx[8];
#pragma unroll
      for (int i = 0; i < 8; ++i) bx[i] = diag(xa[i >> 1], sel, i & 1);
      SB();
      v4f d = {0.f, 0.f, 0.f, 0.f};
#pragma unroll
      for (int i = 0; i < 8; ++i) { d = mfma(bx[i], r16[i], d); bx[i] = diag(xb[i >> 1], sel, i & 1); SB(); }
#pragma unroll
      for (int i = 0; i < 8; ++i) d = mfma(bx[i], r16[8 + i], d);
      ST8(diag_out(d, tb0, tb1), gvo, WS_QG + g * 8192 + kq * 2048 + rb_ * 256);
      SB();
      __builtin_amdgcn_s_barrier();
      SB();
    }
    { v4f d = {0.f, 0.f, 0.f, 0.f};
#pragma unroll
      for (int i = 0; i < 8; ++i) d = mfma(diag(xc[i >> 1], sel, i & 1), f8[i], d);
      ST8(diag_out(d, tb0, tb1), hasw1 ? gvo : OOB, WS_HG + g * (FF * 4) + jw * 256); }
    const int hr0 = gp * 224 + rb_ * 28, hu = min(l & 7, 6), ocol = (b & 63) * 8 + (l & 3) * 2 + (l >> 5);
    int mlo = (l & 51) == 0 ? (n >> 2) * 512 : OOB, mls = WS_ML + g * 2048 + j * 16, hvo = (l >> 3) * (FF * 4) + hu * 16, hso = WS_HG + hr0 * 4;
    int pvo = (l & 31) * 16, pso = WS_ML + gp * 2048 + (j >> 3) * 512;
    asm volatile("" : "+v"(mlo), "+v"(hvo), "+v"(pvo));
    asm volatile("" : "+s"(mls), "+s"(hso), "+s"(pso));
    const float b1v = bflo(b1[hr0 + hu * 4 + gq]), b2v = bflo(b2[ocol]), xo = bflo(x[ocol]);
    BAR();
    BAR();
    v4u hg = LD16(hvo, hso);
    SB();
    BAR();
    { const v2u mm = *(LAS v2u*)(L + L_MX + l * 8);
      const v4f s0 = *(LAS v4f*)(L + L_S + l * 16), s1 = *(LAS v4f*)(L + L_S + 1024 + l * 16);
      const float m = *(LAS float*)(L + L_M + (n >> 2) * 4);
      float ls = fmaf((s0[0] + s0[1]) + (s0[2] + s0[3]), ex2(uf(mm[0]) - m), ((s1[0] + s1[1]) + (s1[2] + s1[3])) * ex2(uf(mm[1]) - m));
      ls += dpp<X1>(ls); ls += dpp<X2>(ls); ls = fold16(ls, ls); ls = fold32(ls, ls);
      ST16((v4u{__float_as_uint(m), tag, __float_as_uint(ls), tag}), mlo, mls);
    }
    SB();
    BAR();
    SB();
    {
      if (__builtin_expect(ANY(DBAD(hg)), 0)) UNTIL(DBAD(hg), hg = LD16(hvo, hso));
      float hs = fold16(fold32(gval(hg[0]), gval(hg[2])), fold32(gval(hg[1]), gval(hg[3]))); hs += dpp<R8>(hs);
      const float hp = rb(rb(hs) + b1v);
      const float u2 = 1.5957691216057308f * fmaf(0.044715f * hp * hp, hp, hp);
      const float ge = hp * rcp(1.f + ex2(-u2 * LOG2E));
      *(LAS u16*)(L + L_H + (l & 7) * 8 + gq * 2) = ((l & 7) < 7) ? (u16)pk(ge, ge) : (u16)0;
    }
    __builtin_amdgcn_s_sleep(8);
    v4u pv = LD16(pvo, pso);
    SB();
    BAR();
    {
      UNTIL(TBAD(pv), pv = LD16(pvo, pso));
      const float m = uf(pv[0]);
      const float M = max16(mx_r4(mx_r8(mx_x2(mx_x1(m)))));
      const float e = ex2(m - M);
      float ls = e * uf(pv[2]);
      ls += dpp<X1>(ls); ls += dpp<X2>(ls); ls += dpp<HM>(ls); ls += dpp<R8>(ls); ls = fold16(ls, ls);
      *(LAS float*)(L + L_WG + (l & 31) * 4) = e * rcp(ls);
    }
    BAR();
    if (b < 64) {
      const int fo = WS_FB + b * 4096;
      v4u f0 = LD16(l16, fo), f1 = LD16(l16 + 1024, fo), f2 = LD16(l16 + 2048, fo), f3 = LD16(l16 + 3072, fo);
      UNTIL(TBAD(f0) | TBAD(f1) | TBAD(f2) | TBAD(f3), (f0 = LD16(l16, fo), f1 = LD16(l16 + 1024, fo), f2 = LD16(l16 + 2048, fo), f3 = LD16(l16 + 3072, fo)));
      float t = fold32((uf(f0[0]) + uf(f1[0])) + (uf(f2[0]) + uf(f3[0])), (uf(f0[2]) + uf(f1[2])) + (uf(f2[2]) + uf(f3[2])));
      t = fold16(t, t); t += dpp<R8>(t); t += dpp<R4>(t);
      const float f = rb(rb(t) + b2v);
      float gt = rb(f * rcp(1.f + ex2(-f * LOG2E)));
      int frd = L_V + ((l >> 5) * 576 + (l & 3) * 136 + ((l >> 2) & 7) * 16) * 4;
      int ovo = (l & 28) ? OOB : ocol * 2; float xo_ = xo;
      asm volatile("" : "+v"(gt), "+v"(frd), "+v"(ovo), "+v"(xo_));
      BAR();
      const v4f u0 = *(LAS v4f*)(L + frd), u1 = *(LAS v4f*)(L + frd + 16);
      const v4f u = (u0 + u1) + (*(LAS v4f*)(L + frd + 32) + *(LAS v4f*)(L + frd + 48));
      float v = (u[0] + u[2]) + (u[1] + u[3]);
      v = fold16(v, v); v += dpp<R8>(v); v += dpp<R4>(v);
      const float o = xo_ + rb(rb(v) * gt);
      __builtin_amdgcn_raw_buffer_store_b16((u16)pk(o, o), mk(out, 1024), ovo, 0, 16);
      if (b == 0 && l == 0) __hip_atomic_store((GAS unsigned*)ws, tag, __ATOMIC_RELAXED, __HIP_MEMORY_SCOPE_AGENT);
    }
  } else {
    const int a = w - 1, a3 = a & 3;
    int obo = (n & 3) == 0 ? (n >> 2) * 16384 + gq * 16 : OOB;
    int sbo = (l & 16) ? OOB : (l >> 5) * 16384 + (n >> 3) * 8192 + (n & 7) * 8;
    int obs = WS_OB + g * 65536 + a3 * 4096 + j * 128, sbs = WS_SB + ((j & 1) * 32 + a * 4) * 8192 + (g * 16 + (j >> 1)) * 64;
    int fbs = WS_FB + (kq * 16 + a * 2) * 4096 + (g * 8 + rb_) * 64;
    int sadr = L_S + (a & 1) * 1024 + gq * 256 + (n & 3) * 64 + (a >> 1) * 16 + (n >> 2) * 4, qadr = L_Q + (n & 3) * QSTR + gq * 64 + (n >> 2) * 16;
    int pso = WS_OB + gp * 65536 + (j >> 1) * 4096 + a3 * 1024, pvo = a >= 4 ? l * 16 : OOB;
    int oadr = L_O + n * 80 + gq * 16, owr = L_O + (a3 * 8 + (l >> 3)) * 80 + (l & 7) * 8;
    int mad = L_M + (n & 3) * 4, l8 = l * 8, mrd = L_M + (n >> 2) * 4;
    asm volatile("" : "+v"(mad), "+v"(l8), "+v"(mrd));
    int fso = WS_SB + b * 8192 + a * 1024, fadr = L_V + ((l & 3) * 136 + a * 16 + (l >> 2)) * 4;
    int vwr = L_V + (a * 16 + gq) * VSTR + n * 16;
    int qso = WS_QG + g * 8192 + a3 * 512, qvo = a >= 4 ? (l >> 5) * 4096 + (l & 31) * 16 : OOB;
    int mxw = L_MX + gq * 128 + (n & 3) * 32 + a * 4, qwr = L_Q + a3 * QSTR + (l & 31) * 8 + (l >> 5) * 4;
    int wov = (l >> 5) * 1024 + ((l & 31) ^ ((l >> 5) * 2)) * 16;
    asm volatile("" : "+v"(wov));
    int wos = (gp * 512 + (j >> 1) * 32 + a3 * 8) * 1024 + (j & 1) * 512, w2s = (gp * 224 + rb_ * 28 + a3 * 8) * 1024 + kq * 256, wvo = gq * 1024 + (n ^ (gq * 2)) * 16;
    int w2t = w2s + (a3 == 3 ? 0 : 4096);
    int wl0 = L_W + a3 * 4 * WBLK, wl1 = L_W + (16 + a3 * 2) * WBLK;
    v4u so_ = {(unsigned)(u64)wo, (unsigned)((u64)wo >> 32) & 0xFFFFu, 4096u * 512 * 2, 0x20000u}, s2_ = {(unsigned)(u64)w2, (unsigned)((u64)w2 >> 32) & 0xFFFFu, FF * 512u * 2, 0x20000u};
    asm volatile("" : "+v"(obo), "+v"(sbo), "+v"(sadr), "+v"(qadr), "+v"(oadr), "+v"(owr), "+v"(vwr), "+v"(fadr), "+v"(qvo), "+v"(mxw), "+v"(qwr), "+v"(pvo), "+v"(wvo));
    asm volatile("" : "+s"(obs), "+s"(sbs), "+s"(pso), "+s"(qso), "+s"(fso), "+s"(wos), "+s"(w2s), "+s"(so_), "+s"(s2_), "+s"(wl0), "+s"(wl1), "+s"(w2t));
    const rsrc_t rv = mk(vc, 8u * SK * 256);
    int tadr = L_V + (gq * 4 + (n >> 2)) * VSTR + a3 * 64 + (n & 3) * 8;
    asm volatile("" : "+v"(tadr));
    SB();
#define WDO() do { unsigned keep; asm volatile("s_mov_b32 %0, m0\n\ts_mov_b32 m0, %4\n\ts_nop 0\n\t" \
      "buffer_load_dwordx4 %2, %3, %1 offen nt lds\n\ts_add_u32 m0, m0, %5\n\ts_add_u32 %1, %1, 0x800\n\tbuffer_load_dwordx4 %2, %3, %1 offen nt lds\n\ts_add_u32 m0, m0, %5\n\ts_add_u32 %1, %1, 0x800\n\t" \
      "buffer_load_dwordx4 %2, %3, %1 offen nt lds\n\ts_add_u32 m0, m0, %5\n\ts_add_u32 %1, %1, 0x800\n\tbuffer_load_dwordx4 %2, %3, %1 offen nt lds\n\ts_mov_b32 m0, %0" \
      : "=&s"(keep), "+s"(wos) : "v"(wov), "s"(so_), "s"(wl0), "i"(WBLK) : "memory", "scc"); } while (0)
#define WD2() do { unsigned keep; asm volatile("s_mov_b32 %0, m0\n\ts_mov_b32 m0, %4\n\ts_nop 0\n\t" \
      "buffer_load_dwordx4 %2, %3, %1 offen nt lds\n\ts_add_u32 m0, m0, %5\n\ts_nop 0\n\tbuffer_load_dwordx4 %2, %3, %6 offen nt lds\n\ts_mov_b32 m0, %0" \
      : "=&s"(keep) : "s"(w2s), "v"(wvo), "s"(s2_), "s"(wl1), "i"(WBLK), "s"(w2t) : "memory", "scc"); } while (0)
    { const rsrc_t rk = mk(kc, 8u * SK * 256);
      if (a >= 4) __builtin_amdgcn_s_barrier();
      SB();
      kv[2] = ldw(rk, kof + 512, kvo); kv[3] = ldw(rk, kof + 768, kvo);
      SB();
      if (a < 4) __builtin_amdgcn_s_barrier(); }
    EPOCH();
    v4u o0 = {0u, tag, 0u, tag}, o1 = o0; u64 lead = 0x000F000F000F000Full;
    asm volatile("" : "+v"(o0), "+v"(o1)); asm volatile("" : "+s"(lead));
    WAITV(3);
    SB();
    if (a >= 4) { WAITV(1); __builtin_amdgcn_s_barrier(); }
    SB();
    v4u q0 = LD16(qvo, qso), q1 = LD16(qvo, qso + 2048);
    SB();
    if (a >= 4) __builtin_amdgcn_s_sleep(1);
    v4u vv[4];
#pragma unroll
    for (int i = 0; i < 4; ++i) vv[i] = ldw(rv, l16 + i * 1024, kvo);
    SB();
    if (a < 4) __builtin_amdgcn_s_barrier();
    SB();
    WAITV(4);
#define QSUM() do { const v4f u_ = __builtin_bit_cast(v4f, q0 & 0xFFFFFF00u) + __builtin_bit_cast(v4f, q1 & 0xFFFFFF00u); float c_, d_; const float a_ = sw32(u_[0], u_[2], c_), b_ = sw32(u_[1], u_[3], d_); \
        *(LAS unsigned*)(L + qwr) = pk(a_ + c_, b_ + d_); } while (0)
    if (a >= 4) {
      QSUM();
      SB();
      if (__builtin_expect(ANY(DBAD(q0) | DBAD(q1)), 0)) {
        UNTIL(DBAD(q0) | DBAD(q1), (q0 = LD16(qvo, qso), q1 = LD16(qvo, qso + 2048)));
        QSUM();
      }
    }
    SB();
    if (a < 4) {
#pragma unroll
      for (int i = 0; i < 4; ++i) *(LAS v4u*)(L + vwr + i * 4 * VSTR) = vv[i];
    }
    BAR();
    {
      const v4u qf = *(LAS v4u*)(L + qadr);
      if (a < 4) WD2();
      v4f sc[4];
#pragma unroll
      for (int i = 0; i < 4; ++i) { sc[i] = mfma(kv[i], qf, v4f{0.f, 0.f, 0.f, 0.f}); SB(); }
      float t, m;
#define DADD(src, acc, rot, bank) "v_add_f32_dpp %0, " src ", " acc " row_ror:" rot " row_mask:0xf bank_mask:" bank "\n\t"
      asm volatile("s_nop 4\n\t"
                   DADD("%3", "%2", "12", "0x1") DADD("%6", "%7", "4", "0x2") DADD("%10", "%12", "8", "0x4") DADD("%14", "%17", "12", "0x8")
                   DADD("%4", "%0", "8", "0x1") DADD("%8", "%0", "12", "0x2") DADD("%11", "%0", "4", "0x4") DADD("%15", "%0", "8", "0x8")
                   DADD("%5", "%0", "4", "0x1") DADD("%9", "%0", "8", "0x2") DADD("%13", "%0", "12", "0x4") DADD("%16", "%0", "4", "0x8")
                   "v_cvt_pk_bf16_f32 %0, 0, %0\n\tv_mul_f32 %0, 0x3db504f3, %0\n\tv_cvt_pk_bf16_f32 %0, 0, %0\n\tv_mul_f32 %0, 0x3fb8aa3b, %0\n\ts_nop 1\n\t"
                   "v_max_f32_dpp %1, %0, %0 row_ror:8 row_mask:0xf bank_mask:0xf\n\ts_nop 1\n\t"
                   "v_max_f32_dpp %1, %1, %1 row_ror:4 row_mask:0xf bank_mask:0xf\n\t"
                   "v_sub_f32 %0, %0, %1\n\tv_exp_f32 %0, %0\n\t"
                   "s_mov_b64 exec, %21\n\tds_max_f32 %18, %1\n\tds_write_b32 %19, %1\n\ts_mov_b64 exec, -1\n\tds_write_b32 %20, %0"
                   : "=&v"(t), "=&v"(m)
                   : "v"(sc[0][0]), "v"(sc[0][1]), "v"(sc[0][2]), "v"(sc[0][3]), "v"(sc[1][0]), "v"(sc[1][1]), "v"(sc[1][2]), "v"(sc[1][3]),
                     "v"(sc[2][0]), "v"(sc[2][1]), "v"(sc[2][2]), "v"(sc[2][3]), "v"(sc[3][0]), "v"(sc[3][1]), "v"(sc[3][2]), "v"(sc[3][3]),
                     "v"(mad), "v"(mxw), "v"(sadr), "s"(lead)
                   : "memory");
#pragma unroll
      for (int i = 0; i < 5; ++i) __builtin_amdgcn_s_setprio(2);
    }
    v2u vt[16];
    if (a < 4) {
#pragma unroll
      for (int i = 0; i < 8; ++i) vt[(i & 3) + (i >> 2) * 8] = trd(L + tadr + (i & 3) * 16 * VSTR + (i >> 2) * 32);
    }
    BAR();
    if (a < 4) {
      const v2u mm = *(LAS v2u*)(L + L_MX + l8);
      const float m = *(LAS float*)(L + mrd);
      const v4f s0 = *(LAS v4f*)(L + L_S + l16), s1 = *(LAS v4f*)(L + L_S + 1024 + l16);
      const v4f p0 = s0 * ex2(uf(mm[0]) - m), p1 = s1 * ex2(uf(mm[1]) - m);
      const v4u pb = {pk(p0[0], p0[1]), pk(p0[2], p0[3]), pk(p1[0], p1[1]), pk(p1[2], p1[3])};
      const v4f z = {0.f, 0.f, 0.f, 0.f};
      v4f d0 = mfma(cat(vt[0], vt[1]), pb, z), d1 = mfma(cat(vt[8], vt[9]), pb, z);
      const v4u pb1 = dpp4<0x55>(pb), pb2 = dpp4<0xAA>(pb);
      SB();
      d0 = mfma(cat(vt[2], vt[3]), pb1, d0); d1 = mfma(cat(vt[10], vt[11]), pb1, d1);
      v4u pb3 = dpp4<0xFF>(pb);
      asm volatile("" : "+v"(pb3) : "v"(pb1), "v"(vt[0]), "v"(vt[1]), "v"(vt[8]), "v"(vt[9]));
      SB();
      WDO();
      BAR();
#pragma unroll
      for (int i = 0; i < 8; ++i) vt[4 + (i & 1) + ((i >> 2) & 1) * 2 + ((i >> 1) & 1) * 8] = trd(L + tadr + (4 + (i & 1) + ((i >> 2) & 1) * 2) * 16 * VSTR + ((i >> 1) & 1) * 32);
      d0 = mfma(cat(vt[4], vt[5]), pb2, d0); d1 = mfma(cat(vt[12], vt[13]), pb2, d1);
      d0 = mfma(cat(vt[6], vt[7]), pb3, d0); d1 = mfma(cat(vt[14], vt[15]), pb3, d1);
      o0[0] = pk(d0[0], d0[1]); o0[2] = pk(d0[2], d0[3]); o1[0] = pk(d1[0], d1[1]); o1[2] = pk(d1[2], d1[3]);
      ST16(o0, obo, obs);
      ST16(o1, obo + 64, obs);
      asm volatile("s_waitcnt vmcnt(2)" ::: "memory");
    }
    SB();
    if (a >= 4) {
#pragma unroll
      for (int i = 0; i < 4; ++i) *(LAS v4u*)(L + vwr + i * 4 * VSTR) = vv[i];
      BAR();
      __builtin_amdgcn_s_sleep(10);
    }
    SB();
    v4u pv = LD16(pvo, pso);
    BAR();
    if (a >= 4 && ANY(TBAD(pv))) pv = LD16(pvo, pso);
    SB();
    v2u wt[4];
    const int wrd = L_W + gq * 2 * WBLK + (n >> 2) * 256 + (a ^ (n >> 2)) * 32 + (n & 3) * 8;
#pragma unroll
    for (int i = 0; i < 4; ++i) wt[i] = trd(L + L_W + (gq * 4 + (i & 1) * 2 + (n >> 3)) * WBLK + ((n >> 2) & 1) * 512 + ((a * 2 + (i >> 1)) ^ ((n >> 2) & 1)) * 32 + (n & 3) * 8);
    SB();
    WAITV(0);
    if (a < 4) {
      const v4u hf = *(LAS v4u*)(L + L_H + gq * 16);
      LAS unsigned char* p = L + wrd + 16 * WBLK;
      const v4f z = {0.f, 0.f, 0.f, 0.f};
      const v4f f0 = mfma(hf, cat(trd(p), trd(p + WBLK)), z), f1 = mfma(hf, cat(trd(p + 128), trd(p + 128 + WBLK)), z);
      ST8((v2u{__float_as_uint(gq ? f1[0] : f0[0]), tag}), l < 32 ? gq * 32768 + (n >> 3) * 4096 + (n & 7) * 8 : OOB, fbs);
    }
    SB();
    if (a >= 4) {
#define OWR() (((LAS unsigned*)(L + owr))[0] = pv[0], ((LAS unsigned*)(L + owr))[1] = pv[2])
      OWR();
      UNTIL(TBAD(pv), (pv = LD16(pvo, pso), OWR()));
    }
    BAR();
    {
      const v4u of0 = *(LAS v4u*)(L + oadr), of1 = *(LAS v4u*)(L + oadr + 16 * 80);
      const v4f w0 = *(LAS v4f*)(L + L_WG + gq * 16), w1_ = *(LAS v4f*)(L + L_WG + 64 + gq * 16);
      SB();
      const v4f z = {0.f, 0.f, 0.f, 0.f};
      const v4f d00 = mfma(of0, cat(wt[0], wt[1]), z), d01 = mfma(of0, cat(wt[2], wt[3]), z), d10 = mfma(of1, cat(wt[0], wt[1]), z), d11 = mfma(of1, cat(wt[2], wt[3]), z);
      SB();
      v2f c0 = d00.hi * w0.hi + d00.lo * w0.lo, c1 = d01.hi * w0.hi + d01.lo * w0.lo;
      c0 = d10.hi * w1_.hi + (d10.lo * w1_.lo + c0); c1 = d11.hi * w1_.hi + (d11.lo * w1_.lo + c1);
      float e = fold32(c0[0] + c0[1], c1[0] + c1[1]);
      e = fold16(e, e);
      ST8((v2u{__float_as_uint(e), tag}), sbo, sbs);
    }
    if (b < 64) {
      __builtin_amdgcn_s_sleep(10); if (a == g) __builtin_amdgcn_s_sleep(10);
      v4u sv = LD16(l16, fso);
#define SWR() (((LAS unsigned*)(L + fadr))[0] = sv[0], ((LAS unsigned*)(L + fadr))[576] = sv[2])
      SWR();
      UNTIL(TBAD(sv), (sv = LD16(l16, fso), SWR()));
      BAR();
    }
  }
}

extern "C" void kernel_launch(void* const* d_in, const int* in_sizes, int n_in, void* d_out, int out_size, void* d_ws, size_t ws_size, hipStream_t stream) {
  fused_decode<<<256, NTHR, 0, stream>>>((const u16*)d_in[0], (const u16*)d_in[3], (const u16*)d_in[6], (const u16*)d_in[2], (unsigned char*)d_ws, (const u16*)d_in[1],
                                         (const u16*)d_in[4], (const u16*)d_in[8], (const u16*)d_in[7], (const u16*)d_in[9], (u16*)d_out);
}
```
